```python
import torch

def _softmax(x, axis=-1):
    return torch.softmax(x, dim=axis)
def _gelu(x):
    return torch.nn.functional.gelu(x, approximate="tanh")
def _silu(x):
    return torch.nn.functional.silu(x)
def _sigmoid(x):
    return torch.sigmoid(x)
def _layernorm(x, axis=-1, eps=1e-5):
    m = x.mean(dim=axis, keepdim=True)
    v = x.var(dim=axis, keepdim=True, unbiased=False)
    return (x - m) / torch.sqrt(v + eps)
def _scatter_add(tgt, idx, src, axis=0):
    return tgt.clone().index_add_(axis, idx, src)
def _segment_sum(src, seg, n):
    out = torch.zeros((n,) + src.shape[1:], dtype=src.dtype)
    return out.index_add_(0, seg, src)
def _pad(x, pw):
    flat = tuple(v for pair in reversed(pw) for v in pair)
    return torch.nn.functional.pad(x, flat)

kernel_name = 'recurrent_scan__sliding_local_dilated__mixer_s_mlp__prompt'

def setup_inputs():

    _w = (lambda a: (lambda t: t.to(torch.bfloat16) if t.is_floating_point() else t)(torch.as_tensor(a)))
    def _A_setup():
        B, S, D = 1, 2048, 640
        return {
            "x": _w((lambda *s: torch.randn(*s, dtype=torch.bfloat16) * 0.05)(B, S, D)),
            "wqkv": _w((lambda *s: torch.randn(*s, dtype=torch.bfloat16) * 0.05)(D, 3 * D)),
            "wo": _w((lambda *s: torch.randn(*s, dtype=torch.bfloat16) * 0.05)(D, D)),
        }
    _A_in = _A_setup()

    def _B_setup():
        B, S, D, F = 1, 2048, 640, 1728
        return {
            "x":  _w((lambda *s: torch.randn(*s, dtype=torch.bfloat16) * 0.05)(B, S, D)),
            "w1": _w((lambda *s: torch.randn(*s, dtype=torch.bfloat16) * 3.2)(D, D) * (D ** -0.5)),
            "w2": _w((lambda *s: torch.randn(*s, dtype=torch.bfloat16) * 3.2)(D, D) * (D ** -0.5)),
            "w3": _w((lambda *s: torch.randn(*s, dtype=torch.bfloat16) * 3.2)(D, F) * (D ** -0.5)),
            "w4": _w((lambda *s: torch.randn(*s, dtype=torch.bfloat16) * 3.2)(F, D) * (F ** -0.5)),
        }
    _B_in = _B_setup()

    return {
        "A_x": _A_in["x"],
        "A_wqkv": _A_in["wqkv"],
        "A_wo": _A_in["wo"],
        "B_x": _B_in["x"],
        "B_w1": _B_in["w1"],
        "B_w2": _B_in["w2"],
        "B_w3": _B_in["w3"],
        "B_w4": _B_in["w4"],
    }

def reference(A_x, A_wqkv, A_wo, B_x, B_w1, B_w2, B_w3, B_w4):
    def _A_ref(x, wqkv, wo):
        B, S, D = x.shape
        H = 32; Dh = D // H
        W, dil = 8, 2
        qkv = (lambda x, *d: x.permute(*d))((x @ wqkv).reshape(B, S, 3, H, Dh), 2, 0, 3, 1, 4)
        q, k, v = qkv[0], qkv[1], qkv[2]
        scale = Dh ** -0.5
        pos = torch.arange(S)
        off = torch.arange(W) * dil
        idx = pos[:, None] - off[None, :]
        mask = idx >= 0
        idx_c = torch.where(mask, idx, 0).reshape(-1)
        kg = k[:, :, idx_c, :].reshape(B, H, S, W, Dh)
        vg = v[:, :, idx_c, :].reshape(B, H, S, W, Dh)
        logits = torch.einsum("bhsd,bhswd->bhsw", q, kg) * scale
        logits = torch.where(mask[None, None, :, :], logits, -1e9)
        attn = _softmax(logits, axis=-1)
        o = torch.einsum("bhsw,bhswd->bhsd", attn, vg)
        return (lambda x, *d: x.permute(*d))(o, 0, 2, 1, 3).reshape(B, S, D) @ wo

    def _B_ref(x, w1, w2, w3, w4):
        B, S, D = x.shape
        h = _gelu(_layernorm(x, axis=-1) @ w1)
        c = D // 4
        g0 = _pad(h[:, :, 0*c:1*c], ((0, 0), (1, 0), (0, 0)))[:, :S, :]
        g1 = _pad(h[:, :, 1*c:2*c], ((0, 0), (0, 1), (0, 0)))[:, 1:, :]
        g2 = _pad(h[:, :, 2*c:3*c], ((0, 0), (2, 0), (0, 0)))[:, :S, :]
        g3 = _pad(h[:, :, 3*c:   ], ((0, 0), (0, 2), (0, 0)))[:, 2:, :]
        hs = torch.concatenate([g0, g1, g2, g3], axis=-1) @ w2
        x1 = x + hs
        h2 = _gelu(_layernorm(x1, axis=-1) @ w3) @ w4
        return x1 + h2

    x = A_x
    xn = _layernorm(x, axis=-1)
    s = _A_ref(xn, A_wqkv, A_wo)
    g = _silu(_B_ref(xn, B_w1, B_w2, B_w3, B_w4))
    return x + s * g
```

```cpp
#include <hip/hip_runtime.h>
#include <hip/hip_bf16.h>
#include <stdint.h>

typedef unsigned short u16;
typedef short bf16x8 __attribute__((ext_vector_type(8)));
typedef float f32x4 __attribute__((ext_vector_type(4)));

constexpr int S = 2048, D = 640, F = 1728, H = 32, DH = 20;

__device__ __forceinline__ float bf2f(u16 b) { return __builtin_bit_cast(float, (unsigned)b << 16); }
__device__ __forceinline__ u16 f2bf(float f) { __hip_bfloat16 h = __float2bfloat16(f); return __builtin_bit_cast(u16, h); }
__device__ __forceinline__ float rbf(float f) { return bf2f(f2bf(f)); }
__device__ __forceinline__ float gelu(float x) {
  float u = 0.7978845608028654f * (x + 0.044715f * x * x * x);
  return x / (1.f + __expf(-2.f * u));
}

__global__ void k_ln(const u16* in, u16* out, u16* out2) {
  int row = blockIdx.x * 4 + (threadIdx.x >> 6), lane = threadIdx.x & 63;
  float v[10];
  float s = 0;
  for (int i = 0; i < 10; i++) { v[i] = bf2f(in[row * D + lane + 64 * i]); s += v[i]; }
  for (int o = 32; o; o >>= 1) s += __shfl_xor(s, o);
  float m = s / D, q = 0;
  for (int i = 0; i < 10; i++) q += (v[i] - m) * (v[i] - m);
  for (int o = 32; o; o >>= 1) q += __shfl_xor(q, o);
  float r = rsqrtf(q / D + 1e-5f);
  s = 0;
  for (int i = 0; i < 10; i++) { v[i] = rbf((v[i] - m) * r); out[row * D + lane + 64 * i] = f2bf(v[i]); s += v[i]; }
  if (out2) {
    for (int o = 32; o; o >>= 1) s += __shfl_xor(s, o);
    m = s / D; q = 0;
    for (int i = 0; i < 10; i++) q += (v[i] - m) * (v[i] - m);
    for (int o = 32; o; o >>= 1) q += __shfl_xor(q, o);
    r = rsqrtf(q / D + 1e-5f);
    for (int i = 0; i < 10; i++) out2[row * D + lane + 64 * i] = f2bf((v[i] - m) * r);
  }
}

template <int MODE>
__global__ __launch_bounds__(256) void k_gemm(const u16* A, const u16* W, u16* C, int N, int K, const u16* e0, const u16* e1, const u16* e2) {
  __shared__ u16 As[64][40];
  __shared__ u16 Bs[64][40];
  int bm = blockIdx.y * 64, bn = blockIdx.x * 64;
  int t = threadIdx.x, w = t >> 6, lane = t & 63, wr = w >> 1, wc = w & 1;
  f32x4 acc[2][2] = {};
  for (int k0 = 0; k0 < K; k0 += 32) {
    {
      int r = t >> 2, c = (t & 3) * 8;
      int row = bm + r;
      bf16x8 v = {};
      if (MODE == 2) {
        int g = (k0 + c) / 160;
        int sh = g == 0 ? -1 : g == 1 ? 1 : g == 2 ? -2 : 2;
        row += sh;
        if (row >= 0 && row < S) v = *(const bf16x8*)(A + (size_t)row * K + k0 + c);
      } else v = *(const bf16x8*)(A + (size_t)row * K + k0 + c);
      *(bf16x8*)&As[r][c] = v;
      int kk = t >> 3, n = (t & 7) * 8;
      bf16x8 wv = *(const bf16x8*)(W + (size_t)(k0 + kk) * N + bn + n);
      for (int j = 0; j < 8; j++) Bs[n + j][kk] = (u16)wv[j];
    }
    __syncthreads();
    bf16x8 a[2], b[2];
    for (int i = 0; i < 2; i++) a[i] = *(const bf16x8*)&As[wr * 32 + i * 16 + (lane & 15)][(lane >> 4) * 8];
    for (int i = 0; i < 2; i++) b[i] = *(const bf16x8*)&Bs[wc * 32 + i * 16 + (lane & 15)][(lane >> 4) * 8];
    for (int i = 0; i < 2; i++) for (int j = 0; j < 2; j++) acc[i][j] = __builtin_amdgcn_mfma_f32_16x16x32_bf16(a[i], b[j], acc[i][j], 0, 0, 0);
    __syncthreads();
  }
  for (int i = 0; i < 2; i++) for (int j = 0; j < 2; j++) for (int r = 0; r < 4; r++) {
    int row = bm + wr * 32 + i * 16 + (lane >> 4) * 4 + r, col = bn + wc * 32 + j * 16 + (lane & 15);
    float v = rbf(acc[i][j][r]);
    size_t o = (size_t)row * N + col;
    if (MODE == 1) v = gelu(v);
    if (MODE == 2) v = bf2f(e0[o]) + v;
    if (MODE == 3) { float y = rbf(bf2f(e0[o]) + v); float g = rbf(y / (1.f + __expf(-y))); v = bf2f(e2[o]) + rbf(bf2f(e1[o]) * g); }
    C[o] = f2bf(v);
  }
}

__global__ void k_attn(const u16* qkv, u16* o) {
  int idx = blockIdx.x * blockDim.x + threadIdx.x;
  int s = idx / H, h = idx % H;
  float q[DH], l[8], mx = -1e30f;
  for (int d = 0; d < DH; d++) q[d] = bf2f(qkv[(size_t)s * 1920 + h * DH + d]);
#pragma unroll
  for (int w = 0; w < 8; w++) {
    int p = s - 2 * w;
    float a = 0;
    if (p >= 0) { for (int d = 0; d < DH; d++) a += q[d] * bf2f(qkv[(size_t)p * 1920 + 640 + h * DH + d]); a = rbf(rbf(a) * rbf(0.22360679774997896f)); }
    else a = -1e9f;
    l[w] = a; mx = fmaxf(mx, a);
  }
  float sum = 0;
#pragma unroll
  for (int w = 0; w < 8; w++) { l[w] = __expf(l[w] - mx); sum += l[w]; }
#pragma unroll
  for (int w = 0; w < 8; w++) l[w] = rbf(l[w] / sum);
  for (int d = 0; d < DH; d++) {
    float a = 0;
#pragma unroll
    for (int w = 0; w < 8; w++) { int p = s - 2 * w; if (p < 0) p = 0; a += l[w] * bf2f(qkv[(size_t)p * 1920 + 1280 + h * DH + d]); }
    o[(size_t)s * D + h * DH + d] = f2bf(a);
  }
}

extern "C" void kernel_launch(void* const* d_in, const int* in_sizes, int n_in, void* d_out, int out_size, void* d_ws, size_t ws_size, hipStream_t stream) {
  const u16* x = (const u16*)d_in[0];
  const u16* wqkv = (const u16*)d_in[1];
  const u16* wo = (const u16*)d_in[2];
  const u16* w1 = (const u16*)d_in[4];
  const u16* w2 = (const u16*)d_in[5];
  const u16* w3 = (const u16*)d_in[6];
  const u16* w4 = (const u16*)d_in[7];
  u16* ws = (u16*)d_ws;
  u16* xn = ws; u16* xnn = xn + S * D; u16* qkv = xnn + S * D; u16* h = qkv + S * 1920; u16* o = h + S * D;
  u16* s = o + S * D; u16* x1 = s + S * D; u16* x1n = x1 + S * D; u16* g = x1n + S * D;
  k_ln<<<S / 4, 256, 0, stream>>>(x, xn, xnn);
  k_gemm<0><<<dim3(1920 / 64, S / 64), 256, 0, stream>>>(xn, wqkv, qkv, 1920, D, nullptr, nullptr, nullptr);
  k_gemm<1><<<dim3(D / 64, S / 64), 256, 0, stream>>>(xnn, w1, h, D, D, nullptr, nullptr, nullptr);
  k_attn<<<S * H / 256, 256, 0, stream>>>(qkv, o);
  k_gemm<0><<<dim3(D / 64, S / 64), 256, 0, stream>>>(o, wo, s, D, D, nullptr, nullptr, nullptr);
  k_gemm<2><<<dim3(D / 64, S / 64), 256, 0, stream>>>(h, w2, x1, D, D, xn, nullptr, nullptr);
  k_ln<<<S / 4, 256, 0, stream>>>(x1, x1n, nullptr);
  k_gemm<1><<<dim3(F / 64, S / 64), 256, 0, stream>>>(x1n, w3, g, F, D, nullptr, nullptr, nullptr);
  k_gemm<3><<<dim3(D / 64, S / 64), 256, 0, stream>>>(g, w4, (u16*)d_out, D, F, x1, s, x);
}
```

```cpp
#include <hip/hip_runtime.h>
#include <hip/hip_bf16.h>
#include <stdint.h>

typedef unsigned short u16;
typedef short bf16x8 __attribute__((ext_vector_type(8)));
typedef float f32x4 __attribute__((ext_vector_type(4)));

constexpr int S = 2048, D = 640, F = 1728, H = 32, DH = 20;

__device__ __forceinline__ float bf2f(u16 b) { return __builtin_bit_cast(float, (unsigned)b << 16); }
__device__ __forceinline__ u16 f2bf(float f) { __hip_bfloat16 h = __float2bfloat16(f); return __builtin_bit_cast(u16, h); }
__device__ __forceinline__ float rbf(float f) { return bf2f(f2bf(f)); }
__device__ __forceinline__ float gelu(float x) {
  float u = 0.7978845608028654f * (x + 0.044715f * x * x * x);
  return x / (1.f + __expf(-2.f * u));
}

__global__ void k_ln(const u16* in, u16* out, u16* out2) {
  int row = blockIdx.x * 4 + (threadIdx.x >> 6), lane = threadIdx.x & 63;
  float v[10];
  float s = 0;
  for (int i = 0; i < 10; i++) { v[i] = bf2f(in[row * D + lane + 64 * i]); s += v[i]; }
  for (int o = 32; o; o >>= 1) s += __shfl_xor(s, o);
  float m = s / D, q = 0;
  for (int i = 0; i < 10; i++) q += (v[i] - m) * (v[i] - m);
  for (int o = 32; o; o >>= 1) q += __shfl_xor(q, o);
  float r = rsqrtf(q / D + 1e-5f);
  s = 0;
  for (int i = 0; i < 10; i++) { v[i] = rbf((v[i] - m) * r); out[row * D + lane + 64 * i] = f2bf(v[i]); s += v[i]; }
  if (out2) {
    for (int o = 32; o; o >>= 1) s += __shfl_xor(s, o);
    m = s / D; q = 0;
    for (int i = 0; i < 10; i++) q += (v[i] - m) * (v[i] - m);
    for (int o = 32; o; o >>= 1) q += __shfl_xor(q, o);
    r = rsqrtf(q / D + 1e-5f);
    for (int i = 0; i < 10; i++) out2[row * D + lane + 64 * i] = f2bf((v[i] - m) * r);
  }
}

template <int MODE>
__global__ __launch_bounds__(256) void k_gemm(const u16* A, const u16* W, u16* C, int N, int K, const u16* e0, const u16* e1, const u16* e2) {
  __shared__ u16 As[64][40];
  __shared__ u16 Bs[64][40];
  int bm = blockIdx.y * 64, bn = blockIdx.x * 64;
  int t = threadIdx.x, w = t >> 6, lane = t & 63, wr = w >> 1, wc = w & 1;
  f32x4 acc[2][2] = {};
  for (int k0 = 0; k0 < K; k0 += 32) {
    {
      int r = t >> 2, c = (t & 3) * 8;
      int row = bm + r;
      bf16x8 v = {};
      if (MODE == 2) {
        int g = (k0 + c) / 160;
        int sh = g == 0 ? -1 : g == 1 ? 1 : g == 2 ? -2 : 2;
        row += sh;
        if (row >= 0 && row < S) v = *(const bf16x8*)(A + (size_t)row * K + k0 + c);
      } else v = *(const bf16x8*)(A + (size_t)row * K + k0 + c);
      *(bf16x8*)&As[r][c] = v;
      int kk = t >> 3, n = (t & 7) * 8;
      bf16x8 wv = *(const bf16x8*)(W + (size_t)(k0 + kk) * N + bn + n);
      for (int j = 0; j < 8; j++) Bs[n + j][kk] = (u16)wv[j];
    }
    __syncthreads();
    bf16x8 a[2], b[2];
    for (int i = 0; i < 2; i++) a[i] = *(const bf16x8*)&As[wr * 32 + i * 16 + (lane & 15)][(lane >> 4) * 8];
    for (int i = 0; i < 2; i++) b[i] = *(const bf16x8*)&Bs[wc * 32 + i * 16 + (lane & 15)][(lane >> 4) * 8];
    for (int i = 0; i < 2; i++) for (int j = 0; j < 2; j++) acc[i][j] = __builtin_amdgcn_mfma_f32_16x16x32_bf16(a[i], b[j], acc[i][j], 0, 0, 0);
    __syncthreads();
  }
  for (int i = 0; i < 2; i++) for (int j = 0; j < 2; j++) for (int r = 0; r < 4; r++) {
    int row = bm + wr * 32 + i * 16 + (lane >> 4) * 4 + r, col = bn + wc * 32 + j * 16 + (lane & 15);
    float v = rbf(acc[i][j][r]);
    size_t o = (size_t)row * N + col;
    if (MODE == 1) v = gelu(v);
    if (MODE == 2) v = bf2f(e0[o]) + v;
    if (MODE == 3) { float y = rbf(bf2f(e0[o]) + v); float g = rbf(y / (1.f + __expf(-y))); v = bf2f(e2[o]) + rbf(bf2f(e1[o]) * g); }
    C[o] = f2bf(v);
  }
}

__global__ void k_attn(const u16* qkv, u16* o) {
  int idx = blockIdx.x * blockDim.x + threadIdx.x;
  int s = idx / H, h = idx % H;
  float q[DH], l[8], mx = -1e30f;
  for (int d = 0; d < DH; d++) q[d] = bf2f(qkv[(size_t)s * 1920 + h * DH + d]);
#pragma unroll
  for (int w = 0; w < 8; w++) {
    int p = s - 2 * w;
    float a = 0;
    if (p >= 0) { for (int d = 0; d < DH; d++) a += q[d] * bf2f(qkv[(size_t)p * 1920 + 640 + h * DH + d]); a = rbf(rbf(a) * rbf(0.22360679774997896f)); }
    else a = -1e9f;
    l[w] = a; mx = fmaxf(mx, a);
  }
  float sum = 0;
#pragma unroll
  for (int w = 0; w < 8; w++) { l[w] = __expf(l[w] - mx); sum += l[w]; }
#pragma unroll
  for (int w = 0; w < 8; w++) l[w] = rbf(l[w] / sum);
  for (int d = 0; d < DH; d++) {
    float a = 0;
#pragma unroll
    for (int w = 0; w < 8; w++) { int p = s - 2 * w; if (p < 0) p = 0; a += l[w] * bf2f(qkv[(size_t)p * 1920 + 1280 + h * DH + d]); }
    o[(size_t)s * D + h * DH + d] = f2bf(a);
  }
}

typedef unsigned v4u __attribute__((ext_vector_type(4)));
struct PP { const char* p[8]; unsigned n[8]; };
__global__ __launch_bounds__(256) void k_probe(const char* p0, const char* p1, const char* p2, const char* p3, unsigned n0, unsigned n1, unsigned n2, unsigned n3, unsigned* sink) {
#if defined(__HIP_DEVICE_COMPILE__)
  unsigned long long t0 = __builtin_amdgcn_s_memrealtime();
  v4u r[12]; unsigned acc = 0;
  const char* ps[4] = {p0, p1, p2, p3}; unsigned ns[4] = {n0, n1, n2, n3};
#pragma unroll
  for (int b = 0; b < 4; b++) {
    __amdgpu_buffer_rsrc_t rs = __builtin_amdgcn_make_buffer_rsrc((void*)ps[b], 0, ns[b], 0x00020000);
    unsigned per = ns[b] / 256;
#pragma unroll
    for (int i = 0; i < 3; i++) { unsigned o = i * 4096 + threadIdx.x * 16; r[b * 3 + i] = __builtin_amdgcn_raw_buffer_load_b128(rs, o < per ? blockIdx.x * per + o : 0x7ffffff0, 0, 0); }
  }
  __builtin_amdgcn_sched_barrier(0);
#pragma unroll
  for (int i = 0; i < 12; i++) acc ^= r[i].x;
  asm volatile("" : "+v"(acc));
  __syncthreads();
  unsigned long long t1 = __builtin_amdgcn_s_memrealtime();
  unsigned long long end = t1 + (t1 - t0) * 50;
  for (int i = 0; i < 2000000; i++) { if (__builtin_amdgcn_s_memrealtime() >= end) break; __builtin_amdgcn_s_sleep(4); }
  if (acc == 0x12345678) sink[0] = acc;
#endif
}
extern "C" void kernel_launch(void* const* d_in, const int* in_sizes, int n_in, void* d_out, int out_size, void* d_ws, size_t ws_size, hipStream_t stream) {
  const u16* x = (const u16*)d_in[0];
  const u16* wqkv = (const u16*)d_in[1];
  const u16* wo = (const u16*)d_in[2];
  const u16* w1 = (const u16*)d_in[4];
  const u16* w2 = (const u16*)d_in[5];
  const u16* w3 = (const u16*)d_in[6];
  const u16* w4 = (const u16*)d_in[7];
  u16* ws = (u16*)d_ws;
  u16* xn = ws; u16* xnn = xn + S * D; u16* qkv = xnn + S * D; u16* h = qkv + S * 1920; u16* o = h + S * D;
  u16* s = o + S * D; u16* x1 = s + S * D; u16* x1n = x1 + S * D; u16* g = x1n + S * D;
  k_probe<<<256, 256, 0, stream>>>((const char*)d_in[3], (const char*)d_in[3], (const char*)d_in[3], (const char*)d_in[3], 0, 0, 0, 0, (unsigned*)d_ws + (200 << 18));
  k_ln<<<S / 4, 256, 0, stream>>>(x, xn, xnn);
  k_gemm<0><<<dim3(1920 / 64, S / 64), 256, 0, stream>>>(xn, wqkv, qkv, 1920, D, nullptr, nullptr, nullptr);
  k_gemm<1><<<dim3(D / 64, S / 64), 256, 0, stream>>>(xnn, w1, h, D, D, nullptr, nullptr, nullptr);
  k_attn<<<S * H / 256, 256, 0, stream>>>(qkv, o);
  k_gemm<0><<<dim3(D / 64, S / 64), 256, 0, stream>>>(o, wo, s, D, D, nullptr, nullptr, nullptr);
  k_gemm<2><<<dim3(D / 64, S / 64), 256, 0, stream>>>(h, w2, x1, D, D, xn, nullptr, nullptr);
  k_ln<<<S / 4, 256, 0, stream>>>(x1, x1n, nullptr);
  k_gemm<1><<<dim3(F / 64, S / 64), 256, 0, stream>>>(x1n, w3, g, F, D, nullptr, nullptr, nullptr);
  k_gemm<3><<<dim3(D / 64, S / 64), 256, 0, stream>>>(g, w4, (u16*)d_out, D, F, x1, s, x);
}
```

```cpp
#include <hip/hip_runtime.h>
#include <hip/hip_bf16.h>
#include <stdint.h>

typedef unsigned short u16;
typedef short bf16x8 __attribute__((ext_vector_type(8)));
typedef float f32x4 __attribute__((ext_vector_type(4)));

constexpr int S = 2048, D = 640, F = 1728, H = 32, DH = 20;

__device__ __forceinline__ float bf2f(u16 b) { return __builtin_bit_cast(float, (unsigned)b << 16); }
__device__ __forceinline__ u16 f2bf(float f) { __hip_bfloat16 h = __float2bfloat16(f); return __builtin_bit_cast(u16, h); }
__device__ __forceinline__ float rbf(float f) { return bf2f(f2bf(f)); }
__device__ __forceinline__ float gelu(float x) {
  float u = 0.7978845608028654f * (x + 0.044715f * x * x * x);
  return x / (1.f + __expf(-2.f * u));
}

__global__ void k_ln(const u16* in, u16* out, u16* out2) {
  int row = blockIdx.x * 4 + (threadIdx.x >> 6), lane = threadIdx.x & 63;
  float v[10];
  float s = 0;
  for (int i = 0; i < 10; i++) { v[i] = bf2f(in[row * D + lane + 64 * i]); s += v[i]; }
  for (int o = 32; o; o >>= 1) s += __shfl_xor(s, o);
  float m = s / D, q = 0;
  for (int i = 0; i < 10; i++) q += (v[i] - m) * (v[i] - m);
  for (int o = 32; o; o >>= 1) q += __shfl_xor(q, o);
  float r = rsqrtf(q / D + 1e-5f);
  s = 0;
  for (int i = 0; i < 10; i++) { v[i] = rbf((v[i] - m) * r); out[row * D + lane + 64 * i] = f2bf(v[i]); s += v[i]; }
  if (out2) {
    for (int o = 32; o; o >>= 1) s += __shfl_xor(s, o);
    m = s / D; q = 0;
    for (int i = 0; i < 10; i++) q += (v[i] - m) * (v[i] - m);
    for (int o = 32; o; o >>= 1) q += __shfl_xor(q, o);
    r = rsqrtf(q / D + 1e-5f);
    for (int i = 0; i < 10; i++) out2[row * D + lane + 64 * i] = f2bf((v[i] - m) * r);
  }
}

template <int MODE>
__global__ __launch_bounds__(256) void k_gemm(const u16* A, const u16* W, u16* C, int N, int K, const u16* e0, const u16* e1, const u16* e2) {
  __shared__ u16 As[64][40];
  __shared__ u16 Bs[64][40];
  int bm = blockIdx.y * 64, bn = blockIdx.x * 64;
  int t = threadIdx.x, w = t >> 6, lane = t & 63, wr = w >> 1, wc = w & 1;
  f32x4 acc[2][2] = {};
  for (int k0 = 0; k0 < K; k0 += 32) {
    {
      int r = t >> 2, c = (t & 3) * 8;
      int row = bm + r;
      bf16x8 v = {};
      if (MODE == 2) {
        int g = (k0 + c) / 160;
        int sh = g == 0 ? -1 : g == 1 ? 1 : g == 2 ? -2 : 2;
        row += sh;
        if (row >= 0 && row < S) v = *(const bf16x8*)(A + (size_t)row * K + k0 + c);
      } else v = *(const bf16x8*)(A + (size_t)row * K + k0 + c);
      *(bf16x8*)&As[r][c] = v;
      int kk = t >> 3, n = (t & 7) * 8;
      bf16x8 wv = *(const bf16x8*)(W + (size_t)(k0 + kk) * N + bn + n);
      for (int j = 0; j < 8; j++) Bs[n + j][kk] = (u16)wv[j];
    }
    __syncthreads();
    bf16x8 a[2], b[2];
    for (int i = 0; i < 2; i++) a[i] = *(const bf16x8*)&As[wr * 32 + i * 16 + (lane & 15)][(lane >> 4) * 8];
    for (int i = 0; i < 2; i++) b[i] = *(const bf16x8*)&Bs[wc * 32 + i * 16 + (lane & 15)][(lane >> 4) * 8];
    for (int i = 0; i < 2; i++) for (int j = 0; j < 2; j++) acc[i][j] = __builtin_amdgcn_mfma_f32_16x16x32_bf16(a[i], b[j], acc[i][j], 0, 0, 0);
    __syncthreads();
  }
  for (int i = 0; i < 2; i++) for (int j = 0; j < 2; j++) for (int r = 0; r < 4; r++) {
    int row = bm + wr * 32 + i * 16 + (lane >> 4) * 4 + r, col = bn + wc * 32 + j * 16 + (lane & 15);
    float v = rbf(acc[i][j][r]);
    size_t o = (size_t)row * N + col;
    if (MODE == 1) v = gelu(v);
    if (MODE == 2) v = bf2f(e0[o]) + v;
    if (MODE == 3) { float y = rbf(bf2f(e0[o]) + v); float g = rbf(y / (1.f + __expf(-y))); v = bf2f(e2[o]) + rbf(bf2f(e1[o]) * g); }
    C[o] = f2bf(v);
  }
}

__global__ void k_attn(const u16* qkv, u16* o) {
  int idx = blockIdx.x * blockDim.x + threadIdx.x;
  int s = idx / H, h = idx % H;
  float q[DH], l[8], mx = -1e30f;
  for (int d = 0; d < DH; d++) q[d] = bf2f(qkv[(size_t)s * 1920 + h * DH + d]);
#pragma unroll
  for (int w = 0; w < 8; w++) {
    int p = s - 2 * w;
    float a = 0;
    if (p >= 0) { for (int d = 0; d < DH; d++) a += q[d] * bf2f(qkv[(size_t)p * 1920 + 640 + h * DH + d]); a = rbf(rbf(a) * rbf(0.22360679774997896f)); }
    else a = -1e9f;
    l[w] = a; mx = fmaxf(mx, a);
  }
  float sum = 0;
#pragma unroll
  for (int w = 0; w < 8; w++) { l[w] = __expf(l[w] - mx); sum += l[w]; }
#pragma unroll
  for (int w = 0; w < 8; w++) l[w] = rbf(l[w] / sum);
  for (int d = 0; d < DH; d++) {
    float a = 0;
#pragma unroll
    for (int w = 0; w < 8; w++) { int p = s - 2 * w; if (p < 0) p = 0; a += l[w] * bf2f(qkv[(size_t)p * 1920 + 1280 + h * DH + d]); }
    o[(size_t)s * D + h * DH + d] = f2bf(a);
  }
}

__global__ __launch_bounds__(256) void k_spin(unsigned* sink, int n) {
#if defined(__HIP_DEVICE_COMPILE__)
  unsigned long long t0 = __builtin_amdgcn_s_memtime();
  for (int i = 0; i < 4000000; i++) { if (__builtin_amdgcn_s_memtime() - t0 >= (unsigned long long)n) break; }
  if (n == 12345) sink[0] = 1;
#endif
}
__global__ __launch_bounds__(256) void k_mf(const u16* x, unsigned* sink, int n) {
#if defined(__HIP_DEVICE_COMPILE__)
  bf16x8 a = *(const bf16x8*)(x + threadIdx.x * 8 + blockIdx.x * 2048), b = *(const bf16x8*)(x + threadIdx.x * 8 + blockIdx.x * 2048 + 600000);
  f32x4 c0 = {}, c1 = {}, c2 = {}, c3 = {};
  for (int i = 0; i < n; i++) {
#pragma unroll
    for (int j = 0; j < 16; j++) {
      c0 = __builtin_amdgcn_mfma_f32_16x16x32_bf16(a, b, c0, 0, 0, 0); c1 = __builtin_amdgcn_mfma_f32_16x16x32_bf16(b, a, c1, 0, 0, 0);
      c2 = __builtin_amdgcn_mfma_f32_16x16x32_bf16(a, a, c2, 0, 0, 0); c3 = __builtin_amdgcn_mfma_f32_16x16x32_bf16(b, b, c3, 0, 0, 0);
    }
  }
  if (c0.x + c1.y + c2.z + c3.w == 12345.f) sink[0] = 1;
#endif
}
extern "C" void kernel_launch(void* const* d_in, const int* in_sizes, int n_in, void* d_out, int out_size, void* d_ws, size_t ws_size, hipStream_t stream) {
  const u16* x = (const u16*)d_in[0];
  const u16* wqkv = (const u16*)d_in[1];
  const u16* wo = (const u16*)d_in[2];
  const u16* w1 = (const u16*)d_in[4];
  const u16* w2 = (const u16*)d_in[5];
  const u16* w3 = (const u16*)d_in[6];
  const u16* w4 = (const u16*)d_in[7];
  u16* ws = (u16*)d_ws;
  u16* xn = ws; u16* xnn = xn + S * D; u16* qkv = xnn + S * D; u16* h = qkv + S * 1920; u16* o = h + S * D;
  u16* s = o + S * D; u16* x1 = s + S * D; u16* x1n = x1 + S * D; u16* g = x1n + S * D;
  k_spin<<<256, 256, 0, stream>>>((unsigned*)d_ws + (200 << 18), 0);
  k_ln<<<S / 4, 256, 0, stream>>>(x, xn, xnn);
  k_gemm<0><<<dim3(1920 / 64, S / 64), 256, 0, stream>>>(xn, wqkv, qkv, 1920, D, nullptr, nullptr, nullptr);
  k_gemm<1><<<dim3(D / 64, S / 64), 256, 0, stream>>>(xnn, w1, h, D, D, nullptr, nullptr, nullptr);
  k_attn<<<S * H / 256, 256, 0, stream>>>(qkv, o);
  k_gemm<0><<<dim3(D / 64, S / 64), 256, 0, stream>>>(o, wo, s, D, D, nullptr, nullptr, nullptr);
  k_gemm<2><<<dim3(D / 64, S / 64), 256, 0, stream>>>(h, w2, x1, D, D, xn, nullptr, nullptr);
  k_ln<<<S / 4, 256, 0, stream>>>(x1, x1n, nullptr);
  k_gemm<1><<<dim3(F / 64, S / 64), 256, 0, stream>>>(x1n, w3, g, F, D, nullptr, nullptr, nullptr);
  k_gemm<3><<<dim3(D / 64, S / 64), 256, 0, stream>>>(g, w4, (u16*)d_out, D, F, x1, s, x);
}
```

```cpp
#include <hip/hip_runtime.h>

__global__ __launch_bounds__(256, 1) void fused_block_kernel(const void* p_x, unsigned n_x, unsigned c_x, const void* p_wqkv, unsigned n_wqkv, unsigned c_wqkv, const void* p_wo, unsigned n_wo, unsigned c_wo, const void* p_w1, unsigned n_w1, unsigned c_w1, const void* p_w2, unsigned n_w2, unsigned c_w2, const void* p_w3, unsigned n_w3, unsigned c_w3, const void* p_w4, unsigned n_w4, unsigned c_w4, const void* p_out, unsigned n_out, unsigned c_out, const void* p_ws, unsigned n_ws, unsigned c_ws) {
#if defined(__HIP_DEVICE_COMPILE__)
  __shared__ __attribute__((aligned(16))) char lds[163840];
  asm volatile(
    "s_load_dwordx16 s[4:19], %0, 0\n"
    "s_load_dwordx16 s[20:35], %0, 64\n"
    "s_load_dwordx4 s[36:39], %0, 128\n"
    "s_mov_b32 s40, %1\n"
    "v_and_b32 v1, 63, %2\n"
    "v_lshrrev_b32 v58, 6, %2\n"
    "s_nop 1\n"
    "v_readfirstlane_b32 s41, v58\n"
    "s_and_b32 s65, s40, 7\n"
    "s_lshr_b32 s42, s40, 3\n"
    "s_lshr_b32 s43, s65, 1\n"
    "s_and_b32 s44, s65, 1\n"
    "s_getpc_b64 s[90:91]\n"
    ".Lpc:\n"
    "s_lshl_b32 s64, s42, 2\n"
    "s_add_u32 s64, s64, s41\n"
    "s_lshl_b32 s64, s64, 10\n"
    "v_lshl_add_u32 v58, v1, 4, s64\n"
    "v_min_u32 v58, .Lcodeend-.Lpc-16, v58\n"
    "global_load_dwordx4 v[124:127], v58, s[90:91]\n"
    "s_mul_i32 s66, s42, 13\n"
    "s_lshr_b32 s66, s66, 6\n"
    "s_max_i32 s68, s42, 20\n"
    "s_sub_u32 s68, s68, 20\n"
    "s_mul_i32 s69, s68, 11\n"
    "s_lshr_b32 s69, s69, 5\n"
    "s_cmp_lt_u32 s42, 20\n"
    "s_cselect_b32 s71, s66, s69\n"
    "s_lshl_b32 s45, s43, 2\n"
    "s_add_u32 s45, s45, s71\n"
    "s_lshr_b32 s73, s71, 1\n"
    "s_mul_i32 s64, s73, -8\n"
    "s_add_u32 s64, s64, 5\n"
    "s_lshl_b32 s47, s71, 1\n"
    "s_add_u32 s64, s64, s47\n"
    "s_lshl_b32 s73, s73, 1\n"
    "s_cmp_lt_u32 s42, 20\n"
    "s_cselect_b32 s73, s73, s64\n"
    "s_mul_i32 s74, s65, 3\n"
    "s_add_u32 s74, s74, s42\n"
    "s_add_u32 s64, s74, s73\n"
    "s_mul_i32 s47, s64, 13\n"
    "s_lshr_b32 s47, s47, 7\n"
    "s_mul_i32 s47, s47, 10\n"
    "s_sub_u32 s47, s64, s47\n"
    "s_lshl_b32 s57, s41, 12\n"
    "v_lshrrev_b32 v2, 4, v1\n"
    "s_lshl_b32 s64, s45, 7\n"
    "s_lshl_b32 s73, s41, 5\n"
    "s_add_u32 s64, s64, s73\n"
    "s_mul_i32 s64, s64, 0x500\n"
    "v_lshrrev_b32 v58, 3, v1\n"
    "v_mul_u32_u24 v58, 0x500, v58\n"
    "v_add_u32 v58, s64, v58\n"
    "v_and_b32 v59, 7, v1\n"
    "v_xor_b32 v59, v59, v2\n"
    "v_lshl_add_u32 v38, v59, 4, v58\n"
    "v_xor_b32 v59, 4, v59\n"
    "v_lshl_add_u32 v39, v59, 4, v58\n"
    "v_add_u32 v39, 0x2400, v39\n"
    "v_add_u32 v40, 0x4800, v38\n"
    "v_add_u32 v41, 0x4800, v39\n"
    "s_lshl_b32 s58, s47, 7\n"
    "s_waitcnt lgkmcnt(0)\n"
    "s_add_u32 m0, s57, 0\n"
    "s_nop 0\n"
    "buffer_load_dwordx4 v38, s[4:7], s58 offen offset:0 lds\n"
    "buffer_load_dwordx4 v39, s[4:7], s58 offen offset:1024 lds\n"
    "buffer_load_dwordx4 v40, s[4:7], s58 offen offset:2048 lds\n"
    "buffer_load_dwordx4 v41, s[4:7], s58 offen offset:3072 lds\n"
    "s_mul_i32 s67, s66, 5\n"
    "s_sub_u32 s67, s42, s67\n"
    "s_mul_i32 s50, s44, 5\n"
    "s_add_u32 s50, s50, s67\n"
    "s_lshl_b32 s73, s68, 3\n"
    "s_add_u32 s73, s73, s65\n"
    "s_cmp_lt_u32 s42, 20\n"
    "s_cselect_b32 s48, s50, s73\n"
    "s_cselect_b32 s73, 0x63, s73\n"
    "s_cmp_lt_u32 s73, 27\n"
    "s_cselect_b32 s99, 1, 0\n"
    "s_cselect_b32 s16, s24, s16\n"
    "s_cselect_b32 s17, s25, s17\n"
    "s_cselect_b32 s64, s26, 0\n"
    "s_mov_b32 s73, 0x500\n"
    "s_cselect_b32 s73, 0xd80, s73\n"
    "s_cmp_lt_u32 s42, 20\n"
    "s_cselect_b32 s18, s18, s64\n"
    "s_lshl_b32 s56, s41, 11\n"
    "v_lshrrev_b32 v22, 3, v1\n"
    "s_lshl_b32 s64, s41, 3\n"
    "v_add_u32 v22, s64, v22\n"
    "v_and_b32 v23, 1, v2\n"
    "v_lshlrev_b32 v23, 2, v23\n"
    "s_and_b32 s64, s41, 1\n"
    "s_lshl_b32 s64, s64, 1\n"
    "v_or_b32 v23, s64, v23\n"
    "v_and_b32 v58, 7, v1\n"
    "v_xor_b32 v23, v23, v58\n"
    "v_lshlrev_b32 v23, 4, v23\n"
    "s_lshl_b32 s97, s73, 6\n"
    "s_mul_i32 s98, s97, 9\n"
    "s_mul_i32 s59, s47, s97\n"
    "s_lshl_b32 s64, s48, 7\n"
    "v_mad_u32_u24 v58, v22, s73, v23\n"
    "v_add_u32 v46, s64, v58\n"
    "s_lshl_b32 s73, s73, 5\n"
    "s_sub_u32 s73, s73, 0x400\n"
    "v_add_u32 v47, s73, v46\n"
    "s_add_u32 m0, s56, 0x4000\n"
    "s_nop 0\n"
    "buffer_load_dwordx4 v46, s[16:19], s59 offen offset:0 lds\n"
    "buffer_load_dwordx4 v47, s[16:19], s59 offen offset:1024 lds\n"
    "s_mul_i32 s70, s69, 3\n"
    "s_sub_u32 s70, s68, s70\n"
    "s_add_u32 s70, s70, 5\n"
    "s_cmp_lt_u32 s42, 20\n"
    "s_cselect_b32 s72, s67, s70\n"
    "s_lshl_b32 s46, s44, 3\n"
    "s_add_u32 s46, s46, s72\n"
    "s_mul_i32 s49, s43, 6\n"
    "s_add_u32 s49, s49, s66\n"
    "v_and_b32 v3, 15, v1\n"
    "s_mul_i32 s64, s74, 13\n"
    "s_lshr_b32 s64, s64, 7\n"
    "s_mul_i32 s64, s64, 10\n"
    "s_sub_u32 s74, s74, s64\n"
    "s_add_u32 s75, s66, s67\n"
    "s_mul_i32 s64, s43, 2\n"
    "s_add_u32 s75, s75, s64\n"
    "s_cmp_ge_u32 s75, 10\n"
    "s_cselect_b32 s64, 10, 0\n"
    "s_sub_u32 s75, s75, s64\n"
    "s_mul_i32 s66, s65, 20\n"
    "s_add_u32 s66, s66, s42\n"
    "s_lshr_b32 s67, s66, 4\n"
    "s_and_b32 s68, s66, 15\n"
    "s_lshl_b32 s64, s67, 6\n"
    "s_lshl_b32 s69, s41, 4\n"
    "s_add_u32 s64, s64, s69\n"
    "s_mul_i32 s64, s64, 0xf00\n"
    "s_mul_i32 s69, s68, 0x50\n"
    "s_add_u32 s64, s64, s69\n"
    "s_cmp_lt_u32 s42, 20\n"
    "s_cselect_b32 s64, s64, 0x40000000\n"
    "v_mul_u32_u24 v58, 0xf00, v2\n"
    "v_add_u32 v58, s64, v58\n"
    "v_lshlrev_b32 v59, 1, v2\n"
    "v_xor_b32 v59, v59, v3\n"
    "v_mov_b32 v60, 0\n"
    "v_cmp_lt_u32 vcc, 4, v59\n"
    "v_mov_b32 v61, 0x4f0\n"
    "s_nop 0\n"
    "v_cndmask_b32 v60, v60, v61, vcc\n"
    "v_cmp_lt_u32 vcc, 5, v59\n"
    "v_mov_b32 v61, 0x4a0\n"
    "s_nop 0\n"
    "v_cndmask_b32 v60, v60, v61, vcc\n"
    "v_cmp_lt_u32 vcc, 9, v59\n"
    "v_mov_b32 v61, 0x960\n"
    "s_nop 0\n"
    "v_cndmask_b32 v60, v60, v61, vcc\n"
    "v_cmp_lt_u32 vcc, 14, v59\n"
    "v_mov_b32 v61, 0x950\n"
    "s_nop 0\n"
    "v_cndmask_b32 v60, v60, v61, vcc\n"
    "v_lshl_add_u32 v60, v59, 4, v60\n"
    "v_add_u32 v60, v58, v60\n"
    "v_add_u32 v80, 0, v60\n"
    "v_add_u32 v81, 0x3800, v60\n"
    "v_xor_b32 v59, 8, v59\n"
    "v_mov_b32 v60, 0\n"
    "v_cmp_lt_u32 vcc, 4, v59\n"
    "v_mov_b32 v61, 0x4f0\n"
    "s_nop 0\n"
    "v_cndmask_b32 v60, v60, v61, vcc\n"
    "v_cmp_lt_u32 vcc, 5, v59\n"
    "v_mov_b32 v61, 0x4a0\n"
    "s_nop 0\n"
    "v_cndmask_b32 v60, v60, v61, vcc\n"
    "v_cmp_lt_u32 vcc, 9, v59\n"
    "v_mov_b32 v61, 0x960\n"
    "s_nop 0\n"
    "v_cndmask_b32 v60, v60, v61, vcc\n"
    "v_cmp_lt_u32 vcc, 14, v59\n"
    "v_mov_b32 v61, 0x950\n"
    "s_nop 0\n"
    "v_cndmask_b32 v60, v60, v61, vcc\n"
    "v_lshl_add_u32 v60, v59, 4, v60\n"
    "v_add_u32 v60, v58, v60\n"
    "v_add_u32 v82, 0x7000, v60\n"
    "v_add_u32 v83, 0xa800, v60\n"
    "v_mov_b32 v58, 0\n"
    "s_add_u32 m0, s57, 0x22000\n"
    "s_nop 0\n"
    "buffer_load_dwordx4 v80, s[8:11], 0 offen offset:0 lds\n"
    "buffer_load_dwordx4 v81, s[8:11], 0 offen offset:1024 lds\n"
    "buffer_load_dwordx4 v82, s[8:11], 0 offen offset:2048 lds\n"
    "buffer_load_dwordx4 v83, s[8:11], 0 offen offset:3072 lds\n"
    "buffer_load_dwordx2 v[4:5], v58, s[36:39], 0 offen offset:0 sc1\n"
    "s_add_u32 m0, s56, 0x6000\n"
    "s_sub_u32 s59, s59, s97\n"
    "s_cselect_b32 s59, s98, s59\n"
    "buffer_load_dwordx4 v46, s[16:19], s59 offen offset:0 lds\n"
    "buffer_load_dwordx4 v47, s[16:19], s59 offen offset:1024 lds\n"
    "s_lshr_b32 s54, s41, 1\n"
    "s_and_b32 s55, s41, 1\n"
    "s_mul_i32 s64, s49, 11\n"
    "s_lshr_b32 s64, s64, 5\n"
    "s_mul_i32 s65, s64, 3\n"
    "s_sub_u32 s65, s49, s65\n"
    "s_mov_b32 s93, s64\n"
    "s_mov_b32 s3, s65\n"
    "s_lshl_b32 s51, s64, 8\n"
    "s_mul_i32 s69, s65, 0x50\n"
    "s_cmp_eq_u32 s65, 0\n"
    "s_cselect_b32 s70, 0, 16\n"
    "s_add_u32 s69, s69, s70\n"
    "s_add_u32 s51, s51, s69\n"
    "s_cmp_lt_u32 s42, 30\n"
    "s_cselect_b32 s22, s22, 0\n"
    "s_cselect_b32 s14, s14, 0\n"
    "s_cselect_b32 s30, s30, 0\n"
    "s_cselect_b32 s34, s34, 0\n"
    "s_lshl_b32 s69, s50, 7\n"
    "v_mul_u32_u24 v58, 0x500, v22\n"
    "v_add_u32 v58, v58, v23\n"
    "v_add_u32 v20, s69, v58\n"
    "v_add_u32 v21, 0x9c00, v20\n"
    "s_mul_i32 s70, s46, 0x28000\n"
    "s_add_u32 s70, s70, 0x16b000\n"
    "s_add_u32 s70, s70, s56\n"
    "v_lshl_add_u32 v18, v1, 4, s70\n"
    "v_add_u32 v19, 0x1800, v18\n"
    "s_mul_i32 s70, s46, 0x1400\n"
    "s_lshl_b32 s71, s41, 7\n"
    "s_add_u32 s70, s70, s71\n"
    "s_add_u32 s70, s70, 0x156000\n"
    "v_and_b32 v59, 3, v1\n"
    "v_lshlrev_b32 v59, 9, v59\n"
    "v_and_b32 v58, 60, v1\n"
    "v_add_u32 v59, v58, v59\n"
    "v_add_u32 v11, s70, v59\n"
    "v_mov_b32 v48, 0x3f803f80\n"
    "v_mov_b32 v49, 0x3f803f80\n"
    "v_mov_b32 v50, 0x3f803f80\n"
    "v_mov_b32 v51, 0x3f803f80\n"
    "v_lshrrev_b32 v58, 1, v3\n"
    "v_xor_b32 v58, v58, v2\n"
    "v_lshlrev_b32 v58, 4, v58\n"
    "v_lshl_add_u32 v58, v3, 7, v58\n"
    "v_add_u32 v42, s57, v58\n"
    "v_xor_b32 v43, 64, v42\n"
    "v_add_u32 v44, 0x10000, v42\n"
    "v_add_u32 v45, 0x10000, v43\n"
    "s_cmp_eq_u32 s41, 0\n"
    "s_cselect_b32 s69, 16, 0\n"
    "v_cmp_gt_u32 vcc, s69, v1\n"
    "v_lshlrev_b32 v58, 3, v1\n"
    "v_mov_b32 v59, 0x40000000\n"
    "v_cndmask_b32 v9, v59, v58, vcc\n"
    "s_lshl_b32 s69, s69, 1\n"
    "v_cmp_gt_u32 vcc, s69, v1\n"
    "v_lshlrev_b32 v58, 7, v1\n"
    "s_nop 0\n"
    "v_cndmask_b32 v8, v59, v58, vcc\n"
    "s_lshl_b32 s69, s46, 7\n"
    "s_add_u32 s69, s69, 0x1000\n"
    "v_lshl_add_u32 v58, v1, 11, s69\n"
    "v_cmp_gt_u32 vcc, 10, v1\n"
    "s_nop 1\n"
    "v_cndmask_b32 v58, v59, v58, vcc\n"
    "s_cmp_eq_u32 s41, 0\n"
    "s_cselect_b32 s69, 0, 0x40000000\n"
    "v_or_b32 v10, s69, v58\n"
    "s_lshl_b32 s69, s67, 4\n"
    "s_add_u32 s69, s69, s68\n"
    "s_cmp_lt_u32 s42, 20\n"
    "s_cselect_b32 s69, s69, s40\n"
    "s_lshl_b32 s69, s69, 7\n"
    "s_add_u32 s83, s69, 0x1000\n"
    "s_mov_b32 s69, 0x3ff\n"
    "s_cmp_eq_u32 s41, 0\n"
    "s_cselect_b32 s69, s69, 0\n"
    "s_not_b32 s76, s69\n"
    "s_mul_i32 s69, s45, 10\n"
    "s_add_u32 s69, s69, s48\n"
    "s_cmp_lt_u32 s42, 20\n"
    "s_cselect_b32 s69, s69, s40\n"
    "s_lshl_b32 s69, s69, 7\n"
    "s_add_u32 s84, s69, 0x9000\n"
    "s_mov_b32 s69, 0xfffff\n"
    "s_cmp_eq_u32 s49, 0\n"
    "s_cselect_b32 s69, 0xffc00, s69\n"
    "s_cmp_eq_u32 s49, 23\n"
    "s_cselect_b32 s69, 0x3ff, s69\n"
    "s_cmp_lt_u32 s42, 30\n"
    "s_cselect_b32 s69, s69, 0\n"
    "s_cmp_eq_u32 s41, 0\n"
    "s_cselect_b32 s69, s69, 0\n"
    "s_not_b32 s77, s69\n"
    "s_mul_i32 s69, s68, 0x28000\n"
    "s_lshl_b32 s70, s67, 14\n"
    "s_add_u32 s69, s69, s70\n"
    "s_add_u32 s69, s69, 0x16b000\n"
    "s_add_u32 s69, s69, s56\n"
    "s_cmp_lt_u32 s42, 20\n"
    "s_cselect_b32 s69, s69, 0x40000000\n"
    "v_lshl_add_u32 v67, v1, 4, s69\n"
    "s_mov_b32 s66, 0x2000\n"
    "s_mul_i32 s69, s68, 10\n"
    "s_add_u32 s69, s69, s67\n"
    "s_lshl_b32 s69, s69, 9\n"
    "s_lshl_b32 s70, s41, 7\n"
    "s_add_u32 s69, s69, s70\n"
    "s_add_u32 s69, s69, 0x157000\n"
    "s_cmp_lt_u32 s42, 20\n"
    "s_cselect_b32 s69, s69, 0x40000000\n"
    "v_lshl_add_u32 v58, v1, 2, s69\n"
    "v_cmp_gt_u32 vcc, 32, v1\n"
    "v_mov_b32 v64, 0x40000000\n"
    "s_nop 0\n"
    "v_cndmask_b32 v64, v64, v58, vcc\n"
    "v_bfe_u32 v58, v1, 2, 2\n"
    "v_and_b32 v59, 1, v2\n"
    "v_lshl_add_u32 v59, v59, 2, v58\n"
    "s_lshl_b32 s69, s41, 1\n"
    "v_xor_b32 v59, s69, v59\n"
    "v_lshlrev_b32 v59, 5, v59\n"
    "v_lshl_add_u32 v58, v2, 3, v58\n"
    "v_lshl_add_u32 v58, v58, 8, v59\n"
    "v_and_b32 v59, 3, v1\n"
    "v_lshl_add_u32 v58, v59, 3, v58\n"
    "v_add_u32 v65, 0x22000, v58\n"
    "v_xor_b32 v66, 32, v65\n"
    "v_bfe_u32 v58, v1, 3, 1\n"
    "v_lshlrev_b32 v58, 1, v58\n"
    "v_and_b32 v59, 1, v2\n"
    "v_or_b32 v59, v58, v59\n"
    "v_xor_b32 v59, s41, v59\n"
    "v_lshlrev_b32 v59, 5, v59\n"
    "v_bfe_u32 v58, v1, 2, 2\n"
    "v_lshlrev_b32 v58, 7, v58\n"
    "v_lshl_add_u32 v58, v2, 11, v58\n"
    "v_or_b32 v59, v58, v59\n"
    "v_and_b32 v58, 3, v1\n"
    "v_lshl_add_u32 v26, v58, 3, v59\n"
    "v_xor_b32 v27, 32, v26\n"
    "v_xor_b32 v28, 64, v26\n"
    "v_xor_b32 v29, 0x60, v26\n"
    "v_add_u32 v30, 0x10000, v26\n"
    "v_add_u32 v31, 0x10000, v27\n"
    "v_add_u32 v32, 0x10000, v28\n"
    "v_add_u32 v33, 0x10000, v29\n"
    "s_add_u32 m0, s57, 0x8000\n"
    "s_sub_u32 s58, s58, 0x80\n"
    "s_cselect_b32 s58, 0x480, s58\n"
    "buffer_load_dwordx4 v38, s[4:7], s58 offen offset:0 lds\n"
    "buffer_load_dwordx4 v39, s[4:7], s58 offen offset:1024 lds\n"
    "buffer_load_dwordx4 v40, s[4:7], s58 offen offset:2048 lds\n"
    "buffer_load_dwordx4 v41, s[4:7], s58 offen offset:3072 lds\n"
    "s_waitcnt vmcnt(11)\n"
    "s_barrier\n"
    "ds_read_b128 a[0:3], v42 offset:0\n"
    "ds_read_b128 a[4:7], v42 offset:2048\n"
    "ds_read_b64_tr_b16 v[192:193], v26 offset:16384\n"
    "ds_read_b64_tr_b16 v[194:195], v26 offset:16896\n"
    "ds_read_b64_tr_b16 v[196:197], v27 offset:16384\n"
    "ds_read_b64_tr_b16 v[198:199], v27 offset:16896\n"
    "ds_read_b64_tr_b16 v[200:201], v28 offset:16384\n"
    "ds_read_b64_tr_b16 v[202:203], v28 offset:16896\n"
    "ds_read_b64_tr_b16 v[204:205], v29 offset:16384\n"
    "ds_read_b64_tr_b16 v[206:207], v29 offset:16896\n"
    "s_waitcnt lgkmcnt(0)\n"
    "v_mfma_f32_16x16x32_bf16 v[160:163], v[48:51], a[0:3], 0\n"
    "ds_read_b128 a[8:11], v43 offset:0\n"
    "s_add_u32 m0, s57, 0xc000\n"
    "v_mfma_f32_16x16x32_bf16 v[168:171], a[0:3], a[0:3], 0\n"
    "ds_read_b128 a[12:15], v43 offset:2048\n"
    "v_mfma_f32_16x16x32_bf16 v[164:167], v[48:51], a[4:7], 0\n"
    "ds_read_b64_tr_b16 v[208:209], v26 offset:17408\n"
    "v_mfma_f32_16x16x32_bf16 v[172:175], a[4:7], a[4:7], 0\n"
    "ds_read_b64_tr_b16 v[210:211], v26 offset:17920\n"
    "v_mfma_f32_16x16x32_bf16 v[176:179], v[192:195], v[48:51], 0\n"
    "s_sub_u32 s58, s58, 0x80\n"
    "s_cselect_b32 s58, 0x480, s58\n"
    "ds_read_b64_tr_b16 v[212:213], v27 offset:17408\n"
    "v_mfma_f32_16x16x32_bf16 v[128:131], v[192:195], a[0:3], 0\n"
    "buffer_load_dwordx4 v38, s[4:7], s58 offen offset:0 lds\n"
    "ds_read_b64_tr_b16 v[214:215], v27 offset:17920\n"
    "v_mfma_f32_16x16x32_bf16 v[144:147], v[192:195], a[4:7], 0\n"
    "ds_read_b64_tr_b16 v[216:217], v28 offset:17408\n"
    "buffer_load_dwordx4 v39, s[4:7], s58 offen offset:1024 lds\n"
    "v_mfma_f32_16x16x32_bf16 v[132:135], v[196:199], a[0:3], 0\n"
    "ds_read_b64_tr_b16 v[218:219], v28 offset:17920\n"
    "v_mfma_f32_16x16x32_bf16 v[148:151], v[196:199], a[4:7], 0\n"
    "ds_read_b64_tr_b16 v[220:221], v29 offset:17408\n"
    "buffer_load_dwordx4 v40, s[4:7], s58 offen offset:2048 lds\n"
    "v_mfma_f32_16x16x32_bf16 v[136:139], v[200:203], a[0:3], 0\n"
    "ds_read_b64_tr_b16 v[222:223], v29 offset:17920\n"
    "v_mfma_f32_16x16x32_bf16 v[152:155], v[200:203], a[4:7], 0\n"
    "buffer_load_dwordx4 v41, s[4:7], s58 offen offset:3072 lds\n"
    "s_add_u32 m0, s56, 0x10000\n"
    "v_mfma_f32_16x16x32_bf16 v[140:143], v[204:207], a[0:3], 0\n"
    "s_sub_u32 s59, s59, s97\n"
    "s_cselect_b32 s59, s98, s59\n"
    "v_mfma_f32_16x16x32_bf16 v[156:159], v[204:207], a[4:7], 0\n"
    "s_waitcnt vmcnt(8) lgkmcnt(0)\n"
    "s_barrier\n"
    "v_mfma_f32_16x16x32_bf16 v[160:163], v[48:51], a[8:11], v[160:163]\n"
    "s_waitcnt vmcnt(4)\n"
    "ds_read_b128 a[16:19], v42 offset:32768\n"
    "ds_read_b128 a[20:23], v42 offset:34816\n"
    "v_mfma_f32_16x16x32_bf16 v[168:171], a[8:11], a[8:11], v[168:171]\n"
    "ds_read_b64_tr_b16 v[192:193], v26 offset:24576\n"
    "ds_read_b64_tr_b16 v[194:195], v26 offset:25088\n"
    "buffer_load_dwordx4 v46, s[16:19], s59 offen offset:0 lds\n"
    "v_mfma_f32_16x16x32_bf16 v[164:167], v[48:51], a[12:15], v[164:167]\n"
    "ds_read_b64_tr_b16 v[196:197], v27 offset:24576\n"
    "ds_read_b64_tr_b16 v[198:199], v27 offset:25088\n"
    "v_mfma_f32_16x16x32_bf16 v[172:175], a[12:15], a[12:15], v[172:175]\n"
    "ds_read_b64_tr_b16 v[200:201], v28 offset:24576\n"
    "ds_read_b64_tr_b16 v[202:203], v28 offset:25088\n"
    "v_mfma_f32_16x16x32_bf16 v[176:179], v[208:211], v[48:51], v[176:179]\n"
    "ds_read_b64_tr_b16 v[204:205], v29 offset:24576\n"
    "buffer_load_dwordx4 v47, s[16:19], s59 offen offset:1024 lds\n"
    "ds_read_b64_tr_b16 v[206:207], v29 offset:25088\n"
    "v_mfma_f32_16x16x32_bf16 v[128:131], v[208:211], a[8:11], v[128:131]\n"
    "ds_read_b64_tr_b16 v[224:225], v65 offset:0\n"
    "ds_read_b64_tr_b16 v[226:227], v65 offset:1024\n"
    "s_add_u32 m0, s56, 0x12000\n"
    "s_sub_u32 s59, s59, s97\n"
    "s_cselect_b32 s59, s98, s59\n"
    "v_mfma_f32_16x16x32_bf16 v[144:147], v[208:211], a[12:15], v[144:147]\n"
    "ds_read_b64_tr_b16 v[228:229], v66 offset:0\n"
    "buffer_load_dwordx4 v46, s[16:19], s59 offen offset:0 lds\n"
    "ds_read_b64_tr_b16 v[230:231], v66 offset:1024\n"
    "v_mfma_f32_16x16x32_bf16 v[132:135], v[212:215], a[8:11], v[132:135]\n"
    "ds_read_b64_tr_b16 v[232:233], v65 offset:8192\n"
    "ds_read_b64_tr_b16 v[234:235], v65 offset:9216\n"
    "v_mfma_f32_16x16x32_bf16 v[148:151], v[212:215], a[12:15], v[148:151]\n"
    "ds_read_b64_tr_b16 v[236:237], v66 offset:8192\n"
    "ds_read_b64_tr_b16 v[238:239], v66 offset:9216\n"
    "v_mfma_f32_16x16x32_bf16 v[136:139], v[216:219], a[8:11], v[136:139]\n"
    "buffer_load_dwordx4 v47, s[16:19], s59 offen offset:1024 lds\n"
    "v_add_u32 v4, 1, v4\n"
    "v_mfma_f32_16x16x32_bf16 v[152:155], v[216:219], a[12:15], v[152:155]\n"
    "s_add_u32 m0, s57, 0x14000\n"
    "v_mfma_f32_16x16x32_bf16 v[140:143], v[220:223], a[8:11], v[140:143]\n"
    "s_sub_u32 s58, s58, 0x80\n"
    "s_cselect_b32 s58, 0x480, s58\n"
    "v_mfma_f32_16x16x32_bf16 v[156:159], v[220:223], a[12:15], v[156:159]\n"
    "buffer_load_dwordx4 v38, s[4:7], s58 offen offset:0 lds\n"
    "s_waitcnt lgkmcnt(8)\n"
    "buffer_load_dwordx4 v39, s[4:7], s58 offen offset:1024 lds\n"
    "v_mfma_f32_16x16x32_bf16 v[160:163], v[48:51], a[16:19], v[160:163]\n"
    "ds_read_b128 a[24:27], v43 offset:32768\n"
    "ds_read_b128 a[28:31], v43 offset:34816\n"
    "v_mfma_f32_16x16x32_bf16 v[168:171], a[16:19], a[16:19], v[168:171]\n"
    "ds_read_b64_tr_b16 v[208:209], v26 offset:25600\n"
    "ds_read_b64_tr_b16 v[210:211], v26 offset:26112\n"
    "v_mfma_f32_16x16x32_bf16 v[164:167], v[48:51], a[20:23], v[164:167]\n"
    "ds_read_b64_tr_b16 v[212:213], v27 offset:25600\n"
    "ds_read_b64_tr_b16 v[214:215], v27 offset:26112\n"
    "v_mfma_f32_16x16x32_bf16 v[172:175], a[20:23], a[20:23], v[172:175]\n"
    "ds_read_b64_tr_b16 v[216:217], v28 offset:25600\n"
    "ds_read_b64_tr_b16 v[218:219], v28 offset:26112\n"
    "v_mfma_f32_16x16x32_bf16 v[176:179], v[192:195], v[48:51], v[176:179]\n"
    "ds_read_b64_tr_b16 v[220:221], v29 offset:25600\n"
    "ds_read_b64_tr_b16 v[222:223], v29 offset:26112\n"
    "buffer_load_dwordx4 v40, s[4:7], s58 offen offset:2048 lds\n"
    "v_mfma_f32_16x16x32_bf16 v[128:131], v[192:195], a[16:19], v[128:131]\n"
    "s_waitcnt lgkmcnt(14)\n"
    "buffer_store_dwordx4 v[224:227], v67, s[36:39], 0 offen offset:0 sc1\n"
    "buffer_store_dwordx4 v[228:231], v67, s[36:39], 0 offen offset:1024 sc1\n"
    "v_mfma_f32_16x16x32_bf16 v[144:147], v[192:195], a[20:23], v[144:147]\n"
    "s_waitcnt lgkmcnt(12)\n"
    "buffer_store_dwordx4 v[232:235], v67, s[36:39], s66 offen offset:0 sc1\n"
    "s_waitcnt lgkmcnt(10)\n"
    "buffer_store_dwordx4 v[236:239], v67, s[36:39], s66 offen offset:1024 sc1\n"
    "v_mfma_f32_16x16x32_bf16 v[132:135], v[196:199], a[16:19], v[132:135]\n"
    "v_mfma_f32_16x16x32_bf16 v[148:151], v[196:199], a[20:23], v[148:151]\n"
    "v_mfma_f32_16x16x32_bf16 v[136:139], v[200:203], a[16:19], v[136:139]\n"
    "v_mfma_f32_16x16x32_bf16 v[152:155], v[200:203], a[20:23], v[152:155]\n"
    "v_mfma_f32_16x16x32_bf16 v[140:143], v[204:207], a[16:19], v[140:143]\n"
    "v_mfma_f32_16x16x32_bf16 v[156:159], v[204:207], a[20:23], v[156:159]\n"
    "v_mfma_f32_16x16x32_bf16 v[240:243], v[48:51], v[224:227], 0\n"
    "v_mfma_f32_16x16x32_bf16 v[244:247], v[48:51], v[228:231], 0\n"
    "buffer_load_dwordx4 v41, s[4:7], s58 offen offset:3072 lds\n"
    "s_add_u32 m0, s57, 0x18000\n"
    "v_mfma_f32_16x16x32_bf16 v[240:243], v[48:51], v[232:235], v[240:243]\n"
    "s_sub_u32 s58, s58, 0x80\n"
    "s_cselect_b32 s58, 0x480, s58\n"
    "v_mfma_f32_16x16x32_bf16 v[244:247], v[48:51], v[236:239], v[244:247]\n"
    "s_waitcnt vmcnt(10) lgkmcnt(0)\n"
    "s_barrier\n"
    "v_mfma_f32_16x16x32_bf16 v[160:163], v[48:51], a[24:27], v[160:163]\n"
    "ds_read_b128 a[32:35], v42 offset:49152\n"
    "ds_read_b128 a[36:39], v42 offset:51200\n"
    "v_mfma_f32_16x16x32_bf16 v[168:171], a[24:27], a[24:27], v[168:171]\n"
    "ds_read_b64_tr_b16 v[192:193], v30 offset:0\n"
    "ds_read_b64_tr_b16 v[194:195], v30 offset:512\n"
    "buffer_load_dwordx4 v38, s[4:7], s58 offen offset:0 lds\n"
    "v_mfma_f32_16x16x32_bf16 v[164:167], v[48:51], a[28:31], v[164:167]\n"
    "ds_read_b64_tr_b16 v[196:197], v31 offset:0\n"
    "ds_read_b64_tr_b16 v[198:199], v31 offset:512\n"
    "v_mfma_f32_16x16x32_bf16 v[172:175], a[28:31], a[28:31], v[172:175]\n"
    "ds_read_b64_tr_b16 v[200:201], v32 offset:0\n"
    "ds_read_b64_tr_b16 v[202:203], v32 offset:512\n"
    "v_mfma_f32_16x16x32_bf16 v[176:179], v[208:211], v[48:51], v[176:179]\n"
    "ds_read_b64_tr_b16 v[204:205], v33 offset:0\n"
    "buffer_load_dwordx4 v39, s[4:7], s58 offen offset:1024 lds\n"
    "ds_read_b64_tr_b16 v[206:207], v33 offset:512\n"
    "v_mfma_f32_16x16x32_bf16 v[128:131], v[208:211], a[24:27], v[128:131]\n"
    "s_mov_b32 s90, 0xffff0000\n"
    "s_mov_b32 s91, 0\n"
    "v_mfma_f32_16x16x32_bf16 v[144:147], v[208:211], a[28:31], v[144:147]\n"
    "v_cndmask_b32 v248, v240, v244, s[90:91]\n"
    "v_cvt_pk_bf16_f32 v249, v248, v248\n"
    "v_mfma_f32_16x16x32_bf16 v[132:135], v[212:215], a[24:27], v[132:135]\n"
    "buffer_load_dwordx4 v40, s[4:7], s58 offen offset:2048 lds\n"
    "v_lshlrev_b32 v250, 16, v249\n"
    "v_sub_f32 v248, v248, v250\n"
    "v_mfma_f32_16x16x32_bf16 v[148:151], v[212:215], a[28:31], v[148:151]\n"
    "v_cvt_pk_bf16_f32 v249, v250, v248\n"
    "buffer_store_dword v249, v64, s[36:39], 0 offen offset:0 sc1\n"
    "v_mfma_f32_16x16x32_bf16 v[136:139], v[216:219], a[24:27], v[136:139]\n"
    "buffer_load_dwordx4 v41, s[4:7], s58 offen offset:3072 lds\n"
    "s_add_u32 m0, s56, 0x1c000\n"
    "v_mfma_f32_16x16x32_bf16 v[152:155], v[216:219], a[28:31], v[152:155]\n"
    "s_sub_u32 s59, s59, s97\n"
    "s_cselect_b32 s59, s98, s59\n"
    "v_mfma_f32_16x16x32_bf16 v[140:143], v[220:223], a[24:27], v[140:143]\n"
    "buffer_load_dwordx4 v46, s[16:19], s59 offen offset:0 lds\n"
    "v_mfma_f32_16x16x32_bf16 v[156:159], v[220:223], a[28:31], v[156:159]\n"
    "s_waitcnt lgkmcnt(0)\n"
    "s_lshr_b32 s64, s41, 1\n"
    "buffer_load_dwordx4 v47, s[16:19], s59 offen offset:1024 lds\n"
    "v_mfma_f32_16x16x32_bf16 v[160:163], v[48:51], a[32:35], v[160:163]\n"
    "ds_read_b128 a[40:43], v43 offset:49152\n"
    "s_add_u32 m0, s56, 0x1e000\n"
    "v_mfma_f32_16x16x32_bf16 v[168:171], a[32:35], a[32:35], v[168:171]\n"
    "ds_read_b128 a[44:47], v43 offset:51200\n"
    "s_xor_b32 s64, s64, 0\n"
    "v_mfma_f32_16x16x32_bf16 v[164:167], v[48:51], a[36:39], v[164:167]\n"
    "ds_read_b64_tr_b16 v[208:209], v30 offset:1024\n"
    "s_lshl_b32 s65, s48, 1\n"
    "v_mfma_f32_16x16x32_bf16 v[172:175], a[36:39], a[36:39], v[172:175]\n"
    "s_sub_u32 s59, s59, s97\n"
    "s_cselect_b32 s59, s98, s59\n"
    "ds_read_b64_tr_b16 v[210:211], v30 offset:1536\n"
    "v_mfma_f32_16x16x32_bf16 v[176:179], v[192:195], v[48:51], v[176:179]\n"
    "ds_read_b64_tr_b16 v[212:213], v31 offset:1024\n"
    "buffer_load_dwordx4 v46, s[16:19], s59 offen offset:0 lds\n"
    "buffer_load_dwordx4 v47, s[16:19], s59 offen offset:1024 lds\n"
    "s_add_u32 m0, s57, 0\n"
    "s_sub_u32 s58, s58, 0x80\n"
    "s_cselect_b32 s58, 0x480, s58\n"
    "buffer_load_dwordx4 v38, s[4:7], s58 offen offset:0 lds\n"
    "buffer_load_dwordx4 v39, s[4:7], s58 offen offset:1024 lds\n"
    "buffer_load_dwordx4 v40, s[4:7], s58 offen offset:2048 lds\n"
    "buffer_load_dwordx4 v41, s[4:7], s58 offen offset:3072 lds\n"
    "v_mfma_f32_16x16x32_bf16 v[128:131], v[192:195], a[32:35], v[128:131]\n"
    "ds_read_b64_tr_b16 v[214:215], v31 offset:1536\n"
    "s_add_u32 m0, s57, 0x8000\n"
    "v_mfma_f32_16x16x32_bf16 v[144:147], v[192:195], a[36:39], v[144:147]\n"
    "ds_read_b64_tr_b16 v[216:217], v32 offset:1024\n"
    "s_add_u32 s64, s64, s65\n"
    "v_mfma_f32_16x16x32_bf16 v[132:135], v[196:199], a[32:35], v[132:135]\n"
    "ds_read_b64_tr_b16 v[218:219], v32 offset:1536\n"
    "s_mul_i32 s65, s64, 13\n"
    "v_mfma_f32_16x16x32_bf16 v[148:151], v[196:199], a[36:39], v[148:151]\n"
    "ds_read_b64_tr_b16 v[220:221], v33 offset:1024\n"
    "s_lshr_b32 s65, s65, 6\n"
    "v_mfma_f32_16x16x32_bf16 v[136:139], v[200:203], a[32:35], v[136:139]\n"
    "ds_read_b64_tr_b16 v[222:223], v33 offset:1536\n"
    "s_and_b32 s71, s65, 1\n"
    "v_mfma_f32_16x16x32_bf16 v[152:155], v[200:203], a[36:39], v[152:155]\n"
    "s_sub_u32 s58, s58, 0x80\n"
    "s_cselect_b32 s58, 0x480, s58\n"
    "v_mfma_f32_16x16x32_bf16 v[140:143], v[204:207], a[32:35], v[140:143]\n"
    "s_lshr_b32 s65, s65, 1\n"
    "s_add_u32 s65, s65, 1\n"
    "v_mfma_f32_16x16x32_bf16 v[156:159], v[204:207], a[36:39], v[156:159]\n"
    "s_waitcnt vmcnt(21) lgkmcnt(0)\n"
    "s_barrier\n"
    "v_mfma_f32_16x16x32_bf16 v[160:163], v[48:51], a[40:43], v[160:163]\n"
    "s_waitcnt vmcnt(13)\n"
    "ds_read_b128 a[48:51], v44 offset:16384\n"
    "s_sub_u32 s68, 0, s65\n"
    "v_mfma_f32_16x16x32_bf16 v[168:171], a[40:43], a[40:43], v[168:171]\n"
    "ds_read_b128 a[52:55], v44 offset:18432\n"
    "buffer_load_dwordx4 v38, s[4:7], s58 offen offset:0 lds\n"
    "v_mfma_f32_16x16x32_bf16 v[164:167], v[48:51], a[44:47], v[164:167]\n"
    "ds_read_b64_tr_b16 v[192:193], v30 offset:8192\n"
    "v_mfma_f32_16x16x32_bf16 v[172:175], a[44:47], a[44:47], v[172:175]\n"
    "ds_read_b64_tr_b16 v[194:195], v30 offset:8704\n"
    "buffer_load_dwordx4 v39, s[4:7], s58 offen offset:1024 lds\n"
    "v_mfma_f32_16x16x32_bf16 v[176:179], v[208:211], v[48:51], v[176:179]\n"
    "ds_read_b64_tr_b16 v[196:197], v31 offset:8192\n"
    "v_mfma_f32_16x16x32_bf16 v[128:131], v[208:211], a[40:43], v[128:131]\n"
    "ds_read_b64_tr_b16 v[198:199], v31 offset:8704\n"
    "buffer_load_dwordx4 v40, s[4:7], s58 offen offset:2048 lds\n"
    "v_mfma_f32_16x16x32_bf16 v[144:147], v[208:211], a[44:47], v[144:147]\n"
    "ds_read_b64_tr_b16 v[200:201], v32 offset:8192\n"
    "v_mfma_f32_16x16x32_bf16 v[132:135], v[212:215], a[40:43], v[132:135]\n"
    "ds_read_b64_tr_b16 v[202:203], v32 offset:8704\n"
    "v_mfma_f32_16x16x32_bf16 v[148:151], v[212:215], a[44:47], v[148:151]\n"
    "ds_read_b64_tr_b16 v[204:205], v33 offset:8192\n"
    "buffer_load_dwordx4 v41, s[4:7], s58 offen offset:3072 lds\n"
    "v_mfma_f32_16x16x32_bf16 v[136:139], v[216:219], a[40:43], v[136:139]\n"
    "ds_read_b64_tr_b16 v[206:207], v33 offset:8704\n"
    "s_add_u32 m0, s56, 0x4000\n"
    "v_mfma_f32_16x16x32_bf16 v[152:155], v[216:219], a[44:47], v[152:155]\n"
    "s_sub_u32 s59, s59, s97\n"
    "s_cselect_b32 s59, s98, s59\n"
    "v_mfma_f32_16x16x32_bf16 v[140:143], v[220:223], a[40:43], v[140:143]\n"
    "buffer_load_dwordx4 v46, s[16:19], s59 offen offset:0 lds\n"
    "v_mfma_f32_16x16x32_bf16 v[156:159], v[220:223], a[44:47], v[156:159]\n"
    "s_waitcnt lgkmcnt(0)\n"
    "s_cmp_eq_u32 s71, 0\n"
    "s_cselect_b32 s69, s65, s68\n"
    "v_mfma_f32_16x16x32_bf16 v[160:163], v[48:51], a[48:51], v[160:163]\n"
    "ds_read_b128 a[56:59], v45 offset:16384\n"
    "buffer_load_dwordx4 v47, s[16:19], s59 offen offset:1024 lds\n"
    "v_mfma_f32_16x16x32_bf16 v[168:171], a[48:51], a[48:51], v[168:171]\n"
    "ds_read_b128 a[60:63], v45 offset:18432\n"
    "s_add_u32 m0, s56, 0x6000\n"
    "v_mfma_f32_16x16x32_bf16 v[164:167], v[48:51], a[52:55], v[164:167]\n"
    "ds_read_b64_tr_b16 v[208:209], v30 offset:9216\n"
    "s_mul_i32 s64, s64, 0x20200\n"
    "v_mfma_f32_16x16x32_bf16 v[172:175], a[52:55], a[52:55], v[172:175]\n"
    "ds_read_b64_tr_b16 v[210:211], v30 offset:9728\n"
    "s_lshl_b32 s65, s45, 7\n"
    "s_sub_u32 s59, s59, s97\n"
    "s_cselect_b32 s59, s98, s59\n"
    "v_mfma_f32_16x16x32_bf16 v[176:179], v[192:195], v[48:51], v[176:179]\n"
    "ds_read_b64_tr_b16 v[212:213], v31 offset:9216\n"
    "buffer_load_dwordx4 v46, s[16:19], s59 offen offset:0 lds\n"
    "v_mfma_f32_16x16x32_bf16 v[128:131], v[192:195], a[48:51], v[128:131]\n"
    "ds_read_b64_tr_b16 v[214:215], v31 offset:9728\n"
    "buffer_load_dwordx4 v47, s[16:19], s59 offen offset:1024 lds\n"
    "v_mfma_f32_16x16x32_bf16 v[144:147], v[192:195], a[52:55], v[144:147]\n"
    "ds_read_b64_tr_b16 v[216:217], v32 offset:9216\n"
    "s_add_u32 m0, s57, 0xc000\n"
    "v_mfma_f32_16x16x32_bf16 v[132:135], v[196:199], a[48:51], v[132:135]\n"
    "ds_read_b64_tr_b16 v[218:219], v32 offset:9728\n"
    "s_lshl_b32 s71, s41, 5\n"
    "v_mfma_f32_16x16x32_bf16 v[148:151], v[196:199], a[52:55], v[148:151]\n"
    "ds_read_b64_tr_b16 v[220:221], v33 offset:9216\n"
    "s_sub_u32 s58, s58, 0x80\n"
    "s_cselect_b32 s58, 0x480, s58\n"
    "v_mfma_f32_16x16x32_bf16 v[136:139], v[200:203], a[48:51], v[136:139]\n"
    "ds_read_b64_tr_b16 v[222:223], v33 offset:9728\n"
    "buffer_load_dwordx4 v38, s[4:7], s58 offen offset:0 lds\n"
    "v_mfma_f32_16x16x32_bf16 v[152:155], v[200:203], a[52:55], v[152:155]\n"
    "buffer_load_dwordx4 v39, s[4:7], s58 offen offset:1024 lds\n"
    "s_add_u32 s65, s65, s71\n"
    "v_mfma_f32_16x16x32_bf16 v[140:143], v[204:207], a[48:51], v[140:143]\n"
    "s_add_u32 s72, s65, s69\n"
    "s_lshl_b32 s72, s72, 4\n"
    "v_mfma_f32_16x16x32_bf16 v[156:159], v[204:207], a[52:55], v[156:159]\n"
    "s_waitcnt vmcnt(16) lgkmcnt(0)\n"
    "s_barrier\n"
    "v_mfma_f32_16x16x32_bf16 v[160:163], v[48:51], a[56:59], v[160:163]\n"
    "buffer_store_dwordx2 v[4:5], v9, s[36:39], s83 offen offset:0 sc1\n"
    "buffer_load_dwordx4 v40, s[4:7], s58 offen offset:2048 lds\n"
    "v_mfma_f32_16x16x32_bf16 v[168:171], a[56:59], a[56:59], v[168:171]\n"
    "ds_read_b128 a[64:67], v44 offset:32768\n"
    "buffer_load_dwordx4 v41, s[4:7], s58 offen offset:3072 lds\n"
    "v_mfma_f32_16x16x32_bf16 v[164:167], v[48:51], a[60:63], v[164:167]\n"
    "ds_read_b128 a[68:71], v44 offset:34816\n"
    "s_add_u32 m0, s57, 0x14000\n"
    "v_mfma_f32_16x16x32_bf16 v[172:175], a[60:63], a[60:63], v[172:175]\n"
    "ds_read_b64_tr_b16 v[192:193], v30 offset:49152\n"
    "s_add_u32 s64, s64, s72\n"
    "v_mfma_f32_16x16x32_bf16 v[176:179], v[208:211], v[48:51], v[176:179]\n"
    "ds_read_b64_tr_b16 v[194:195], v30 offset:49664\n"
    "s_add_u32 s64, s64, 0x3eb400\n"
    "v_mfma_f32_16x16x32_bf16 v[128:131], v[208:211], a[56:59], v[128:131]\n"
    "s_sub_u32 s58, s58, 0x80\n"
    "s_cselect_b32 s58, 0x480, s58\n"
    "ds_read_b64_tr_b16 v[196:197], v31 offset:49152\n"
    "v_mfma_f32_16x16x32_bf16 v[144:147], v[208:211], a[60:63], v[144:147]\n"
    "buffer_load_dwordx4 v38, s[4:7], s58 offen offset:0 lds\n"
    "ds_read_b64_tr_b16 v[198:199], v31 offset:49664\n"
    "v_mfma_f32_16x16x32_bf16 v[132:135], v[212:215], a[56:59], v[132:135]\n"
    "ds_read_b64_tr_b16 v[200:201], v32 offset:49152\n"
    "buffer_load_dwordx4 v39, s[4:7], s58 offen offset:1024 lds\n"
    "v_mfma_f32_16x16x32_bf16 v[148:151], v[212:215], a[60:63], v[148:151]\n"
    "ds_read_b64_tr_b16 v[202:203], v32 offset:49664\n"
    "v_mfma_f32_16x16x32_bf16 v[136:139], v[216:219], a[56:59], v[136:139]\n"
    "ds_read_b64_tr_b16 v[204:205], v33 offset:49152\n"
    "buffer_load_dwordx4 v40, s[4:7], s58 offen offset:2048 lds\n"
    "v_mfma_f32_16x16x32_bf16 v[152:155], v[216:219], a[60:63], v[152:155]\n"
    "ds_read_b64_tr_b16 v[206:207], v33 offset:49664\n"
    "v_mfma_f32_16x16x32_bf16 v[140:143], v[220:223], a[56:59], v[140:143]\n"
    "buffer_load_dwordx4 v41, s[4:7], s58 offen offset:3072 lds\n"
    "s_add_u32 m0, s56, 0x10000\n"
    "v_mfma_f32_16x16x32_bf16 v[156:159], v[220:223], a[60:63], v[156:159]\n"
    "s_waitcnt lgkmcnt(0)\n"
    "s_sub_u32 s59, s59, s97\n"
    "s_cselect_b32 s59, s98, s59\n"
    "v_mfma_f32_16x16x32_bf16 v[160:163], v[48:51], a[64:67], v[160:163]\n"
    "ds_read_b128 a[72:75], v45 offset:32768\n"
    "buffer_load_dwordx4 v46, s[16:19], s59 offen offset:0 lds\n"
    "v_mfma_f32_16x16x32_bf16 v[168:171], a[64:67], a[64:67], v[168:171]\n"
    "ds_read_b128 a[76:79], v45 offset:34816\n"
    "v_mfma_f32_16x16x32_bf16 v[164:167], v[48:51], a[68:71], v[164:167]\n"
    "ds_read_b64_tr_b16 v[208:209], v30 offset:50176\n"
    "v_mfma_f32_16x16x32_bf16 v[172:175], a[68:71], a[68:71], v[172:175]\n"
    "ds_read_b64_tr_b16 v[210:211], v30 offset:50688\n"
    "buffer_load_dwordx4 v47, s[16:19], s59 offen offset:1024 lds\n"
    "v_mfma_f32_16x16x32_bf16 v[176:179], v[192:195], v[48:51], v[176:179]\n"
    "ds_read_b64_tr_b16 v[212:213], v31 offset:50176\n"
    "s_add_u32 m0, s56, 0x12000\n"
    "v_mfma_f32_16x16x32_bf16 v[128:131], v[192:195], a[64:67], v[128:131]\n"
    "ds_read_b64_tr_b16 v[214:215], v31 offset:50688\n"
    "v_mfma_f32_16x16x32_bf16 v[144:147], v[192:195], a[68:71], v[144:147]\n"
    "ds_read_b64_tr_b16 v[216:217], v32 offset:50176\n"
    "v_mfma_f32_16x16x32_bf16 v[132:135], v[196:199], a[64:67], v[132:135]\n"
    "s_sub_u32 s59, s59, s97\n"
    "s_cselect_b32 s59, s98, s59\n"
    "ds_read_b64_tr_b16 v[218:219], v32 offset:50688\n"
    "v_mfma_f32_16x16x32_bf16 v[148:151], v[196:199], a[68:71], v[148:151]\n"
    "buffer_load_dwordx4 v46, s[16:19], s59 offen offset:0 lds\n"
    "ds_read_b64_tr_b16 v[220:221], v33 offset:50176\n"
    "v_mfma_f32_16x16x32_bf16 v[136:139], v[200:203], a[64:67], v[136:139]\n"
    "ds_read_b64_tr_b16 v[222:223], v33 offset:50688\n"
    "buffer_load_dwordx4 v47, s[16:19], s59 offen offset:1024 lds\n"
    "v_mfma_f32_16x16x32_bf16 v[152:155], v[200:203], a[68:71], v[152:155]\n"
    "s_cmp_lt_u32 s42, 20\n"
    "s_cselect_b32 s64, s64, 0x40000000\n"
    "v_mfma_f32_16x16x32_bf16 v[140:143], v[204:207], a[64:67], v[140:143]\n"
    "v_lshrrev_b32 v58, 5, v1\n"
    "v_mul_u32_u24 v58, 0x8080, v58\n"
    "v_mfma_f32_16x16x32_bf16 v[156:159], v[204:207], a[68:71], v[156:159]\n"
    "s_waitcnt vmcnt(25) lgkmcnt(0)\n"
    "s_barrier\n"
    "s_add_u32 m0, s57, 0x18000\n"
    "s_sub_u32 s58, s58, 0x80\n"
    "s_cselect_b32 s58, 0x480, s58\n"
    "buffer_load_dwordx4 v38, s[4:7], s58 offen offset:0 lds\n"
    "v_mfma_f32_16x16x32_bf16 v[160:163], v[48:51], a[72:75], v[160:163]\n"
    "s_waitcnt vmcnt(22)\n"
    "ds_read_b128 a[80:83], v42 offset:0\n"
    "buffer_load_dwordx4 v39, s[4:7], s58 offen offset:1024 lds\n"
    "v_mfma_f32_16x16x32_bf16 v[168:171], a[72:75], a[72:75], v[168:171]\n"
    "ds_read_b128 a[84:87], v42 offset:2048\n"
    "v_and_b32 v59, 31, v1\n"
    "v_mfma_f32_16x16x32_bf16 v[164:167], v[48:51], a[76:79], v[164:167]\n"
    "ds_read_b64_tr_b16 v[192:193], v30 offset:57344\n"
    "buffer_load_dwordx4 v40, s[4:7], s58 offen offset:2048 lds\n"
    "v_mfma_f32_16x16x32_bf16 v[172:175], a[76:79], a[76:79], v[172:175]\n"
    "ds_read_b64_tr_b16 v[194:195], v30 offset:57856\n"
    "v_lshl_add_u32 v58, v59, 4, v58\n"
    "v_mfma_f32_16x16x32_bf16 v[176:179], v[208:211], v[48:51], v[176:179]\n"
    "ds_read_b64_tr_b16 v[196:197], v31 offset:57344\n"
    "buffer_load_dwordx4 v41, s[4:7], s58 offen offset:3072 lds\n"
    "v_mfma_f32_16x16x32_bf16 v[128:131], v[208:211], a[72:75], v[128:131]\n"
    "ds_read_b64_tr_b16 v[198:199], v31 offset:57856\n"
    "v_add_u32 v68, s64, v58\n"
    "v_mfma_f32_16x16x32_bf16 v[144:147], v[208:211], a[76:79], v[144:147]\n"
    "ds_read_b64_tr_b16 v[200:201], v32 offset:57344\n"
    "s_lshr_b32 s64, s41, 1\n"
    "v_mfma_f32_16x16x32_bf16 v[132:135], v[212:215], a[72:75], v[132:135]\n"
    "ds_read_b64_tr_b16 v[202:203], v32 offset:57856\n"
    "s_xor_b32 s64, s64, 1\n"
    "v_mfma_f32_16x16x32_bf16 v[148:151], v[212:215], a[76:79], v[148:151]\n"
    "ds_read_b64_tr_b16 v[204:205], v33 offset:57344\n"
    "s_lshl_b32 s65, s48, 1\n"
    "v_mfma_f32_16x16x32_bf16 v[136:139], v[216:219], a[72:75], v[136:139]\n"
    "ds_read_b64_tr_b16 v[206:207], v33 offset:57856\n"
    "s_add_u32 s64, s64, s65\n"
    "v_mfma_f32_16x16x32_bf16 v[152:155], v[216:219], a[76:79], v[152:155]\n"
    "s_mul_i32 s65, s64, 13\n"
    "s_lshr_b32 s65, s65, 6\n"
    "v_mfma_f32_16x16x32_bf16 v[140:143], v[220:223], a[72:75], v[140:143]\n"
    "s_and_b32 s71, s65, 1\n"
    "s_lshr_b32 s65, s65, 1\n"
    "v_mfma_f32_16x16x32_bf16 v[156:159], v[220:223], a[76:79], v[156:159]\n"
    "s_waitcnt lgkmcnt(0)\n"
    "s_add_u32 s65, s65, 1\n"
    "s_sub_u32 s68, 0, s65\n"
    "v_mfma_f32_16x16x32_bf16 v[160:163], v[48:51], a[80:83], v[160:163]\n"
    "ds_read_b128 a[88:91], v43 offset:0\n"
    "v_mfma_f32_16x16x32_bf16 v[168:171], a[80:83], a[80:83], v[168:171]\n"
    "ds_read_b128 a[92:95], v43 offset:2048\n"
    "v_mfma_f32_16x16x32_bf16 v[164:167], v[48:51], a[84:87], v[164:167]\n"
    "ds_read_b64_tr_b16 v[208:209], v30 offset:58368\n"
    "v_mfma_f32_16x16x32_bf16 v[172:175], a[84:87], a[84:87], v[172:175]\n"
    "ds_read_b64_tr_b16 v[210:211], v30 offset:58880\n"
    "v_mfma_f32_16x16x32_bf16 v[176:179], v[192:195], v[48:51], v[176:179]\n"
    "ds_read_b64_tr_b16 v[212:213], v31 offset:58368\n"
    "v_mfma_f32_16x16x32_bf16 v[128:131], v[192:195], a[80:83], v[128:131]\n"
    "ds_read_b64_tr_b16 v[214:215], v31 offset:58880\n"
    "v_mfma_f32_16x16x32_bf16 v[144:147], v[192:195], a[84:87], v[144:147]\n"
    "ds_read_b64_tr_b16 v[216:217], v32 offset:58368\n"
    "v_mfma_f32_16x16x32_bf16 v[132:135], v[196:199], a[80:83], v[132:135]\n"
    "ds_read_b64_tr_b16 v[218:219], v32 offset:58880\n"
    "v_mfma_f32_16x16x32_bf16 v[148:151], v[196:199], a[84:87], v[148:151]\n"
    "ds_read_b64_tr_b16 v[220:221], v33 offset:58368\n"
    "v_mfma_f32_16x16x32_bf16 v[136:139], v[200:203], a[80:83], v[136:139]\n"
    "ds_read_b64_tr_b16 v[222:223], v33 offset:58880\n"
    "v_mfma_f32_16x16x32_bf16 v[152:155], v[200:203], a[84:87], v[152:155]\n"
    "s_cmp_eq_u32 s71, 0\n"
    "s_cselect_b32 s70, s65, s68\n"
    "v_mfma_f32_16x16x32_bf16 v[140:143], v[204:207], a[80:83], v[140:143]\n"
    "s_mul_i32 s64, s64, 0x20200\n"
    "s_lshl_b32 s65, s45, 7\n"
    "v_mfma_f32_16x16x32_bf16 v[156:159], v[204:207], a[84:87], v[156:159]\n"
    "s_waitcnt vmcnt(19) lgkmcnt(0)\n"
    "s_barrier\n"
    "s_add_u32 m0, s56, 0\n"
    "s_mul_i32 s61, s75, 0x14000\n"
    "buffer_load_dwordx4 v20, s[20:23], s61 offen offset:0 lds\n"
    "v_mfma_f32_16x16x32_bf16 v[160:163], v[48:51], a[88:91], v[160:163]\n"
    "ds_read_b128 a[96:99], v42 offset:32768\n"
    "s_lshl_b32 s71, s41, 5\n"
    "v_mfma_f32_16x16x32_bf16 v[168:171], a[88:91], a[88:91], v[168:171]\n"
    "ds_read_b128 a[100:103], v42 offset:34816\n"
    "buffer_load_dwordx4 v21, s[20:23], s61 offen offset:1024 lds\n"
    "v_mfma_f32_16x16x32_bf16 v[164:167], v[48:51], a[92:95], v[164:167]\n"
    "ds_read_b64_tr_b16 v[192:193], v26 offset:16384\n"
    "s_add_u32 m0, s56, 0x2000\n"
    "v_mfma_f32_16x16x32_bf16 v[172:175], a[92:95], a[92:95], v[172:175]\n"
    "ds_read_b64_tr_b16 v[194:195], v26 offset:16896\n"
    "s_add_u32 s62, s61, 0x64000\n"
    "v_mfma_f32_16x16x32_bf16 v[176:179], v[208:211], v[48:51], v[176:179]\n"
    "ds_read_b64_tr_b16 v[196:197], v27 offset:16384\n"
    "s_add_u32 s65, s65, s71\n"
    "v_mfma_f32_16x16x32_bf16 v[128:131], v[208:211], a[88:91], v[128:131]\n"
    "ds_read_b64_tr_b16 v[198:199], v27 offset:16896\n"
    "s_add_u32 s72, s65, s70\n"
    "v_mfma_f32_16x16x32_bf16 v[144:147], v[208:211], a[92:95], v[144:147]\n"
    "ds_read_b64_tr_b16 v[200:201], v28 offset:16384\n"
    "s_cmp_ge_u32 s62, 0xc8000\n"
    "s_cselect_b32 s0, 0xc8000, 0\n"
    "s_sub_u32 s62, s62, s0\n"
    "v_mfma_f32_16x16x32_bf16 v[132:135], v[212:215], a[88:91], v[132:135]\n"
    "ds_read_b64_tr_b16 v[202:203], v28 offset:16896\n"
    "buffer_load_dwordx4 v20, s[20:23], s62 offen offset:0 lds\n"
    "v_mfma_f32_16x16x32_bf16 v[148:151], v[212:215], a[92:95], v[148:151]\n"
    "ds_read_b64_tr_b16 v[204:205], v29 offset:16384\n"
    "s_lshl_b32 s72, s72, 4\n"
    "v_mfma_f32_16x16x32_bf16 v[136:139], v[216:219], a[88:91], v[136:139]\n"
    "ds_read_b64_tr_b16 v[206:207], v29 offset:16896\n"
    "buffer_load_dwordx4 v21, s[20:23], s62 offen offset:1024 lds\n"
    "v_mfma_f32_16x16x32_bf16 v[152:155], v[216:219], a[92:95], v[152:155]\n"
    "s_add_u32 m0, s56, 0x1c000\n"
    "s_add_u32 s64, s64, s72\n"
    "v_mfma_f32_16x16x32_bf16 v[140:143], v[220:223], a[88:91], v[140:143]\n"
    "s_sub_u32 s61, s61, 0x14000\n"
    "s_cselect_b32 s61, 0xb4000, s61\n"
    "v_mfma_f32_16x16x32_bf16 v[156:159], v[220:223], a[92:95], v[156:159]\n"
    "s_waitcnt lgkmcnt(0)\n"
    "s_add_u32 s64, s64, 0x3eb400\n"
    "v_mfma_f32_16x16x32_bf16 v[160:163], v[48:51], a[96:99], v[160:163]\n"
    "ds_read_b128 a[104:107], v43 offset:32768\n"
    "buffer_load_dwordx4 v20, s[20:23], s61 offen offset:0 lds\n"
    "v_mfma_f32_16x16x32_bf16 v[168:171], a[96:99], a[96:99], v[168:171]\n"
    "ds_read_b128 a[108:111], v43 offset:34816\n"
    "v_mfma_f32_16x16x32_bf16 v[164:167], v[48:51], a[100:103], v[164:167]\n"
    "ds_read_b64_tr_b16 v[208:209], v26 offset:17408\n"
    "v_mfma_f32_16x16x32_bf16 v[172:175], a[100:103], a[100:103], v[172:175]\n"
    "ds_read_b64_tr_b16 v[210:211], v26 offset:17920\n"
    "v_mfma_f32_16x16x32_bf16 v[176:179], v[192:195], v[48:51], v[176:179]\n"
    "ds_read_b64_tr_b16 v[212:213], v27 offset:17408\n"
    "buffer_load_dwordx4 v21, s[20:23], s61 offen offset:1024 lds\n"
    "v_mfma_f32_16x16x32_bf16 v[128:131], v[192:195], a[96:99], v[128:131]\n"
    "ds_read_b64_tr_b16 v[214:215], v27 offset:17920\n"
    "s_add_u32 m0, s56, 0x1e000\n"
    "v_mfma_f32_16x16x32_bf16 v[144:147], v[192:195], a[100:103], v[144:147]\n"
    "ds_read_b64_tr_b16 v[216:217], v28 offset:17408\n"
    "v_mfma_f32_16x16x32_bf16 v[132:135], v[196:199], a[96:99], v[132:135]\n"
    "ds_read_b64_tr_b16 v[218:219], v28 offset:17920\n"
    "v_mfma_f32_16x16x32_bf16 v[148:151], v[196:199], a[100:103], v[148:151]\n"
    "ds_read_b64_tr_b16 v[220:221], v29 offset:17408\n"
    "v_mfma_f32_16x16x32_bf16 v[136:139], v[200:203], a[96:99], v[136:139]\n"
    "ds_read_b64_tr_b16 v[222:223], v29 offset:17920\n"
    "s_sub_u32 s62, s62, 0x14000\n"
    "s_cselect_b32 s62, 0xb4000, s62\n"
    "v_mfma_f32_16x16x32_bf16 v[152:155], v[200:203], a[100:103], v[152:155]\n"
    "buffer_load_dwordx4 v20, s[20:23], s62 offen offset:0 lds\n"
    "v_mfma_f32_16x16x32_bf16 v[140:143], v[204:207], a[96:99], v[140:143]\n"
    "s_cmp_lt_u32 s42, 20\n"
    "s_cselect_b32 s64, s64, 0x40000000\n"
    "v_mfma_f32_16x16x32_bf16 v[156:159], v[204:207], a[100:103], v[156:159]\n"
    "s_waitcnt vmcnt(24) lgkmcnt(0)\n"
    "s_barrier\n"
    "v_mfma_f32_16x16x32_bf16 v[160:163], v[48:51], a[104:107], v[160:163]\n"
    "s_waitcnt vmcnt(19)\n"
    "ds_read_b128 a[112:115], v42 offset:49152\n"
    "buffer_load_dwordx4 v21, s[20:23], s62 offen offset:1024 lds\n"
    "v_mfma_f32_16x16x32_bf16 v[168:171], a[104:107], a[104:107], v[168:171]\n"
    "ds_read_b128 a[116:119], v42 offset:51200\n"
    "s_add_u32 m0, s56, 0x8000\n"
    "v_mfma_f32_16x16x32_bf16 v[164:167], v[48:51], a[108:111], v[164:167]\n"
    "ds_read_b64_tr_b16 v[192:193], v26 offset:24576\n"
    "v_add_u32 v69, s64, v58\n"
    "v_mfma_f32_16x16x32_bf16 v[172:175], a[108:111], a[108:111], v[172:175]\n"
    "ds_read_b64_tr_b16 v[194:195], v26 offset:25088\n"
    "s_sub_u32 s61, s61, 0x14000\n"
    "s_cselect_b32 s61, 0xb4000, s61\n"
    "v_mfma_f32_16x16x32_bf16 v[176:179], v[208:211], v[48:51], v[176:179]\n"
    "ds_read_b64_tr_b16 v[196:197], v27 offset:24576\n"
    "buffer_load_dwordx4 v20, s[20:23], s61 offen offset:0 lds\n"
    "v_mfma_f32_16x16x32_bf16 v[128:131], v[208:211], a[104:107], v[128:131]\n"
    "ds_read_b64_tr_b16 v[198:199], v27 offset:25088\n"
    "s_and_b32 s68, s41, 1\n"
    "v_mfma_f32_16x16x32_bf16 v[144:147], v[208:211], a[108:111], v[144:147]\n"
    "ds_read_b64_tr_b16 v[200:201], v28 offset:24576\n"
    "buffer_load_dwordx4 v21, s[20:23], s61 offen offset:1024 lds\n"
    "v_mfma_f32_16x16x32_bf16 v[132:135], v[212:215], a[104:107], v[132:135]\n"
    "ds_read_b64_tr_b16 v[202:203], v28 offset:25088\n"
    "s_add_u32 m0, s56, 0xa000\n"
    "v_mfma_f32_16x16x32_bf16 v[148:151], v[212:215], a[108:111], v[148:151]\n"
    "ds_read_b64_tr_b16 v[204:205], v29 offset:24576\n"
    "s_mul_i32 s68, s68, 0x10100\n"
    "v_mfma_f32_16x16x32_bf16 v[136:139], v[216:219], a[104:107], v[136:139]\n"
    "ds_read_b64_tr_b16 v[206:207], v29 offset:25088\n"
    "s_sub_u32 s73, 0x10100, s68\n"
    "v_mfma_f32_16x16x32_bf16 v[152:155], v[216:219], a[108:111], v[152:155]\n"
    "buffer_load_dwordx2 v[6:7], v10, s[36:39], 0 offen offset:0 sc1\n"
    "v_add_u32 v59, 1, v59\n"
    "v_mfma_f32_16x16x32_bf16 v[140:143], v[220:223], a[104:107], v[140:143]\n"
    "s_sub_u32 s62, s62, 0x14000\n"
    "s_cselect_b32 s62, 0xb4000, s62\n"
    "v_mfma_f32_16x16x32_bf16 v[156:159], v[220:223], a[108:111], v[156:159]\n"
    "s_waitcnt lgkmcnt(0)\n"
    "buffer_load_dwordx4 v20, s[20:23], s62 offen offset:0 lds\n"
    "v_and_b32 v59, 30, v59\n"
    "v_mfma_f32_16x16x32_bf16 v[160:163], v[48:51], a[112:115], v[160:163]\n"
    "ds_read_b128 a[120:123], v43 offset:49152\n"
    "v_mov_b32 v60, 0x40000000\n"
    "v_mfma_f32_16x16x32_bf16 v[168:171], a[112:115], a[112:115], v[168:171]\n"
    "ds_read_b128 a[124:127], v43 offset:51200\n"
    "v_mfma_f32_16x16x32_bf16 v[164:167], v[48:51], a[116:119], v[164:167]\n"
    "ds_read_b64_tr_b16 v[208:209], v26 offset:25600\n"
    "v_mfma_f32_16x16x32_bf16 v[172:175], a[116:119], a[116:119], v[172:175]\n"
    "ds_read_b64_tr_b16 v[210:211], v26 offset:26112\n"
    "buffer_load_dwordx4 v21, s[20:23], s62 offen offset:1024 lds\n"
    "v_mfma_f32_16x16x32_bf16 v[176:179], v[192:195], v[48:51], v[176:179]\n"
    "ds_read_b64_tr_b16 v[212:213], v27 offset:25600\n"
    "v_mfma_f32_16x16x32_bf16 v[128:131], v[192:195], a[112:115], v[128:131]\n"
    "ds_read_b64_tr_b16 v[214:215], v27 offset:26112\n"
    "v_mfma_f32_16x16x32_bf16 v[144:147], v[192:195], a[116:119], v[144:147]\n"
    "ds_read_b64_tr_b16 v[216:217], v28 offset:25600\n"
    "v_mfma_f32_16x16x32_bf16 v[132:135], v[196:199], a[112:115], v[132:135]\n"
    "ds_read_b64_tr_b16 v[218:219], v28 offset:26112\n"
    "v_mfma_f32_16x16x32_bf16 v[148:151], v[196:199], a[116:119], v[148:151]\n"
    "ds_read_b64_tr_b16 v[220:221], v29 offset:25600\n"
    "v_mfma_f32_16x16x32_bf16 v[136:139], v[200:203], a[112:115], v[136:139]\n"
    "ds_read_b64_tr_b16 v[222:223], v29 offset:26112\n"
    "v_mfma_f32_16x16x32_bf16 v[152:155], v[200:203], a[116:119], v[152:155]\n"
    "v_cmp_eq_u32 vcc, 0, v59\n"
    "s_nop 1\n"
    "v_cndmask_b32 v60, v60, 0, vcc\n"
    "v_mfma_f32_16x16x32_bf16 v[140:143], v[204:207], a[112:115], v[140:143]\n"
    "v_or_b32 v59, v60, v68\n"
    "buffer_load_dword v92, v59, s[36:39], s68 offen offset:0\n"
    "v_mfma_f32_16x16x32_bf16 v[156:159], v[204:207], a[116:119], v[156:159]\n"
    "s_waitcnt vmcnt(20) lgkmcnt(0)\n"
    "s_barrier\n"
    "v_mfma_f32_16x16x32_bf16 v[160:163], v[48:51], a[120:123], v[160:163]\n"
    "ds_read_b128 a[128:131], v44 offset:16384\n"
    "buffer_load_dword v93, v59, s[36:39], s73 offen offset:0\n"
    "v_mfma_f32_16x16x32_bf16 v[168:171], a[120:123], a[120:123], v[168:171]\n"
    "ds_read_b128 a[132:135], v44 offset:18432\n"
    "v_or_b32 v59, v60, v69\n"
    "v_mfma_f32_16x16x32_bf16 v[164:167], v[48:51], a[124:127], v[164:167]\n"
    "ds_read_b64_tr_b16 v[192:193], v30 offset:0\n"
    "buffer_load_dword v94, v59, s[36:39], s68 offen offset:0\n"
    "v_mfma_f32_16x16x32_bf16 v[172:175], a[124:127], a[124:127], v[172:175]\n"
    "ds_read_b64_tr_b16 v[194:195], v30 offset:512\n"
    "buffer_load_dword v95, v59, s[36:39], s73 offen offset:0\n"
    "v_mfma_f32_16x16x32_bf16 v[176:179], v[208:211], v[48:51], v[176:179]\n"
    "ds_read_b64_tr_b16 v[196:197], v31 offset:0\n"
    "s_lshl_b32 s64, s41, 6\n"
    "v_mfma_f32_16x16x32_bf16 v[128:131], v[208:211], a[120:123], v[128:131]\n"
    "ds_read_b64_tr_b16 v[198:199], v31 offset:512\n"
    "s_add_u32 s64, s64, 0x22000\n"
    "v_mfma_f32_16x16x32_bf16 v[144:147], v[208:211], a[124:127], v[144:147]\n"
    "ds_read_b64_tr_b16 v[200:201], v32 offset:0\n"
    "v_lshl_add_u32 v70, v2, 4, s64\n"
    "v_mfma_f32_16x16x32_bf16 v[132:135], v[212:215], a[120:123], v[132:135]\n"
    "ds_read_b64_tr_b16 v[202:203], v32 offset:512\n"
    "v_xor_b32 v71, 64, v70\n"
    "v_mfma_f32_16x16x32_bf16 v[148:151], v[212:215], a[124:127], v[148:151]\n"
    "ds_read_b64_tr_b16 v[204:205], v33 offset:0\n"
    "v_xor_b32 v72, 0x80, v70\n"
    "v_mfma_f32_16x16x32_bf16 v[136:139], v[216:219], a[120:123], v[136:139]\n"
    "ds_read_b64_tr_b16 v[206:207], v33 offset:512\n"
    "v_xor_b32 v73, 0xc0, v70\n"
    "v_mfma_f32_16x16x32_bf16 v[152:155], v[216:219], a[124:127], v[152:155]\n"
    "s_lshl_b32 s64, s65, 3\n"
    "s_add_u32 s64, s64, 0x41000\n"
    "v_mfma_f32_16x16x32_bf16 v[140:143], v[220:223], a[120:123], v[140:143]\n"
    "s_cmp_eq_u32 s48, 0\n"
    "s_cselect_b32 s64, s64, 0x40000000\n"
    "v_mfma_f32_16x16x32_bf16 v[156:159], v[220:223], a[124:127], v[156:159]\n"
    "s_waitcnt lgkmcnt(0)\n"
    "s_cmp_lt_u32 s42, 20\n"
    "s_cselect_b32 s64, s64, 0x40000000\n"
    "v_mfma_f32_16x16x32_bf16 v[160:163], v[48:51], a[128:131], v[160:163]\n"
    "ds_read_b128 a[136:139], v45 offset:16384\n"
    "v_lshl_add_u32 v58, v1, 3, s64\n"
    "v_mfma_f32_16x16x32_bf16 v[168:171], a[128:131], a[128:131], v[168:171]\n"
    "ds_read_b128 a[140:143], v45 offset:18432\n"
    "v_mov_b32 v61, 0x40000000\n"
    "v_mfma_f32_16x16x32_bf16 v[164:167], v[48:51], a[132:135], v[164:167]\n"
    "ds_read_b64_tr_b16 v[208:209], v30 offset:1024\n"
    "v_mfma_f32_16x16x32_bf16 v[172:175], a[132:135], a[132:135], v[172:175]\n"
    "ds_read_b64_tr_b16 v[210:211], v30 offset:1536\n"
    "v_mfma_f32_16x16x32_bf16 v[176:179], v[192:195], v[48:51], v[176:179]\n"
    "ds_read_b64_tr_b16 v[212:213], v31 offset:1024\n"
    "v_mfma_f32_16x16x32_bf16 v[128:131], v[192:195], a[128:131], v[128:131]\n"
    "ds_read_b64_tr_b16 v[214:215], v31 offset:1536\n"
    "v_mfma_f32_16x16x32_bf16 v[144:147], v[192:195], a[132:135], v[144:147]\n"
    "ds_read_b64_tr_b16 v[216:217], v32 offset:1024\n"
    "v_mfma_f32_16x16x32_bf16 v[132:135], v[196:199], a[128:131], v[132:135]\n"
    "ds_read_b64_tr_b16 v[218:219], v32 offset:1536\n"
    "v_mfma_f32_16x16x32_bf16 v[148:151], v[196:199], a[132:135], v[148:151]\n"
    "ds_read_b64_tr_b16 v[220:221], v33 offset:1024\n"
    "v_mfma_f32_16x16x32_bf16 v[136:139], v[200:203], a[128:131], v[136:139]\n"
    "ds_read_b64_tr_b16 v[222:223], v33 offset:1536\n"
    "v_mfma_f32_16x16x32_bf16 v[152:155], v[200:203], a[132:135], v[152:155]\n"
    "v_cmp_gt_u32 vcc, 16, v1\n"
    "s_nop 1\n"
    "v_cndmask_b32 v74, v61, v58, vcc\n"
    "v_mfma_f32_16x16x32_bf16 v[140:143], v[204:207], a[128:131], v[140:143]\n"
    "s_lshl_b32 s64, s48, 7\n"
    "s_lshl_b32 s65, s41, 5\n"
    "v_mfma_f32_16x16x32_bf16 v[156:159], v[204:207], a[132:135], v[156:159]\n"
    "s_waitcnt vmcnt(6)\n"
    "v_cmp_eq_u64 vcc, v[6:7], v[4:5]\n"
    "s_or_b32 vcc_lo, vcc_lo, s76\n"
    "s_cmp_eq_u32 vcc_lo, -1\n"
    "s_cbranch_scc0 .Lspin_R\n"
    ".Ldone_R:\n"
    "s_waitcnt lgkmcnt(0)\n"
    "s_barrier\n"
    "s_add_u32 m0, s57, 0xc000\n"
    "s_lshl_b32 s60, s47, 14\n"
    "buffer_load_dwordx4 v18, s[36:39], s60 offen offset:0 sc1 lds\n"
    "v_mfma_f32_16x16x32_bf16 v[160:163], v[48:51], a[136:139], v[160:163]\n"
    "ds_read_b128 a[144:147], v44 offset:32768\n"
    "s_add_u32 s64, s64, s65\n"
    "v_mfma_f32_16x16x32_bf16 v[168:171], a[136:139], a[136:139], v[168:171]\n"
    "ds_read_b128 a[148:151], v44 offset:34816\n"
    "buffer_load_dwordx4 v18, s[36:39], s60 offen offset:1024 sc1 lds\n"
    "v_mfma_f32_16x16x32_bf16 v[164:167], v[48:51], a[140:143], v[164:167]\n"
    "ds_read_b64_tr_b16 v[192:193], v30 offset:8192\n"
    "s_add_u32 s64, s64, 0x45000\n"
    "v_mfma_f32_16x16x32_bf16 v[172:175], a[140:143], a[140:143], v[172:175]\n"
    "ds_read_b64_tr_b16 v[194:195], v30 offset:8704\n"
    "v_mfma_f32_16x16x32_bf16 v[176:179], v[208:211], v[48:51], v[176:179]\n"
    "ds_read_b64_tr_b16 v[196:197], v31 offset:8192\n"
    "v_mfma_f32_16x16x32_bf16 v[128:131], v[208:211], a[136:139], v[128:131]\n"
    "ds_read_b64_tr_b16 v[198:199], v31 offset:8704\n"
    "buffer_load_dwordx4 v19, s[36:39], s60 offen offset:2048 sc1 lds\n"
    "v_mfma_f32_16x16x32_bf16 v[144:147], v[208:211], a[140:143], v[144:147]\n"
    "ds_read_b64_tr_b16 v[200:201], v32 offset:8192\n"
    "v_mfma_f32_16x16x32_bf16 v[132:135], v[212:215], a[136:139], v[132:135]\n"
    "ds_read_b64_tr_b16 v[202:203], v32 offset:8704\n"
    "v_mfma_f32_16x16x32_bf16 v[148:151], v[212:215], a[140:143], v[148:151]\n"
    "ds_read_b64_tr_b16 v[204:205], v33 offset:8192\n"
    "v_mfma_f32_16x16x32_bf16 v[136:139], v[216:219], a[136:139], v[136:139]\n"
    "ds_read_b64_tr_b16 v[206:207], v33 offset:8704\n"
    "buffer_load_dwordx4 v19, s[36:39], s60 offen offset:3072 sc1 lds\n"
    "v_mfma_f32_16x16x32_bf16 v[152:155], v[216:219], a[140:143], v[152:155]\n"
    "s_add_u32 m0, s57, 0x4000\n"
    "v_mfma_f32_16x16x32_bf16 v[140:143], v[220:223], a[136:139], v[140:143]\n"
    "s_sub_u32 s60, s60, 0x4000\n"
    "s_cselect_b32 s60, 0x24000, s60\n"
    "v_mfma_f32_16x16x32_bf16 v[156:159], v[220:223], a[140:143], v[156:159]\n"
    "s_waitcnt lgkmcnt(0)\n"
    "s_cmp_eq_u32 s99, 1\n"
    "s_cselect_b32 s64, s64, 0x40000000\n"
    "v_mfma_f32_16x16x32_bf16 v[160:163], v[48:51], a[144:147], v[160:163]\n"
    "ds_read_b128 a[152:155], v45 offset:32768\n"
    "buffer_load_dwordx4 v18, s[36:39], s60 offen offset:0 sc1 lds\n"
    "v_mfma_f32_16x16x32_bf16 v[168:171], a[144:147], a[144:147], v[168:171]\n"
    "ds_read_b128 a[156:159], v45 offset:34816\n"
    "v_lshl_add_u32 v58, v2, 3, s64\n"
    "v_mfma_f32_16x16x32_bf16 v[164:167], v[48:51], a[148:151], v[164:167]\n"
    "ds_read_b64_tr_b16 v[208:209], v30 offset:9216\n"
    "v_mfma_f32_16x16x32_bf16 v[172:175], a[148:151], a[148:151], v[172:175]\n"
    "ds_read_b64_tr_b16 v[210:211], v30 offset:9728\n"
    "v_mfma_f32_16x16x32_bf16 v[176:179], v[192:195], v[48:51], v[176:179]\n"
    "ds_read_b64_tr_b16 v[212:213], v31 offset:9216\n"
    "buffer_load_dwordx4 v18, s[36:39], s60 offen offset:1024 sc1 lds\n"
    "v_mfma_f32_16x16x32_bf16 v[128:131], v[192:195], a[144:147], v[128:131]\n"
    "ds_read_b64_tr_b16 v[214:215], v31 offset:9728\n"
    "v_mfma_f32_16x16x32_bf16 v[144:147], v[192:195], a[148:151], v[144:147]\n"
    "ds_read_b64_tr_b16 v[216:217], v32 offset:9216\n"
    "v_mfma_f32_16x16x32_bf16 v[132:135], v[196:199], a[144:147], v[132:135]\n"
    "ds_read_b64_tr_b16 v[218:219], v32 offset:9728\n"
    "v_mfma_f32_16x16x32_bf16 v[148:151], v[196:199], a[148:151], v[148:151]\n"
    "ds_read_b64_tr_b16 v[220:221], v33 offset:9216\n"
    "buffer_load_dwordx4 v19, s[36:39], s60 offen offset:2048 sc1 lds\n"
    "v_mfma_f32_16x16x32_bf16 v[136:139], v[200:203], a[144:147], v[136:139]\n"
    "ds_read_b64_tr_b16 v[222:223], v33 offset:9728\n"
    "v_mfma_f32_16x16x32_bf16 v[152:155], v[200:203], a[148:151], v[152:155]\n"
    "v_cmp_eq_u32 vcc, 0, v3\n"
    "s_nop 1\n"
    "v_cndmask_b32 v75, v61, v58, vcc\n"
    "v_mfma_f32_16x16x32_bf16 v[140:143], v[204:207], a[144:147], v[140:143]\n"
    "v_lshrrev_b32 v58, 1, v1\n"
    "v_mul_u32_u24 v58, 0x8080, v58\n"
    "v_mfma_f32_16x16x32_bf16 v[156:159], v[204:207], a[148:151], v[156:159]\n"
    "s_waitcnt lgkmcnt(0)\n"
    "s_barrier\n"
    "v_mfma_f32_16x16x32_bf16 v[160:163], v[48:51], a[152:155], v[160:163]\n"
    "buffer_load_dwordx4 v19, s[36:39], s60 offen offset:3072 sc1 lds\n"
    "s_add_u32 m0, s57, 0x14000\n"
    "v_mfma_f32_16x16x32_bf16 v[168:171], a[152:155], a[152:155], v[168:171]\n"
    "s_sub_u32 s60, s60, 0x4000\n"
    "s_cselect_b32 s60, 0x24000, s60\n"
    "v_mfma_f32_16x16x32_bf16 v[164:167], v[48:51], a[156:159], v[164:167]\n"
    "buffer_load_dwordx4 v18, s[36:39], s60 offen offset:0 sc1 lds\n"
    "v_and_b32 v59, 1, v1\n"
    "v_mfma_f32_16x16x32_bf16 v[172:175], a[156:159], a[156:159], v[172:175]\n"
    "v_mov_b32 v60, s70\n"
    "v_mov_b32 v76, s69\n"
    "v_mfma_f32_16x16x32_bf16 v[176:179], v[208:211], v[48:51], v[176:179]\n"
    "v_cmp_gt_u32 vcc, 8, v1\n"
    "s_nop 1\n"
    "v_cndmask_b32 v60, v60, v76, vcc\n"
    "v_mfma_f32_16x16x32_bf16 v[128:131], v[208:211], a[152:155], v[128:131]\n"
    "v_cmp_lt_i32 s[90:91], v59, v60\n"
    "v_mfma_f32_16x16x32_bf16 v[144:147], v[208:211], a[156:159], v[144:147]\n"
    "buffer_load_dwordx4 v18, s[36:39], s60 offen offset:1024 sc1 lds\n"
    "v_mfma_f32_16x16x32_bf16 v[132:135], v[212:215], a[152:155], v[132:135]\n"
    "s_cmp_eq_u32 s45, 0\n"
    "s_cselect_b64 s[90:91], s[90:91], 0\n"
    "v_mfma_f32_16x16x32_bf16 v[148:151], v[212:215], a[156:159], v[148:151]\n"
    "v_sub_u32 v60, 0, v60\n"
    "v_mfma_f32_16x16x32_bf16 v[136:139], v[216:219], a[152:155], v[136:139]\n"
    "v_cmp_lt_i32 vcc, v59, v60\n"
    "s_cmp_eq_u32 s45, 15\n"
    "s_cselect_b64 vcc, vcc, 0\n"
    "v_sub_u32 v60, 0x7ff, v59\n"
    "v_cndmask_b32 v59, v59, v60, vcc\n"
    "s_or_b64 s[90:91], s[90:91], vcc\n"
    "v_mfma_f32_16x16x32_bf16 v[152:155], v[216:219], a[156:159], v[152:155]\n"
    "buffer_load_dwordx4 v19, s[36:39], s60 offen offset:2048 sc1 lds\n"
    "s_and_b64 s[90:91], s[90:91], 0xffff\n"
    "v_mfma_f32_16x16x32_bf16 v[140:143], v[220:223], a[152:155], v[140:143]\n"
    "s_mul_i32 s64, s48, 0x40400\n"
    "s_add_u32 s64, s64, 0x3eb400\n"
    "v_mfma_f32_16x16x32_bf16 v[156:159], v[220:223], a[156:159], v[156:159]\n"
    "ds_write_b128 v70, v[176:179] offset:0\n"
    "s_cmp_eq_u32 s41, 0\n"
    "s_cselect_b32 s64, s64, 0x40000000\n"
    "s_cmp_lt_u32 s42, 20\n"
    "s_cselect_b32 s64, s64, 0x40000000\n"
    "v_lshl_add_u32 v58, v59, 4, v58\n"
    "v_add_u32 v58, s64, v58\n"
    "v_cndmask_b32 v76, v61, v58, s[90:91]\n"
    "s_mov_b32 s90, 0x22222222\n"
    "s_mov_b32 s91, 0x22222222\n"
    "v_cndmask_b32 v168, v168, v169, s[90:91]\n"
    "buffer_load_dwordx4 v19, s[36:39], s60 offen offset:3072 sc1 lds\n"
    "s_add_u32 m0, s57, 0x18000\n"
    "s_sub_u32 s60, s60, 0x4000\n"
    "s_cselect_b32 s60, 0x24000, s60\n"
    "buffer_load_dwordx4 v18, s[36:39], s60 offen offset:0 sc1 lds\n"
    "v_cndmask_b32 v172, v172, v173, s[90:91]\n"
    "s_mov_b32 s90, 0x44444444\n"
    "s_mov_b32 s91, 0x44444444\n"
    "v_cndmask_b32 v168, v168, v170, s[90:91]\n"
    "v_cndmask_b32 v172, v172, v174, s[90:91]\n"
    "s_mov_b32 s90, 0x88888888\n"
    "s_mov_b32 s91, 0x88888888\n"
    "v_cndmask_b32 v168, v168, v171, s[90:91]\n"
    "v_cndmask_b32 v172, v172, v175, s[90:91]\n"
    "s_mov_b32 s90, 0xf0000f\n"
    "s_mov_b32 s91, 0xf0000f00\n"
    "v_cndmask_b32 v168, 0, v168, s[90:91]\n"
    "v_mul_f32 v54, 0xbacccccd, v160\n"
    "v_cndmask_b32 v172, 0, v172, s[90:91]\n"
    "v_mul_f32 v55, 0xbacccccd, v164\n"
    "s_waitcnt lgkmcnt(0)\n"
    "s_barrier\n"
    "ds_read_b128 v[224:227], v70 offset:0\n"
    "ds_read_b128 v[228:231], v71 offset:0\n"
    "ds_read_b128 v[232:235], v72 offset:0\n"
    "ds_read_b128 v[236:239], v73 offset:0\n"
    "v_permlane16_swap_b32 v168, v172\n"
    "v_add_f32 v58, v168, v172\n"
    "buffer_load_dwordx4 v18, s[36:39], s60 offen offset:1024 sc1 lds\n"
    "buffer_load_dwordx4 v19, s[36:39], s60 offen offset:2048 sc1 lds\n"
    "v_mov_b32 v59, v58\n"
    "v_cvt_pk_bf16_f32 v184, v54, v54\n"
    "v_cvt_pk_bf16_f32 v188, v55, v55\n"
    "v_permlane32_swap_b32 v58, v59\n"
    "v_add_f32 v58, v58, v59\n"
    "v_mov_b32 v59, v58\n"
    "v_mov_b32 v185, v184\n"
    "v_mov_b32 v186, v184\n"
    "v_mov_b32 v187, v184\n"
    "v_mov_b32 v189, v188\n"
    "v_mov_b32 v190, v188\n"
    "v_mov_b32 v191, v188\n"
    "v_permlane16_swap_b32 v58, v59\n"
    "v_mul_f32 v60, v54, v54\n"
    "v_mul_f32 v61, v55, v55\n"
    "v_mul_f32 v58, 0x3acccccd, v58\n"
    "v_mul_f32 v59, 0x3acccccd, v59\n"
    "v_sub_f32 v58, v58, v60\n"
    "v_sub_f32 v59, v59, v61\n"
    "v_max_f32 v58, 0, v58\n"
    "v_max_f32 v59, 0, v59\n"
    "v_add_f32 v60, 0x3727c5ac, v58\n"
    "v_add_f32 v61, 0x3727c5ac, v59\n"
    "v_fmamk_f32 v58, v60, 0x3727c5ac, v58\n"
    "v_fmamk_f32 v59, v61, 0x3727c5ac, v59\n"
    "v_rsq_f32 v87, v58\n"
    "v_rsq_f32 v91, v59\n"
    "buffer_load_dwordx4 v19, s[36:39], s60 offen offset:3072 sc1 lds\n"
    "s_add_u32 m0, s57, 0x10000\n"
    "s_sub_u32 s60, s60, 0x4000\n"
    "s_cselect_b32 s60, 0x24000, s60\n"
    "buffer_load_dwordx4 v18, s[36:39], s60 offen offset:0 sc1 lds\n"
    "v_rsq_f32 v52, v60\n"
    "v_rsq_f32 v53, v61\n"
    "v_mul_f32 v85, v87, v87\n"
    "v_mul_f32 v89, v91, v91\n"
    "v_mul_f32 v84, 0xc0135761, v87\n"
    "v_mul_f32 v88, 0xc0135761, v91\n"
    "v_mul_f32 v86, v58, v87\n"
    "v_mul_f32 v90, v59, v91\n"
    "v_mul_f32 v85, 0x3d372713, v85\n"
    "v_mul_f32 v89, 0x3d372713, v89\n"
    "v_mul_f32 v85, v85, v84\n"
    "v_mul_f32 v89, v89, v88\n"
    "s_waitcnt lgkmcnt(3)\n"
    "v_pk_fma_f32 v[128:129], v[224:225], v[54:55], v[128:129] op_sel:[0,0,0] op_sel_hi:[1,0,1]\n"
    "v_pk_fma_f32 v[130:131], v[226:227], v[54:55], v[130:131] op_sel:[0,0,0] op_sel_hi:[1,0,1]\n"
    "s_waitcnt lgkmcnt(2)\n"
    "v_pk_fma_f32 v[132:133], v[228:229], v[54:55], v[132:133] op_sel:[0,0,0] op_sel_hi:[1,0,1]\n"
    "v_pk_fma_f32 v[134:135], v[230:231], v[54:55], v[134:135] op_sel:[0,0,0] op_sel_hi:[1,0,1]\n"
    "s_waitcnt lgkmcnt(1)\n"
    "v_pk_fma_f32 v[136:137], v[232:233], v[54:55], v[136:137] op_sel:[0,0,0] op_sel_hi:[1,0,1]\n"
    "v_pk_fma_f32 v[138:139], v[234:235], v[54:55], v[138:139] op_sel:[0,0,0] op_sel_hi:[1,0,1]\n"
    "s_waitcnt lgkmcnt(0)\n"
    "v_pk_fma_f32 v[140:141], v[236:237], v[54:55], v[140:141] op_sel:[0,0,0] op_sel_hi:[1,0,1]\n"
    "v_pk_fma_f32 v[142:143], v[238:239], v[54:55], v[142:143] op_sel:[0,0,0] op_sel_hi:[1,0,1]\n"
    "v_pk_mul_f32 v[240:241], v[128:129], v[128:129] op_sel:[0,0] op_sel_hi:[1,1]\n"
    "v_pk_mul_f32 v[242:243], v[130:131], v[130:131] op_sel:[0,0] op_sel_hi:[1,1]\n"
    "v_pk_mul_f32 v[244:245], v[132:133], v[132:133] op_sel:[0,0] op_sel_hi:[1,1]\n"
    "v_pk_mul_f32 v[246:247], v[134:135], v[134:135] op_sel:[0,0] op_sel_hi:[1,1]\n"
    "v_pk_mul_f32 v[248:249], v[136:137], v[136:137] op_sel:[0,0] op_sel_hi:[1,1]\n"
    "buffer_load_dwordx4 v18, s[36:39], s60 offen offset:1024 sc1 lds\n"
    "buffer_load_dwordx4 v19, s[36:39], s60 offen offset:2048 sc1 lds\n"
    "v_pk_mul_f32 v[250:251], v[138:139], v[138:139] op_sel:[0,0] op_sel_hi:[1,1]\n"
    "v_pk_mul_f32 v[252:253], v[140:141], v[140:141] op_sel:[0,0] op_sel_hi:[1,1]\n"
    "v_pk_mul_f32 v[254:255], v[142:143], v[142:143] op_sel:[0,0] op_sel_hi:[1,1]\n"
    "v_pk_fma_f32 v[240:241], v[240:241], v[84:85], v[84:85] op_sel:[0,1,0] op_sel_hi:[1,1,0]\n"
    "v_pk_fma_f32 v[242:243], v[242:243], v[84:85], v[84:85] op_sel:[0,1,0] op_sel_hi:[1,1,0]\n"
    "v_pk_fma_f32 v[244:245], v[244:245], v[84:85], v[84:85] op_sel:[0,1,0] op_sel_hi:[1,1,0]\n"
    "v_pk_fma_f32 v[246:247], v[246:247], v[84:85], v[84:85] op_sel:[0,1,0] op_sel_hi:[1,1,0]\n"
    "v_pk_fma_f32 v[248:249], v[248:249], v[84:85], v[84:85] op_sel:[0,1,0] op_sel_hi:[1,1,0]\n"
    "v_pk_fma_f32 v[250:251], v[250:251], v[84:85], v[84:85] op_sel:[0,1,0] op_sel_hi:[1,1,0]\n"
    "v_pk_fma_f32 v[252:253], v[252:253], v[84:85], v[84:85] op_sel:[0,1,0] op_sel_hi:[1,1,0]\n"
    "v_pk_fma_f32 v[254:255], v[254:255], v[84:85], v[84:85] op_sel:[0,1,0] op_sel_hi:[1,1,0]\n"
    "v_pk_mul_f32 v[240:241], v[240:241], v[128:129] op_sel:[0,0] op_sel_hi:[1,1]\n"
    "v_pk_mul_f32 v[242:243], v[242:243], v[130:131] op_sel:[0,0] op_sel_hi:[1,1]\n"
    "v_pk_mul_f32 v[244:245], v[244:245], v[132:133] op_sel:[0,0] op_sel_hi:[1,1]\n"
    "v_pk_mul_f32 v[246:247], v[246:247], v[134:135] op_sel:[0,0] op_sel_hi:[1,1]\n"
    "v_pk_mul_f32 v[248:249], v[248:249], v[136:137] op_sel:[0,0] op_sel_hi:[1,1]\n"
    "v_pk_mul_f32 v[250:251], v[250:251], v[138:139] op_sel:[0,0] op_sel_hi:[1,1]\n"
    "v_pk_mul_f32 v[252:253], v[252:253], v[140:141] op_sel:[0,0] op_sel_hi:[1,1]\n"
    "v_pk_mul_f32 v[254:255], v[254:255], v[142:143] op_sel:[0,0] op_sel_hi:[1,1]\n"
    "v_exp_f32 v240, v240\n"
    "v_exp_f32 v241, v241\n"
    "v_exp_f32 v242, v242\n"
    "v_exp_f32 v243, v243\n"
    "v_exp_f32 v244, v244\n"
    "v_exp_f32 v245, v245\n"
    "buffer_load_dwordx4 v19, s[36:39], s60 offen offset:3072 sc1 lds\n"
    "v_exp_f32 v246, v246\n"
    "v_exp_f32 v247, v247\n"
    "v_exp_f32 v248, v248\n"
    "v_exp_f32 v249, v249\n"
    "v_exp_f32 v250, v250\n"
    "v_exp_f32 v251, v251\n"
    "v_exp_f32 v252, v252\n"
    "v_exp_f32 v253, v253\n"
    "v_exp_f32 v254, v254\n"
    "v_exp_f32 v255, v255\n"
    "v_pk_fma_f32 v[240:241], v[240:241], v[86:87], v[86:87] op_sel:[0,0,0] op_sel_hi:[1,0,0]\n"
    "v_pk_fma_f32 v[242:243], v[242:243], v[86:87], v[86:87] op_sel:[0,0,0] op_sel_hi:[1,0,0]\n"
    "v_pk_fma_f32 v[244:245], v[244:245], v[86:87], v[86:87] op_sel:[0,0,0] op_sel_hi:[1,0,0]\n"
    "v_pk_fma_f32 v[246:247], v[246:247], v[86:87], v[86:87] op_sel:[0,0,0] op_sel_hi:[1,0,0]\n"
    "v_pk_fma_f32 v[248:249], v[248:249], v[86:87], v[86:87] op_sel:[0,0,0] op_sel_hi:[1,0,0]\n"
    "v_pk_fma_f32 v[250:251], v[250:251], v[86:87], v[86:87] op_sel:[0,0,0] op_sel_hi:[1,0,0]\n"
    "v_pk_fma_f32 v[252:253], v[252:253], v[86:87], v[86:87] op_sel:[0,0,0] op_sel_hi:[1,0,0]\n"
    "v_pk_fma_f32 v[254:255], v[254:255], v[86:87], v[86:87] op_sel:[0,0,0] op_sel_hi:[1,0,0]\n"
    "v_rcp_f32 v240, v240\n"
    "v_rcp_f32 v241, v241\n"
    "v_rcp_f32 v242, v242\n"
    "v_rcp_f32 v243, v243\n"
    "v_rcp_f32 v244, v244\n"
    "v_rcp_f32 v245, v245\n"
    "v_rcp_f32 v246, v246\n"
    "v_rcp_f32 v247, v247\n"
    "v_rcp_f32 v248, v248\n"
    "v_rcp_f32 v249, v249\n"
    "v_rcp_f32 v250, v250\n"
    "v_rcp_f32 v251, v251\n"
    "v_rcp_f32 v252, v252\n"
    "v_rcp_f32 v253, v253\n"
    "v_rcp_f32 v254, v254\n"
    "v_rcp_f32 v255, v255\n"
    "v_pk_mul_f32 v[128:129], v[128:129], v[240:241] op_sel:[0,0] op_sel_hi:[1,1]\n"
    "v_pk_mul_f32 v[130:131], v[130:131], v[242:243] op_sel:[0,0] op_sel_hi:[1,1]\n"
    "v_pk_mul_f32 v[132:133], v[132:133], v[244:245] op_sel:[0,0] op_sel_hi:[1,1]\n"
    "v_pk_mul_f32 v[134:135], v[134:135], v[246:247] op_sel:[0,0] op_sel_hi:[1,1]\n"
    "v_pk_mul_f32 v[136:137], v[136:137], v[248:249] op_sel:[0,0] op_sel_hi:[1,1]\n"
    "v_pk_mul_f32 v[138:139], v[138:139], v[250:251] op_sel:[0,0] op_sel_hi:[1,1]\n"
    "v_pk_mul_f32 v[140:141], v[140:141], v[252:253] op_sel:[0,0] op_sel_hi:[1,1]\n"
    "v_pk_mul_f32 v[142:143], v[142:143], v[254:255] op_sel:[0,0] op_sel_hi:[1,1]\n"
    "v_cvt_pk_bf16_f32 v160, v128, v129\n"
    "v_cvt_pk_bf16_f32 v161, v130, v131\n"
    "v_cvt_pk_bf16_f32 v162, v132, v133\n"
    "v_cvt_pk_bf16_f32 v163, v134, v135\n"
    "v_cvt_pk_bf16_f32 v164, v136, v137\n"
    "v_cvt_pk_bf16_f32 v165, v138, v139\n"
    "v_cvt_pk_bf16_f32 v166, v140, v141\n"
    "v_cvt_pk_bf16_f32 v167, v142, v143\n"
    "v_permlane16_swap_b32 v160, v162\n"
    "v_permlane16_swap_b32 v161, v163\n"
    "v_permlane16_swap_b32 v164, v166\n"
    "v_permlane16_swap_b32 v165, v167\n"
    "v_pk_fma_f32 v[144:145], v[224:225], v[54:55], v[144:145] op_sel:[0,1,0] op_sel_hi:[1,1,1]\n"
    "v_pk_fma_f32 v[146:147], v[226:227], v[54:55], v[146:147] op_sel:[0,1,0] op_sel_hi:[1,1,1]\n"
    "v_pk_fma_f32 v[148:149], v[228:229], v[54:55], v[148:149] op_sel:[0,1,0] op_sel_hi:[1,1,1]\n"
    "v_pk_fma_f32 v[150:151], v[230:231], v[54:55], v[150:151] op_sel:[0,1,0] op_sel_hi:[1,1,1]\n"
    "v_pk_fma_f32 v[152:153], v[232:233], v[54:55], v[152:153] op_sel:[0,1,0] op_sel_hi:[1,1,1]\n"
    "v_pk_fma_f32 v[154:155], v[234:235], v[54:55], v[154:155] op_sel:[0,1,0] op_sel_hi:[1,1,1]\n"
    "v_pk_fma_f32 v[156:157], v[236:237], v[54:55], v[156:157] op_sel:[0,1,0] op_sel_hi:[1,1,1]\n"
    "v_pk_fma_f32 v[158:159], v[238:239], v[54:55], v[158:159] op_sel:[0,1,0] op_sel_hi:[1,1,1]\n"
    "v_pk_mul_f32 v[240:241], v[144:145], v[144:145] op_sel:[0,0] op_sel_hi:[1,1]\n"
    "v_pk_mul_f32 v[242:243], v[146:147], v[146:147] op_sel:[0,0] op_sel_hi:[1,1]\n"
    "v_pk_mul_f32 v[244:245], v[148:149], v[148:149] op_sel:[0,0] op_sel_hi:[1,1]\n"
    "v_pk_mul_f32 v[246:247], v[150:151], v[150:151] op_sel:[0,0] op_sel_hi:[1,1]\n"
    "v_pk_mul_f32 v[248:249], v[152:153], v[152:153] op_sel:[0,0] op_sel_hi:[1,1]\n"
    "v_pk_mul_f32 v[250:251], v[154:155], v[154:155] op_sel:[0,0] op_sel_hi:[1,1]\n"
    "v_pk_mul_f32 v[252:253], v[156:157], v[156:157] op_sel:[0,0] op_sel_hi:[1,1]\n"
    "v_pk_mul_f32 v[254:255], v[158:159], v[158:159] op_sel:[0,0] op_sel_hi:[1,1]\n"
    "v_pk_fma_f32 v[240:241], v[240:241], v[88:89], v[88:89] op_sel:[0,1,0] op_sel_hi:[1,1,0]\n"
    "v_pk_fma_f32 v[242:243], v[242:243], v[88:89], v[88:89] op_sel:[0,1,0] op_sel_hi:[1,1,0]\n"
    "v_pk_fma_f32 v[244:245], v[244:245], v[88:89], v[88:89] op_sel:[0,1,0] op_sel_hi:[1,1,0]\n"
    "v_pk_fma_f32 v[246:247], v[246:247], v[88:89], v[88:89] op_sel:[0,1,0] op_sel_hi:[1,1,0]\n"
    "v_pk_fma_f32 v[248:249], v[248:249], v[88:89], v[88:89] op_sel:[0,1,0] op_sel_hi:[1,1,0]\n"
    "v_pk_fma_f32 v[250:251], v[250:251], v[88:89], v[88:89] op_sel:[0,1,0] op_sel_hi:[1,1,0]\n"
    "v_pk_fma_f32 v[252:253], v[252:253], v[88:89], v[88:89] op_sel:[0,1,0] op_sel_hi:[1,1,0]\n"
    "v_pk_fma_f32 v[254:255], v[254:255], v[88:89], v[88:89] op_sel:[0,1,0] op_sel_hi:[1,1,0]\n"
    "v_pk_mul_f32 v[240:241], v[240:241], v[144:145] op_sel:[0,0] op_sel_hi:[1,1]\n"
    "v_pk_mul_f32 v[242:243], v[242:243], v[146:147] op_sel:[0,0] op_sel_hi:[1,1]\n"
    "v_pk_mul_f32 v[244:245], v[244:245], v[148:149] op_sel:[0,0] op_sel_hi:[1,1]\n"
    "v_pk_mul_f32 v[246:247], v[246:247], v[150:151] op_sel:[0,0] op_sel_hi:[1,1]\n"
    "v_pk_mul_f32 v[248:249], v[248:249], v[152:153] op_sel:[0,0] op_sel_hi:[1,1]\n"
    "v_pk_mul_f32 v[250:251], v[250:251], v[154:155] op_sel:[0,0] op_sel_hi:[1,1]\n"
    "v_pk_mul_f32 v[252:253], v[252:253], v[156:157] op_sel:[0,0] op_sel_hi:[1,1]\n"
    "v_pk_mul_f32 v[254:255], v[254:255], v[158:159] op_sel:[0,0] op_sel_hi:[1,1]\n"
    "v_exp_f32 v240, v240\n"
    "v_exp_f32 v241, v241\n"
    "v_exp_f32 v242, v242\n"
    "v_exp_f32 v243, v243\n"
    "v_exp_f32 v244, v244\n"
    "v_exp_f32 v245, v245\n"
    "v_exp_f32 v246, v246\n"
    "v_exp_f32 v247, v247\n"
    "v_exp_f32 v248, v248\n"
    "v_exp_f32 v249, v249\n"
    "v_exp_f32 v250, v250\n"
    "v_exp_f32 v251, v251\n"
    "v_exp_f32 v252, v252\n"
    "v_exp_f32 v253, v253\n"
    "v_exp_f32 v254, v254\n"
    "v_exp_f32 v255, v255\n"
    "v_pk_fma_f32 v[240:241], v[240:241], v[90:91], v[90:91] op_sel:[0,0,0] op_sel_hi:[1,0,0]\n"
    "v_pk_fma_f32 v[242:243], v[242:243], v[90:91], v[90:91] op_sel:[0,0,0] op_sel_hi:[1,0,0]\n"
    "v_pk_fma_f32 v[244:245], v[244:245], v[90:91], v[90:91] op_sel:[0,0,0] op_sel_hi:[1,0,0]\n"
    "v_pk_fma_f32 v[246:247], v[246:247], v[90:91], v[90:91] op_sel:[0,0,0] op_sel_hi:[1,0,0]\n"
    "v_pk_fma_f32 v[248:249], v[248:249], v[90:91], v[90:91] op_sel:[0,0,0] op_sel_hi:[1,0,0]\n"
    "v_pk_fma_f32 v[250:251], v[250:251], v[90:91], v[90:91] op_sel:[0,0,0] op_sel_hi:[1,0,0]\n"
    "v_pk_fma_f32 v[252:253], v[252:253], v[90:91], v[90:91] op_sel:[0,0,0] op_sel_hi:[1,0,0]\n"
    "v_pk_fma_f32 v[254:255], v[254:255], v[90:91], v[90:91] op_sel:[0,0,0] op_sel_hi:[1,0,0]\n"
    "v_rcp_f32 v240, v240\n"
    "v_rcp_f32 v241, v241\n"
    "v_rcp_f32 v242, v242\n"
    "v_rcp_f32 v243, v243\n"
    "v_rcp_f32 v244, v244\n"
    "v_rcp_f32 v245, v245\n"
    "v_rcp_f32 v246, v246\n"
    "v_rcp_f32 v247, v247\n"
    "v_rcp_f32 v248, v248\n"
    "v_rcp_f32 v249, v249\n"
    "v_rcp_f32 v250, v250\n"
    "v_rcp_f32 v251, v251\n"
    "v_rcp_f32 v252, v252\n"
    "v_rcp_f32 v253, v253\n"
    "v_rcp_f32 v254, v254\n"
    "v_rcp_f32 v255, v255\n"
    "v_pk_mul_f32 v[144:145], v[144:145], v[240:241] op_sel:[0,0] op_sel_hi:[1,1]\n"
    "v_pk_mul_f32 v[146:147], v[146:147], v[242:243] op_sel:[0,0] op_sel_hi:[1,1]\n"
    "v_pk_mul_f32 v[148:149], v[148:149], v[244:245] op_sel:[0,0] op_sel_hi:[1,1]\n"
    "v_pk_mul_f32 v[150:151], v[150:151], v[246:247] op_sel:[0,0] op_sel_hi:[1,1]\n"
    "v_pk_mul_f32 v[152:153], v[152:153], v[248:249] op_sel:[0,0] op_sel_hi:[1,1]\n"
    "v_pk_mul_f32 v[154:155], v[154:155], v[250:251] op_sel:[0,0] op_sel_hi:[1,1]\n"
    "v_pk_mul_f32 v[156:157], v[156:157], v[252:253] op_sel:[0,0] op_sel_hi:[1,1]\n"
    "v_pk_mul_f32 v[158:159], v[158:159], v[254:255] op_sel:[0,0] op_sel_hi:[1,1]\n"
    "v_cvt_pk_bf16_f32 v168, v144, v145\n"
    "v_cvt_pk_bf16_f32 v169, v146, v147\n"
    "v_cvt_pk_bf16_f32 v170, v148, v149\n"
    "v_cvt_pk_bf16_f32 v171, v150, v151\n"
    "v_cvt_pk_bf16_f32 v172, v152, v153\n"
    "v_cvt_pk_bf16_f32 v173, v154, v155\n"
    "v_cvt_pk_bf16_f32 v174, v156, v157\n"
    "v_cvt_pk_bf16_f32 v175, v158, v159\n"
    "v_permlane16_swap_b32 v168, v170\n"
    "v_permlane16_swap_b32 v169, v171\n"
    "s_nop 0\n"
    "v_permlane16_swap_b32 v160, v168\n"
    "v_permlane16_swap_b32 v161, v169\n"
    "v_permlane16_swap_b32 v162, v170\n"
    "v_permlane16_swap_b32 v163, v171\n"
    "buffer_store_dwordx4 v[160:163], v68, s[36:39], s68 offen offset:0 sc1\n"
    "buffer_store_dwordx4 v[168:171], v68, s[36:39], s73 offen offset:0 sc1\n"
    "v_permlane16_swap_b32 v172, v174\n"
    "v_permlane16_swap_b32 v173, v175\n"
    "s_nop 0\n"
    "v_permlane16_swap_b32 v164, v172\n"
    "v_permlane16_swap_b32 v165, v173\n"
    "v_permlane16_swap_b32 v166, v174\n"
    "v_permlane16_swap_b32 v167, v175\n"
    "buffer_store_dwordx4 v[164:167], v69, s[36:39], s68 offen offset:0 sc1\n"
    "buffer_store_dwordx4 v[172:175], v69, s[36:39], s73 offen offset:0 sc1\n"
    "v_mul_f32 v128, v54, v52\n"
    "v_mov_b32 v129, v52\n"
    "buffer_store_dwordx2 v[128:129], v74, s[36:39], 0 offen offset:0 sc1\n"
    "v_mul_f32 v130, v55, v53\n"
    "v_mov_b32 v131, v53\n"
    "buffer_store_dwordx2 v[130:131], v74, s[36:39], 0 offen offset:128 sc1\n"
    "v_cvt_pk_bf16_f32 v132, v176, v177\n"
    "v_cvt_pk_bf16_f32 v133, v178, v179\n"
    "v_lshlrev_b32 v58, 16, v132\n"
    "v_and_b32 v60, 0xffff0000, v132\n"
    "v_sub_f32 v58, v176, v58\n"
    "v_sub_f32 v60, v177, v60\n"
    "v_cvt_pk_bf16_f32 v134, v58, v60\n"
    "v_lshlrev_b32 v58, 16, v133\n"
    "v_and_b32 v60, 0xffff0000, v133\n"
    "v_sub_f32 v58, v178, v58\n"
    "v_sub_f32 v60, v179, v60\n"
    "v_cvt_pk_bf16_f32 v135, v58, v60\n"
    "buffer_store_dwordx2 v[132:133], v75, s[36:39], 0 offen offset:0 sc1\n"
    "buffer_store_dwordx2 v[134:135], v75, s[36:39], 0 offen offset:3456 sc1\n"
    "v_mov_b32 v136, 0\n"
    "v_mov_b32 v137, 0\n"
    "v_mov_b32 v138, 0\n"
    "v_mov_b32 v139, 0\n"
    "buffer_store_dwordx4 v[136:139], v76, s[36:39], 0 offen offset:0 sc1\n"
    "v_lshlrev_b32 v38, 4, v1\n"
    "v_add_u32 v39, 0x10000, v38\n"
    "v_add_u32 v40, 0x20000, v38\n"
    "s_waitcnt vmcnt(25)\n"
    "s_barrier\n"
    "ds_read_b128 v[192:195], v38 offset:49152\n"
    "ds_read_b128 v[196:199], v38 offset:50176\n"
    "ds_read_b128 v[200:203], v38 offset:53248\n"
    "ds_read_b128 v[204:207], v38 offset:54272\n"
    "ds_read_b128 v[208:211], v38 offset:57344\n"
    "ds_read_b128 v[212:215], v38 offset:58368\n"
    "ds_read_b128 v[216:219], v38 offset:61440\n"
    "ds_read_b128 v[220:223], v38 offset:62464\n"
    "s_waitcnt lgkmcnt(0)\n"
    "v_mfma_f32_16x16x32_bf16 v[64:67], v[192:195], a[0:3], 0\n"
    "ds_read_b128 v[224:227], v38 offset:51200\n"
    "s_add_u32 m0, s56, 0x20000\n"
    "v_mfma_f32_16x16x32_bf16 v[96:99], v[192:195], a[4:7], 0\n"
    "ds_read_b128 v[228:231], v38 offset:52224\n"
    "v_and_b32 v62, 2, v1\n"
    "v_mfma_f32_16x16x32_bf16 v[68:71], v[196:199], a[0:3], 0\n"
    "ds_read_b128 v[232:235], v38 offset:55296\n"
    "v_lshlrev_b32 v62, 29, v62\n"
    "v_mfma_f32_16x16x32_bf16 v[100:103], v[196:199], a[4:7], 0\n"
    "ds_read_b128 v[236:239], v38 offset:56320\n"
    "v_add_u32 v62, v62, v11\n"
    "v_mfma_f32_16x16x32_bf16 v[72:75], v[200:203], a[0:3], 0\n"
    "ds_read_b128 v[240:243], v38 offset:59392\n"
    "s_mov_b32 s0, 0x1000\n"
    "v_mfma_f32_16x16x32_bf16 v[104:107], v[200:203], a[4:7], 0\n"
    "ds_read_b128 v[244:247], v38 offset:60416\n"
    "s_mul_i32 s64, s41, 24\n"
    "v_mfma_f32_16x16x32_bf16 v[76:79], v[204:207], a[0:3], 0\n"
    "ds_read_b128 v[248:251], v38 offset:63488\n"
    "s_add_u32 s64, s64, s51\n"
    "v_mfma_f32_16x16x32_bf16 v[108:111], v[204:207], a[4:7], 0\n"
    "ds_read_b128 v[252:255], v38 offset:64512\n"
    "s_mul_i32 s64, s64, 0x500\n"
    "v_mfma_f32_16x16x32_bf16 v[80:83], v[208:211], a[0:3], 0\n"
    "buffer_load_dword v11, s[36:39], s0 offen offset:0 sc1 lds\n"
    "s_mov_b32 s0, 0x1700\n"
    "v_mfma_f32_16x16x32_bf16 v[112:115], v[208:211], a[4:7], 0\n"
    "buffer_load_dword v11, s[36:39], s0 offen offset:256 sc1 lds\n"
    "s_mov_b32 s0, 0x1e00\n"
    "v_mfma_f32_16x16x32_bf16 v[84:87], a[0:3], v[212:215], 0\n"
    "buffer_load_dword v62, s[36:39], s0 offen offset:512 sc1 lds\n"
    "s_mov_b32 s0, 0x40000000\n"
    "v_mfma_f32_16x16x32_bf16 v[116:119], a[4:7], v[212:215], 0\n"
    "buffer_load_dword v11, s[36:39], s0 offen offset:768 sc1 lds\n"
    "s_mov_b32 s0, 0xc40\n"
    "v_mfma_f32_16x16x32_bf16 v[88:91], a[0:3], v[216:219], 0\n"
    "buffer_load_dword v11, s[36:39], s0 offen offset:1024 sc1 lds\n"
    "s_mov_b32 s0, 0x1340\n"
    "v_mfma_f32_16x16x32_bf16 v[120:123], a[4:7], v[216:219], 0\n"
    "buffer_load_dword v11, s[36:39], s0 offen offset:1280 sc1 lds\n"
    "s_mov_b32 s0, 0x1a40\n"
    "v_mfma_f32_16x16x32_bf16 v[92:95], a[0:3], v[220:223], 0\n"
    "buffer_load_dword v62, s[36:39], s0 offen offset:1536 sc1 lds\n"
    "s_mov_b32 s0, 0x40000000\n"
    "v_mfma_f32_16x16x32_bf16 v[124:127], a[4:7], v[220:223], 0\n"
    "s_waitcnt vmcnt(28) lgkmcnt(0)\n"
    "s_barrier\n"
    "v_mfma_f32_16x16x32_bf16 v[64:67], v[224:227], a[8:11], v[64:67]\n"
    "ds_read_b128 v[192:195], v38 offset:16384\n"
    "buffer_load_dword v11, s[36:39], s0 offen offset:1792 sc1 lds\n"
    "s_add_u32 m0, s57, 0xc000\n"
    "s_sub_u32 s60, s60, 0x4000\n"
    "s_cselect_b32 s60, 0x24000, s60\n"
    "v_mfma_f32_16x16x32_bf16 v[96:99], v[224:227], a[12:15], v[96:99]\n"
    "ds_read_b128 v[196:199], v38 offset:17408\n"
    "buffer_load_dwordx4 v18, s[36:39], s60 offen offset:0 sc1 lds\n"
    "v_mfma_f32_16x16x32_bf16 v[68:71], v[228:231], a[8:11], v[68:71]\n"
    "ds_read_b128 v[200:203], v38 offset:20480\n"
    "s_lshl_b32 s65, s50, 7\n"
    "v_mfma_f32_16x16x32_bf16 v[100:103], v[228:231], a[12:15], v[100:103]\n"
    "ds_read_b128 v[204:207], v38 offset:21504\n"
    "buffer_load_dwordx4 v18, s[36:39], s60 offen offset:1024 sc1 lds\n"
    "v_mfma_f32_16x16x32_bf16 v[72:75], v[232:235], a[8:11], v[72:75]\n"
    "ds_read_b128 v[208:211], v38 offset:24576\n"
    "s_add_u32 s64, s64, s65\n"
    "v_mfma_f32_16x16x32_bf16 v[104:107], v[232:235], a[12:15], v[104:107]\n"
    "ds_read_b128 v[212:215], v38 offset:25600\n"
    "v_mfma_f32_16x16x32_bf16 v[76:79], v[236:239], a[8:11], v[76:79]\n"
    "ds_read_b128 v[216:219], v38 offset:28672\n"
    "v_mfma_f32_16x16x32_bf16 v[108:111], v[236:239], a[12:15], v[108:111]\n"
    "ds_read_b128 v[220:223], v38 offset:29696\n"
    "buffer_load_dwordx4 v19, s[36:39], s60 offen offset:2048 sc1 lds\n"
    "v_mfma_f32_16x16x32_bf16 v[80:83], v[240:243], a[8:11], v[80:83]\n"
    "s_cmp_lt_u32 s42, 30\n"
    "s_cselect_b32 s64, s64, 0x40000000\n"
    "v_mfma_f32_16x16x32_bf16 v[112:115], v[240:243], a[12:15], v[112:115]\n"
    "v_lshrrev_b32 v58, 3, v1\n"
    "v_mul_u32_u24 v58, 0x500, v58\n"
    "v_mfma_f32_16x16x32_bf16 v[84:87], a[8:11], v[244:247], v[84:87]\n"
    "v_add_u32 v58, s64, v58\n"
    "v_and_b32 v59, 7, v1\n"
    "v_mfma_f32_16x16x32_bf16 v[116:119], a[12:15], v[244:247], v[116:119]\n"
    "buffer_load_dwordx4 v19, s[36:39], s60 offen offset:3072 sc1 lds\n"
    "v_xor_b32 v59, v59, v2\n"
    "v_mfma_f32_16x16x32_bf16 v[88:91], a[8:11], v[248:251], v[88:91]\n"
    "s_and_b32 s65, s41, 1\n"
    "s_lshl_b32 s65, s65, 2\n"
    "v_mfma_f32_16x16x32_bf16 v[120:123], a[12:15], v[248:251], v[120:123]\n"
    "v_xor_b32 v59, s65, v59\n"
    "v_lshl_add_u32 v172, v59, 4, v58\n"
    "v_mfma_f32_16x16x32_bf16 v[92:95], a[8:11], v[252:255], v[92:95]\n"
    "v_xor_b32 v59, 4, v59\n"
    "v_lshl_add_u32 v173, v59, 4, v58\n"
    "v_mfma_f32_16x16x32_bf16 v[124:127], a[12:15], v[252:255], v[124:127]\n"
    "s_waitcnt lgkmcnt(0)\n"
    "v_add_u32 v173, 0x2400, v173\n"
    "v_add_u32 v174, 0x4800, v172\n"
    "v_mfma_f32_16x16x32_bf16 v[64:67], v[192:195], a[16:19], v[64:67]\n"
    "ds_read_b128 v[224:227], v38 offset:18432\n"
    "s_mul_i32 s72, s41, 0xc00\n"
    "v_mfma_f32_16x16x32_bf16 v[96:99], v[192:195], a[20:23], v[96:99]\n"
    "ds_read_b128 v[228:231], v38 offset:19456\n"
    "s_mul_i32 s64, s55, 0x1800\n"
    "v_mfma_f32_16x16x32_bf16 v[68:71], v[196:199], a[16:19], v[68:71]\n"
    "ds_read_b128 v[232:235], v38 offset:22528\n"
    "s_add_u32 s64, s64, 0x22000\n"
    "v_mfma_f32_16x16x32_bf16 v[100:103], v[196:199], a[20:23], v[100:103]\n"
    "ds_read_b128 v[236:239], v38 offset:23552\n"
    "v_lshrrev_b32 v58, 1, v2\n"
    "v_mfma_f32_16x16x32_bf16 v[72:75], v[200:203], a[16:19], v[72:75]\n"
    "ds_read_b128 v[240:243], v38 offset:26624\n"
    "s_lshl_b32 s65, s54, 2\n"
    "v_mfma_f32_16x16x32_bf16 v[104:107], v[200:203], a[20:23], v[104:107]\n"
    "ds_read_b128 v[244:247], v38 offset:27648\n"
    "v_or_b32 v58, s65, v58\n"
    "v_mfma_f32_16x16x32_bf16 v[76:79], v[204:207], a[16:19], v[76:79]\n"
    "ds_read_b128 v[248:251], v38 offset:30720\n"
    "v_lshrrev_b32 v59, 1, v3\n"
    "v_mfma_f32_16x16x32_bf16 v[108:111], v[204:207], a[20:23], v[108:111]\n"
    "ds_read_b128 v[252:255], v38 offset:31744\n"
    "v_xor_b32 v58, v58, v59\n"
    "v_mfma_f32_16x16x32_bf16 v[80:83], v[208:211], a[16:19], v[80:83]\n"
    "v_lshlrev_b32 v58, 4, v58\n"
    "v_lshl_add_u32 v58, v3, 7, v58\n"
    "v_mfma_f32_16x16x32_bf16 v[112:115], v[208:211], a[20:23], v[112:115]\n"
    "v_and_b32 v59, 1, v2\n"
    "v_lshl_add_u32 v58, v59, 3, v58\n"
    "v_mfma_f32_16x16x32_bf16 v[84:87], a[16:19], v[212:215], v[84:87]\n"
    "v_add_u32 v14, s64, v58\n"
    "v_xor_b32 v175, 32, v14\n"
    "v_mfma_f32_16x16x32_bf16 v[116:119], a[20:23], v[212:215], v[116:119]\n"
    "s_mul_i32 s69, s42, 37\n"
    "s_lshr_b32 s69, s69, 8\n"
    "v_mfma_f32_16x16x32_bf16 v[88:91], a[16:19], v[216:219], v[88:91]\n"
    "s_mul_i32 s70, s69, 7\n"
    "s_sub_u32 s70, s42, s70\n"
    "v_mfma_f32_16x16x32_bf16 v[120:123], a[20:23], v[216:219], v[120:123]\n"
    "s_lshl_b32 s52, s43, 2\n"
    "s_add_u32 s52, s52, s69\n"
    "v_mfma_f32_16x16x32_bf16 v[92:95], a[16:19], v[220:223], v[92:95]\n"
    "s_mul_i32 s53, s44, 7\n"
    "s_add_u32 s53, s53, s70\n"
    "v_mfma_f32_16x16x32_bf16 v[124:127], a[20:23], v[220:223], v[124:127]\n"
    "s_waitcnt vmcnt(12) lgkmcnt(0)\n"
    "s_barrier\n"
    "s_add_u32 m0, s57, 0x4000\n"
    "s_sub_u32 s60, s60, 0x4000\n"
    "s_cselect_b32 s60, 0x24000, s60\n"
    "buffer_load_dwordx4 v18, s[36:39], s60 offen offset:0 sc1 lds\n"
    "v_mfma_f32_16x16x32_bf16 v[64:67], v[224:227], a[24:27], v[64:67]\n"
    "buffer_store_dwordx2 v[4:5], v9, s[36:39], s84 offen offset:0 sc1\n"
    "v_mfma_f32_16x16x32_bf16 v[96:99], v[224:227], a[28:31], v[96:99]\n"
    "ds_read_b128 v[192:195], v39 offset:16384\n"
    "v_mfma_f32_16x16x32_bf16 v[68:71], v[228:231], a[24:27], v[68:71]\n"
    "ds_read_b128 v[196:199], v39 offset:17408\n"
    "buffer_load_dwordx4 v18, s[36:39], s60 offen offset:1024 sc1 lds\n"
    "v_mfma_f32_16x16x32_bf16 v[100:103], v[228:231], a[28:31], v[100:103]\n"
    "ds_read_b128 v[200:203], v39 offset:20480\n"
    "v_mfma_f32_16x16x32_bf16 v[72:75], v[232:235], a[24:27], v[72:75]\n"
    "ds_read_b128 v[204:207], v39 offset:21504\n"
    "v_mfma_f32_16x16x32_bf16 v[104:107], v[232:235], a[28:31], v[104:107]\n"
    "ds_read_b128 v[208:211], v39 offset:24576\n"
    "v_mfma_f32_16x16x32_bf16 v[76:79], v[236:239], a[24:27], v[76:79]\n"
    "ds_read_b128 v[212:215], v39 offset:25600\n"
    "buffer_load_dwordx4 v19, s[36:39], s60 offen offset:2048 sc1 lds\n"
    "v_mfma_f32_16x16x32_bf16 v[108:111], v[236:239], a[28:31], v[108:111]\n"
    "ds_read_b128 v[216:219], v39 offset:28672\n"
    "v_mfma_f32_16x16x32_bf16 v[80:83], v[240:243], a[24:27], v[80:83]\n"
    "ds_read_b128 v[220:223], v39 offset:29696\n"
    "v_mfma_f32_16x16x32_bf16 v[112:115], v[240:243], a[28:31], v[112:115]\n"
    "s_cmp_lt_u32 s42, 28\n"
    "s_cselect_b32 s26, s26, 0\n"
    "v_mfma_f32_16x16x32_bf16 v[84:87], a[24:27], v[244:247], v[84:87]\n"
    "buffer_load_dwordx4 v19, s[36:39], s60 offen offset:3072 sc1 lds\n"
    "s_lshl_b32 s69, s53, 8\n"
    "v_mfma_f32_16x16x32_bf16 v[116:119], a[28:31], v[244:247], v[116:119]\n"
    "v_mul_u32_u24 v58, 0xd80, v22\n"
    "v_add_u32 v58, v58, v23\n"
    "v_mfma_f32_16x16x32_bf16 v[88:91], a[24:27], v[248:251], v[88:91]\n"
    "v_add_u32 v22, s69, v58\n"
    "v_add_u32 v23, 0x1ac00, v22\n"
    "v_mfma_f32_16x16x32_bf16 v[120:123], a[28:31], v[248:251], v[120:123]\n"
    "v_mov_b32 v58, 0x40000000\n"
    "v_lshrrev_b32 v59, 3, v1\n"
    "v_mfma_f32_16x16x32_bf16 v[92:95], a[24:27], v[252:255], v[92:95]\n"
    "v_mul_u32_u24 v59, 0xd80, v59\n"
    "v_and_b32 v60, 7, v1\n"
    "v_mfma_f32_16x16x32_bf16 v[124:127], a[28:31], v[252:255], v[124:127]\n"
    "s_waitcnt lgkmcnt(0)\n"
    "v_lshl_add_u32 v59, v60, 4, v59\n"
    "s_add_u32 s69, s69, 0x45000\n"
    "v_mfma_f32_16x16x32_bf16 v[64:67], v[192:195], a[32:35], v[64:67]\n"
    "ds_read_b128 v[224:227], v39 offset:18432\n"
    "v_add_u32 v59, s69, v59\n"
    "v_mfma_f32_16x16x32_bf16 v[96:99], v[192:195], a[36:39], v[96:99]\n"
    "ds_read_b128 v[228:231], v39 offset:19456\n"
    "v_mfma_f32_16x16x32_bf16 v[68:71], v[196:199], a[32:35], v[68:71]\n"
    "ds_read_b128 v[232:235], v39 offset:22528\n"
    "v_mfma_f32_16x16x32_bf16 v[100:103], v[196:199], a[36:39], v[100:103]\n"
    "ds_read_b128 v[236:239], v39 offset:23552\n"
    "v_mfma_f32_16x16x32_bf16 v[72:75], v[200:203], a[32:35], v[72:75]\n"
    "ds_read_b128 v[240:243], v39 offset:26624\n"
    "v_mfma_f32_16x16x32_bf16 v[104:107], v[200:203], a[36:39], v[104:107]\n"
    "ds_read_b128 v[244:247], v39 offset:27648\n"
    "v_mfma_f32_16x16x32_bf16 v[76:79], v[204:207], a[32:35], v[76:79]\n"
    "ds_read_b128 v[248:251], v39 offset:30720\n"
    "v_mfma_f32_16x16x32_bf16 v[108:111], v[204:207], a[36:39], v[108:111]\n"
    "ds_read_b128 v[252:255], v39 offset:31744\n"
    "v_mfma_f32_16x16x32_bf16 v[80:83], v[208:211], a[32:35], v[80:83]\n"
    "s_cmp_eq_u32 s41, 0\n"
    "s_cselect_b32 s70, 16, 0\n"
    "v_mfma_f32_16x16x32_bf16 v[112:115], v[208:211], a[36:39], v[112:115]\n"
    "s_cmp_lt_u32 s42, 28\n"
    "s_cselect_b32 s70, s70, 0\n"
    "v_mfma_f32_16x16x32_bf16 v[84:87], a[32:35], v[212:215], v[84:87]\n"
    "v_cmp_gt_u32 vcc, s70, v1\n"
    "s_nop 1\n"
    "v_cndmask_b32 v24, v58, v59, vcc\n"
    "v_mfma_f32_16x16x32_bf16 v[116:119], a[36:39], v[212:215], v[116:119]\n"
    "v_add_u32 v25, 0xfffffc80, v24\n"
    "s_mul_i32 s69, s55, 48\n"
    "v_mfma_f32_16x16x32_bf16 v[88:91], a[32:35], v[216:219], v[88:91]\n"
    "s_add_u32 s69, s69, s51\n"
    "s_lshl_b32 s69, s69, 4\n"
    "v_mfma_f32_16x16x32_bf16 v[120:123], a[36:39], v[216:219], v[120:123]\n"
    "s_add_u32 s69, s69, 0x3eb400\n"
    "v_mfma_f32_16x16x32_bf16 v[92:95], a[32:35], v[220:223], v[92:95]\n"
    "s_cmp_lt_u32 s42, 30\n"
    "s_cselect_b32 s69, s69, 0x40000000\n"
    "v_mfma_f32_16x16x32_bf16 v[124:127], a[36:39], v[220:223], v[124:127]\n"
    "s_waitcnt lgkmcnt(0)\n"
    "s_barrier\n"
    "s_add_u32 m0, s57, 0x14000\n"
    "s_sub_u32 s60, s60, 0x4000\n"
    "s_cselect_b32 s60, 0x24000, s60\n"
    "buffer_load_dwordx4 v18, s[36:39], s60 offen offset:0 sc1 lds\n"
    "v_mfma_f32_16x16x32_bf16 v[64:67], v[224:227], a[40:43], v[64:67]\n"
    "ds_read_b128 v[192:195], v39 offset:32768\n"
    "v_mul_u32_u24 v58, 0x8080, v2\n"
    "v_mfma_f32_16x16x32_bf16 v[96:99], v[224:227], a[44:47], v[96:99]\n"
    "ds_read_b128 v[196:199], v39 offset:33792\n"
    "buffer_load_dwordx4 v18, s[36:39], s60 offen offset:1024 sc1 lds\n"
    "v_mfma_f32_16x16x32_bf16 v[68:71], v[228:231], a[40:43], v[68:71]\n"
    "ds_read_b128 v[200:203], v39 offset:36864\n"
    "v_lshl_add_u32 v58, v3, 4, v58\n"
    "v_mfma_f32_16x16x32_bf16 v[100:103], v[228:231], a[44:47], v[100:103]\n"
    "ds_read_b128 v[204:207], v39 offset:37888\n"
    "v_add_u32 v34, s69, v58\n"
    "v_mfma_f32_16x16x32_bf16 v[72:75], v[232:235], a[40:43], v[72:75]\n"
    "ds_read_b128 v[208:211], v39 offset:40960\n"
    "v_add_u32 v35, 0x20200, v34\n"
    "v_mfma_f32_16x16x32_bf16 v[104:107], v[232:235], a[44:47], v[104:107]\n"
    "ds_read_b128 v[212:215], v39 offset:41984\n"
    "buffer_load_dwordx4 v19, s[36:39], s60 offen offset:2048 sc1 lds\n"
    "v_mfma_f32_16x16x32_bf16 v[76:79], v[236:239], a[40:43], v[76:79]\n"
    "ds_read_b128 v[216:219], v39 offset:45056\n"
    "v_mfma_f32_16x16x32_bf16 v[108:111], v[236:239], a[44:47], v[108:111]\n"
    "ds_read_b128 v[220:223], v39 offset:46080\n"
    "v_mfma_f32_16x16x32_bf16 v[80:83], v[240:243], a[40:43], v[80:83]\n"
    "s_cmp_eq_u32 s3, 0\n"
    "s_cselect_b32 s94, 0, s55\n"
    "v_mfma_f32_16x16x32_bf16 v[112:115], v[240:243], a[44:47], v[112:115]\n"
    "buffer_load_dwordx4 v19, s[36:39], s60 offen offset:3072 sc1 lds\n"
    "s_add_u32 m0, s72, 0x22000\n"
    "v_mfma_f32_16x16x32_bf16 v[84:87], a[40:43], v[244:247], v[84:87]\n"
    "s_mul_i32 s94, s94, 0x40000000\n"
    "v_add_u32 v36, s94, v34\n"
    "v_mfma_f32_16x16x32_bf16 v[116:119], a[44:47], v[244:247], v[116:119]\n"
    "v_add_u32 v37, s94, v35\n"
    "s_lshl_b32 s69, s45, 4\n"
    "v_mfma_f32_16x16x32_bf16 v[88:91], a[40:43], v[248:251], v[88:91]\n"
    "s_add_u32 s69, s69, s46\n"
    "s_lshl_b32 s69, s69, 7\n"
    "v_mfma_f32_16x16x32_bf16 v[120:123], a[44:47], v[248:251], v[120:123]\n"
    "buffer_load_dwordx4 v172, s[4:7], 0 offen offset:0 lds\n"
    "s_add_u32 s85, s69, 0x11000\n"
    "v_mfma_f32_16x16x32_bf16 v[92:95], a[40:43], v[252:255], v[92:95]\n"
    "s_cmp_eq_u32 s45, 0\n"
    "s_cselect_b32 s69, 0, 1\n"
    "v_mfma_f32_16x16x32_bf16 v[124:127], a[44:47], v[252:255], v[124:127]\n"
    "s_waitcnt lgkmcnt(0)\n"
    "s_cmp_eq_u32 s41, 0\n"
    "s_cselect_b32 s69, s69, 0\n"
    "v_mfma_f32_16x16x32_bf16 v[64:67], v[192:195], a[48:51], v[64:67]\n"
    "ds_read_b128 v[224:227], v39 offset:34816\n"
    "s_not_b32 s78, s69\n"
    "v_mfma_f32_16x16x32_bf16 v[96:99], v[192:195], a[52:55], v[96:99]\n"
    "ds_read_b128 v[228:231], v39 offset:35840\n"
    "buffer_load_dwordx4 v173, s[4:7], 0 offen offset:1024 lds\n"
    "v_mfma_f32_16x16x32_bf16 v[68:71], v[196:199], a[48:51], v[68:71]\n"
    "ds_read_b128 v[232:235], v39 offset:38912\n"
    "s_mul_i32 s69, s49, 10\n"
    "v_mfma_f32_16x16x32_bf16 v[100:103], v[196:199], a[52:55], v[100:103]\n"
    "ds_read_b128 v[236:239], v39 offset:39936\n"
    "s_add_u32 s69, s69, s50\n"
    "v_mfma_f32_16x16x32_bf16 v[72:75], v[200:203], a[48:51], v[72:75]\n"
    "ds_read_b128 v[240:243], v39 offset:43008\n"
    "s_lshl_b32 s69, s69, 7\n"
    "v_mfma_f32_16x16x32_bf16 v[104:107], v[200:203], a[52:55], v[104:107]\n"
    "ds_read_b128 v[244:247], v39 offset:44032\n"
    "buffer_load_dwordx4 v174, s[4:7], 0 offen offset:2048 lds\n"
    "v_mfma_f32_16x16x32_bf16 v[76:79], v[204:207], a[48:51], v[76:79]\n"
    "ds_read_b128 v[248:251], v39 offset:47104\n"
    "s_add_u32 s86, s69, 0x19000\n"
    "v_mfma_f32_16x16x32_bf16 v[108:111], v[204:207], a[52:55], v[108:111]\n"
    "ds_read_b128 v[252:255], v39 offset:48128\n"
    "v_mfma_f32_16x16x32_bf16 v[80:83], v[208:211], a[48:51], v[80:83]\n"
    "s_cmp_lt_u32 s42, 30\n"
    "s_cselect_b32 s86, s86, 0x40000000\n"
    "v_mfma_f32_16x16x32_bf16 v[112:115], v[208:211], a[52:55], v[112:115]\n"
    "s_mov_b32 s69, 0x3fffff\n"
    "v_mfma_f32_16x16x32_bf16 v[84:87], a[48:51], v[212:215], v[84:87]\n"
    "s_cmp_eq_u32 s53, 13\n"
    "s_cselect_b32 s69, 0x1fffff, s69\n"
    "v_mfma_f32_16x16x32_bf16 v[116:119], a[52:55], v[212:215], v[116:119]\n"
    "s_cmp_lt_u32 s42, 28\n"
    "s_cselect_b32 s69, s69, 0\n"
    "v_mfma_f32_16x16x32_bf16 v[88:91], a[48:51], v[216:219], v[88:91]\n"
    "s_cmp_eq_u32 s41, 0\n"
    "s_cselect_b32 s69, s69, 0\n"
    "v_mfma_f32_16x16x32_bf16 v[120:123], a[52:55], v[216:219], v[120:123]\n"
    "s_not_b32 s79, s69\n"
    "s_lshr_b32 s69, s52, 1\n"
    "v_mfma_f32_16x16x32_bf16 v[92:95], a[48:51], v[220:223], v[92:95]\n"
    "s_mul_i32 s69, s69, 3\n"
    "s_and_b32 s70, s52, 1\n"
    "v_mfma_f32_16x16x32_bf16 v[124:127], a[52:55], v[220:223], v[124:127]\n"
    "s_waitcnt lgkmcnt(0)\n"
    "s_barrier\n"
    "s_add_u32 m0, s57, 0x18000\n"
    "s_sub_u32 s60, s60, 0x4000\n"
    "s_cselect_b32 s60, 0x24000, s60\n"
    "buffer_load_dwordx4 v18, s[36:39], s60 offen offset:0 sc1 lds\n"
    "v_mfma_f32_16x16x32_bf16 v[64:67], v[224:227], a[56:59], v[64:67]\n"
    "ds_read_b128 v[192:195], v39 offset:0\n"
    "s_add_u32 s69, s69, s70\n"
    "v_mfma_f32_16x16x32_bf16 v[96:99], v[224:227], a[60:63], v[96:99]\n"
    "ds_read_b128 v[196:199], v39 offset:1024\n"
    "buffer_load_dwordx4 v18, s[36:39], s60 offen offset:1024 sc1 lds\n"
    "v_mfma_f32_16x16x32_bf16 v[68:71], v[228:231], a[56:59], v[68:71]\n"
    "ds_read_b128 v[200:203], v39 offset:4096\n"
    "s_mul_i32 s69, s69, 0x500\n"
    "v_mfma_f32_16x16x32_bf16 v[100:103], v[228:231], a[60:63], v[100:103]\n"
    "ds_read_b128 v[204:207], v39 offset:5120\n"
    "s_add_u32 s69, s69, 0x19000\n"
    "v_mfma_f32_16x16x32_bf16 v[72:75], v[232:235], a[56:59], v[72:75]\n"
    "ds_read_b128 v[208:211], v39 offset:8192\n"
    "s_lshl_b32 s70, s53, 8\n"
    "v_mfma_f32_16x16x32_bf16 v[104:107], v[232:235], a[60:63], v[104:107]\n"
    "ds_read_b128 v[212:215], v39 offset:9216\n"
    "buffer_load_dwordx4 v19, s[36:39], s60 offen offset:2048 sc1 lds\n"
    "v_mfma_f32_16x16x32_bf16 v[76:79], v[236:239], a[56:59], v[76:79]\n"
    "ds_read_b128 v[216:219], v39 offset:12288\n"
    "s_add_u32 s70, s70, 0xd600\n"
    "v_mfma_f32_16x16x32_bf16 v[108:111], v[236:239], a[60:63], v[108:111]\n"
    "ds_read_b128 v[220:223], v39 offset:13312\n"
    "v_mov_b32 v58, s69\n"
    "v_mfma_f32_16x16x32_bf16 v[80:83], v[240:243], a[56:59], v[80:83]\n"
    "v_mov_b32 v59, s70\n"
    "v_mfma_f32_16x16x32_bf16 v[112:115], v[240:243], a[60:63], v[112:115]\n"
    "buffer_load_dwordx4 v19, s[36:39], s60 offen offset:3072 sc1 lds\n"
    "v_mfma_f32_16x16x32_bf16 v[84:87], a[56:59], v[244:247], v[84:87]\n"
    "v_cmp_gt_u32 vcc, 20, v1\n"
    "s_nop 1\n"
    "v_cndmask_b32 v58, v59, v58, vcc\n"
    "v_mfma_f32_16x16x32_bf16 v[116:119], a[60:63], v[244:247], v[116:119]\n"
    "v_add_u32 v10, v58, v8\n"
    "s_mul_i32 s64, s41, 0xc00\n"
    "v_mfma_f32_16x16x32_bf16 v[88:91], a[56:59], v[248:251], v[88:91]\n"
    "s_add_u32 s64, s64, 0x22000\n"
    "v_lshl_add_u32 v16, v1, 4, s64\n"
    "v_mfma_f32_16x16x32_bf16 v[120:123], a[60:63], v[248:251], v[120:123]\n"
    "s_mul_i32 s95, s40, 0xc00\n"
    "s_add_u32 s95, s95, 0x97000\n"
    "v_mfma_f32_16x16x32_bf16 v[92:95], a[56:59], v[252:255], v[92:95]\n"
    "s_cmp_eq_u32 s41, 3\n"
    "s_cselect_b32 s95, s95, 0x40000000\n"
    "v_mfma_f32_16x16x32_bf16 v[124:127], a[60:63], v[252:255], v[124:127]\n"
    "s_waitcnt lgkmcnt(0)\n"
    "s_mul_i32 s64, s41, 0x1800\n"
    "s_add_u32 s64, s64, 0x22000\n"
    "v_mfma_f32_16x16x32_bf16 v[64:67], v[192:195], a[64:67], v[64:67]\n"
    "ds_read_b128 v[224:227], v39 offset:2048\n"
    "v_lshl_add_u32 v56, v1, 4, s64\n"
    "v_mfma_f32_16x16x32_bf16 v[96:99], v[192:195], a[68:71], v[96:99]\n"
    "ds_read_b128 v[228:231], v39 offset:3072\n"
    "s_xor_b32 s64, s41, 2\n"
    "v_mfma_f32_16x16x32_bf16 v[68:71], v[196:199], a[64:67], v[68:71]\n"
    "ds_read_b128 v[232:235], v39 offset:6144\n"
    "s_mul_i32 s64, s64, 0x1800\n"
    "v_mfma_f32_16x16x32_bf16 v[100:103], v[196:199], a[68:71], v[100:103]\n"
    "ds_read_b128 v[236:239], v39 offset:7168\n"
    "s_add_u32 s64, s64, 0x22000\n"
    "v_mfma_f32_16x16x32_bf16 v[72:75], v[200:203], a[64:67], v[72:75]\n"
    "ds_read_b128 v[240:243], v39 offset:10240\n"
    "v_lshl_add_u32 v57, v1, 4, s64\n"
    "v_mfma_f32_16x16x32_bf16 v[104:107], v[200:203], a[68:71], v[104:107]\n"
    "ds_read_b128 v[244:247], v39 offset:11264\n"
    "s_mul_i32 s64, s55, 48\n"
    "v_mfma_f32_16x16x32_bf16 v[76:79], v[204:207], a[64:67], v[76:79]\n"
    "ds_read_b128 v[248:251], v39 offset:14336\n"
    "s_add_u32 s64, s64, s51\n"
    "v_mfma_f32_16x16x32_bf16 v[108:111], v[204:207], a[68:71], v[108:111]\n"
    "ds_read_b128 v[252:255], v39 offset:15360\n"
    "v_mfma_f32_16x16x32_bf16 v[80:83], v[208:211], a[64:67], v[80:83]\n"
    "s_cmp_lt_u32 s42, 30\n"
    "s_cselect_b32 s65, 0, 0x40000000\n"
    "v_mfma_f32_16x16x32_bf16 v[112:115], v[208:211], a[68:71], v[112:115]\n"
    "s_lshl_b32 s69, s50, 1\n"
    "s_add_u32 s69, s69, s54\n"
    "v_mfma_f32_16x16x32_bf16 v[84:87], a[64:67], v[212:215], v[84:87]\n"
    "s_lshl_b32 s70, s64, 3\n"
    "s_add_u32 s70, s70, 0x41000\n"
    "v_mfma_f32_16x16x32_bf16 v[116:119], a[68:71], v[212:215], v[116:119]\n"
    "s_add_u32 s70, s70, s65\n"
    "v_lshl_add_u32 v15, v3, 3, s70\n"
    "v_mfma_f32_16x16x32_bf16 v[88:91], a[64:67], v[216:219], v[88:91]\n"
    "s_mul_i32 s70, s69, 0x20200\n"
    "s_lshl_b32 s71, s64, 4\n"
    "v_mfma_f32_16x16x32_bf16 v[120:123], a[68:71], v[216:219], v[120:123]\n"
    "s_add_u32 s70, s70, s71\n"
    "s_add_u32 s70, s70, 0x66e000\n"
    "v_mfma_f32_16x16x32_bf16 v[92:95], a[64:67], v[220:223], v[92:95]\n"
    "s_add_u32 s70, s70, s65\n"
    "v_and_b32 v58, 1, v2\n"
    "v_mfma_f32_16x16x32_bf16 v[124:127], a[68:71], v[220:223], v[124:127]\n"
    "s_waitcnt vmcnt(16) lgkmcnt(0)\n"
    "s_barrier\n"
    "s_add_u32 m0, s57, 0x10000\n"
    "s_sub_u32 s60, s60, 0x4000\n"
    "s_cselect_b32 s60, 0x24000, s60\n"
    "buffer_load_dwordx4 v18, s[36:39], s60 offen offset:0 sc1 lds\n"
    "v_mfma_f32_16x16x32_bf16 v[64:67], v[224:227], a[72:75], v[64:67]\n"
    "ds_read_b128 v[192:195], v38 offset:49152\n"
    "v_lshrrev_b32 v59, 1, v2\n"
    "v_mfma_f32_16x16x32_bf16 v[96:99], v[224:227], a[76:79], v[96:99]\n"
    "ds_read_b128 v[196:199], v38 offset:50176\n"
    "buffer_load_dwordx4 v18, s[36:39], s60 offen offset:1024 sc1 lds\n"
    "v_mfma_f32_16x16x32_bf16 v[68:71], v[228:231], a[72:75], v[68:71]\n"
    "ds_read_b128 v[200:203], v38 offset:53248\n"
    "v_lshl_add_u32 v58, v58, 1, v59\n"
    "v_mfma_f32_16x16x32_bf16 v[100:103], v[228:231], a[76:79], v[100:103]\n"
    "ds_read_b128 v[204:207], v38 offset:54272\n"
    "v_mul_u32_u24 v58, 0x8080, v58\n"
    "v_mfma_f32_16x16x32_bf16 v[72:75], v[232:235], a[72:75], v[72:75]\n"
    "ds_read_b128 v[208:211], v38 offset:57344\n"
    "v_lshl_add_u32 v58, v3, 4, v58\n"
    "v_mfma_f32_16x16x32_bf16 v[104:107], v[232:235], a[76:79], v[104:107]\n"
    "ds_read_b128 v[212:215], v38 offset:58368\n"
    "buffer_load_dwordx4 v19, s[36:39], s60 offen offset:2048 sc1 lds\n"
    "v_mfma_f32_16x16x32_bf16 v[76:79], v[236:239], a[72:75], v[76:79]\n"
    "ds_read_b128 v[216:219], v38 offset:61440\n"
    "v_add_u32 v12, s70, v58\n"
    "v_mfma_f32_16x16x32_bf16 v[108:111], v[236:239], a[76:79], v[108:111]\n"
    "ds_read_b128 v[220:223], v38 offset:62464\n"
    "s_lshl_b32 s70, s69, 14\n"
    "v_mfma_f32_16x16x32_bf16 v[80:83], v[240:243], a[72:75], v[80:83]\n"
    "s_lshl_b32 s71, s64, 3\n"
    "s_add_u32 s70, s70, s71\n"
    "v_mfma_f32_16x16x32_bf16 v[112:115], v[240:243], a[76:79], v[112:115]\n"
    "buffer_load_dwordx4 v19, s[36:39], s60 offen offset:3072 sc1 lds\n"
    "s_add_u32 s70, s70, 0x47000\n"
    "v_mfma_f32_16x16x32_bf16 v[84:87], a[72:75], v[244:247], v[84:87]\n"
    "s_add_u32 s70, s70, s65\n"
    "v_lshlrev_b32 v58, 3, v3\n"
    "v_mfma_f32_16x16x32_bf16 v[116:119], a[76:79], v[244:247], v[116:119]\n"
    "v_lshl_add_u32 v58, v2, 2, v58\n"
    "v_add_u32 v58, s70, v58\n"
    "v_mfma_f32_16x16x32_bf16 v[88:91], a[72:75], v[248:251], v[88:91]\n"
    "v_mov_b32 v59, 0x40000000\n"
    "v_mfma_f32_16x16x32_bf16 v[120:123], a[76:79], v[248:251], v[120:123]\n"
    "v_cmp_gt_u32 vcc, 32, v1\n"
    "s_nop 1\n"
    "v_cndmask_b32 v13, v59, v58, vcc\n"
    "v_mfma_f32_16x16x32_bf16 v[92:95], a[72:75], v[252:255], v[92:95]\n"
    "s_lshl_b32 s64, s54, 1\n"
    "s_lshl_b32 s65, s54, 13\n"
    "v_mfma_f32_16x16x32_bf16 v[124:127], a[76:79], v[252:255], v[124:127]\n"
    "s_waitcnt lgkmcnt(0)\n"
    "v_bfe_u32 v58, v1, 3, 1\n"
    "v_lshlrev_b32 v58, 1, v58\n"
    "v_mfma_f32_16x16x32_bf16 v[64:67], v[192:195], a[80:83], v[64:67]\n"
    "ds_read_b128 v[224:227], v38 offset:51200\n"
    "v_and_b32 v59, 1, v2\n"
    "v_mfma_f32_16x16x32_bf16 v[96:99], v[192:195], a[84:87], v[96:99]\n"
    "ds_read_b128 v[228:231], v38 offset:52224\n"
    "v_or_b32 v59, v58, v59\n"
    "v_mfma_f32_16x16x32_bf16 v[68:71], v[196:199], a[80:83], v[68:71]\n"
    "ds_read_b128 v[232:235], v38 offset:55296\n"
    "v_xor_b32 v59, s64, v59\n"
    "v_mfma_f32_16x16x32_bf16 v[100:103], v[196:199], a[84:87], v[100:103]\n"
    "ds_read_b128 v[236:239], v38 offset:56320\n"
    "v_lshlrev_b32 v59, 5, v59\n"
    "v_mfma_f32_16x16x32_bf16 v[72:75], v[200:203], a[80:83], v[72:75]\n"
    "ds_read_b128 v[240:243], v38 offset:59392\n"
    "v_bfe_u32 v58, v1, 2, 2\n"
    "v_mfma_f32_16x16x32_bf16 v[104:107], v[200:203], a[84:87], v[104:107]\n"
    "ds_read_b128 v[244:247], v38 offset:60416\n"
    "v_lshlrev_b32 v58, 7, v58\n"
    "v_mfma_f32_16x16x32_bf16 v[76:79], v[204:207], a[80:83], v[76:79]\n"
    "ds_read_b128 v[248:251], v38 offset:63488\n"
    "v_lshl_add_u32 v58, v2, 11, v58\n"
    "v_mfma_f32_16x16x32_bf16 v[108:111], v[204:207], a[84:87], v[108:111]\n"
    "ds_read_b128 v[252:255], v38 offset:64512\n"
    "v_or_b32 v59, v58, v59\n"
    "v_mfma_f32_16x16x32_bf16 v[80:83], v[208:211], a[80:83], v[80:83]\n"
    "v_and_b32 v58, 3, v1\n"
    "v_lshl_add_u32 v26, v58, 3, v59\n"
    "v_mfma_f32_16x16x32_bf16 v[112:115], v[208:211], a[84:87], v[112:115]\n"
    "v_add_u32 v26, s65, v26\n"
    "v_xor_b32 v27, 32, v26\n"
    "v_mfma_f32_16x16x32_bf16 v[84:87], a[80:83], v[212:215], v[84:87]\n"
    "s_mul_i32 s92, s93, 2\n"
    "s_add_u32 s92, s92, s3\n"
    "s_mul_i32 s92, s92, 0x500\n"
    "s_add_u32 s92, s92, 0x8b00\n"
    "buffer_load_dwordx2 v[6:7], v8, s[36:39], s92 offen offset:0 sc1\n"
    "v_mfma_f32_16x16x32_bf16 v[116:119], a[84:87], v[212:215], v[116:119]\n"
    "v_xor_b32 v28, 64, v26\n"
    "v_xor_b32 v29, 0x60, v26\n"
    "v_mfma_f32_16x16x32_bf16 v[88:91], a[80:83], v[216:219], v[88:91]\n"
    "v_add_u32 v30, 0x10000, v26\n"
    "v_add_u32 v31, 0x10000, v27\n"
    "v_mfma_f32_16x16x32_bf16 v[120:123], a[84:87], v[216:219], v[120:123]\n"
    "v_add_u32 v32, 0x10000, v28\n"
    "v_add_u32 v33, 0x10000, v29\n"
    "v_mfma_f32_16x16x32_bf16 v[92:95], a[80:83], v[220:223], v[92:95]\n"
    "s_lshl_b32 s64, s41, 7\n"
    "s_add_u32 s64, s64, 0x26800\n"
    "v_mfma_f32_16x16x32_bf16 v[124:127], a[84:87], v[220:223], v[124:127]\n"
    "s_waitcnt vmcnt(16) lgkmcnt(0)\n"
    "s_barrier\n"
    "s_add_u32 m0, s56, 0xc000\n"
    "s_sub_u32 s61, s61, 0x14000\n"
    "s_cselect_b32 s61, 0xb4000, s61\n"
    "buffer_load_dwordx4 v20, s[20:23], s61 offen offset:0 lds\n"
    "v_mfma_f32_16x16x32_bf16 v[64:67], v[224:227], a[88:91], v[64:67]\n"
    "ds_read_b128 v[192:195], v38 offset:16384\n"
    "v_lshl_add_u32 v58, v3, 2, s64\n"
    "v_mfma_f32_16x16x32_bf16 v[96:99], v[224:227], a[92:95], v[96:99]\n"
    "ds_read_b128 v[196:199], v38 offset:17408\n"
    "buffer_load_dwordx4 v21, s[20:23], s61 offen offset:1024 lds\n"
    "v_mfma_f32_16x16x32_bf16 v[68:71], v[228:231], a[88:91], v[68:71]\n"
    "ds_read_b128 v[200:203], v38 offset:20480\n"
    "s_add_u32 m0, s56, 0xe000\n"
    "v_mfma_f32_16x16x32_bf16 v[100:103], v[228:231], a[92:95], v[100:103]\n"
    "ds_read_b128 v[204:207], v38 offset:21504\n"
    "v_lshl_add_u32 v59, v2, 4, s64\n"
    "v_mfma_f32_16x16x32_bf16 v[72:75], v[232:235], a[88:91], v[72:75]\n"
    "ds_read_b128 v[208:211], v38 offset:24576\n"
    "ds_write_b32 v58, v52 offset:0\n"
    "v_mfma_f32_16x16x32_bf16 v[104:107], v[232:235], a[92:95], v[104:107]\n"
    "ds_read_b128 v[212:215], v38 offset:25600\n"
    "ds_write_b32 v58, v53 offset:64\n"
    "v_mfma_f32_16x16x32_bf16 v[76:79], v[236:239], a[88:91], v[76:79]\n"
    "ds_read_b128 v[216:219], v38 offset:28672\n"
    "s_sub_u32 s62, s62, 0x14000\n"
    "s_cselect_b32 s62, 0xb4000, s62\n"
    "v_mfma_f32_16x16x32_bf16 v[108:111], v[236:239], a[92:95], v[108:111]\n"
    "ds_read_b128 v[220:223], v38 offset:29696\n"
    "buffer_load_dwordx4 v20, s[20:23], s62 offen offset:0 lds\n"
    "v_mfma_f32_16x16x32_bf16 v[80:83], v[240:243], a[88:91], v[80:83]\n"
    "ds_read_b128 v[164:167], v59 offset:0\n"
    "ds_read_b128 v[168:171], v59 offset:64\n"
    "v_mfma_f32_16x16x32_bf16 v[112:115], v[240:243], a[92:95], v[112:115]\n"
    "buffer_load_dwordx4 v21, s[20:23], s62 offen offset:1024 lds\n"
    "v_mfma_f32_16x16x32_bf16 v[84:87], a[88:91], v[244:247], v[84:87]\n"
    "v_mfma_f32_16x16x32_bf16 v[116:119], a[92:95], v[244:247], v[116:119]\n"
    "v_mfma_f32_16x16x32_bf16 v[88:91], a[88:91], v[248:251], v[88:91]\n"
    "v_mfma_f32_16x16x32_bf16 v[120:123], a[92:95], v[248:251], v[120:123]\n"
    "v_mfma_f32_16x16x32_bf16 v[92:95], a[88:91], v[252:255], v[92:95]\n"
    "v_mfma_f32_16x16x32_bf16 v[124:127], a[92:95], v[252:255], v[124:127]\n"
    "s_waitcnt lgkmcnt(2)\n"
    "v_mfma_f32_16x16x32_bf16 v[64:67], v[192:195], a[96:99], v[64:67]\n"
    "ds_read_b128 v[224:227], v38 offset:18432\n"
    "v_mfma_f32_16x16x32_bf16 v[96:99], v[192:195], a[100:103], v[96:99]\n"
    "ds_read_b128 v[228:231], v38 offset:19456\n"
    "v_mfma_f32_16x16x32_bf16 v[68:71], v[196:199], a[96:99], v[68:71]\n"
    "ds_read_b128 v[232:235], v38 offset:22528\n"
    "v_mfma_f32_16x16x32_bf16 v[100:103], v[196:199], a[100:103], v[100:103]\n"
    "ds_read_b128 v[236:239], v38 offset:23552\n"
    "v_mfma_f32_16x16x32_bf16 v[72:75], v[200:203], a[96:99], v[72:75]\n"
    "ds_read_b128 v[240:243], v38 offset:26624\n"
    "v_mfma_f32_16x16x32_bf16 v[104:107], v[200:203], a[100:103], v[104:107]\n"
    "ds_read_b128 v[244:247], v38 offset:27648\n"
    "v_mfma_f32_16x16x32_bf16 v[76:79], v[204:207], a[96:99], v[76:79]\n"
    "ds_read_b128 v[248:251], v38 offset:30720\n"
    "v_mfma_f32_16x16x32_bf16 v[108:111], v[204:207], a[100:103], v[108:111]\n"
    "ds_read_b128 v[252:255], v38 offset:31744\n"
    "v_mfma_f32_16x16x32_bf16 v[80:83], v[208:211], a[96:99], v[80:83]\n"
    "v_mfma_f32_16x16x32_bf16 v[112:115], v[208:211], a[100:103], v[112:115]\n"
    "v_mfma_f32_16x16x32_bf16 v[84:87], a[96:99], v[212:215], v[84:87]\n"
    "v_mfma_f32_16x16x32_bf16 v[116:119], a[100:103], v[212:215], v[116:119]\n"
    "v_mfma_f32_16x16x32_bf16 v[88:91], a[96:99], v[216:219], v[88:91]\n"
    "v_mfma_f32_16x16x32_bf16 v[120:123], a[100:103], v[216:219], v[120:123]\n"
    "v_mfma_f32_16x16x32_bf16 v[92:95], a[96:99], v[220:223], v[92:95]\n"
    "v_mfma_f32_16x16x32_bf16 v[124:127], a[100:103], v[220:223], v[124:127]\n"
    "s_waitcnt vmcnt(4)\n"
    "v_cmp_eq_u64 vcc, v[6:7], v[4:5]\n"
    "s_or_b32 vcc_lo, vcc_lo, s77\n"
    "s_cmp_eq_u32 vcc_lo, -1\n"
    "s_cbranch_scc0 .Lspin_H\n"
    ".Ldone_H:\n"
    "s_waitcnt lgkmcnt(0)\n"
    "s_barrier\n"
    "buffer_load_dwordx2 v[176:177], v15, s[36:39], 0 offen offset:0 sc1\n"
    "v_mfma_f32_16x16x32_bf16 v[64:67], v[224:227], a[104:107], v[64:67]\n"
    "ds_read_b128 v[192:195], v39 offset:16384\n"
    "buffer_load_dwordx2 v[178:179], v15, s[36:39], 0 offen offset:128 sc1\n"
    "v_mfma_f32_16x16x32_bf16 v[96:99], v[224:227], a[108:111], v[96:99]\n"
    "ds_read_b128 v[196:199], v39 offset:17408\n"
    "buffer_load_dwordx2 v[180:181], v15, s[36:39], s94 offen offset:256 sc1\n"
    "v_mfma_f32_16x16x32_bf16 v[68:71], v[228:231], a[104:107], v[68:71]\n"
    "ds_read_b128 v[200:203], v39 offset:20480\n"
    "s_mul_i32 s63, s54, 5\n"
    "v_mfma_f32_16x16x32_bf16 v[100:103], v[228:231], a[108:111], v[100:103]\n"
    "ds_read_b128 v[204:207], v39 offset:21504\n"
    "s_add_u32 s63, s63, s75\n"
    "v_mfma_f32_16x16x32_bf16 v[72:75], v[232:235], a[104:107], v[72:75]\n"
    "ds_read_b128 v[208:211], v39 offset:24576\n"
    "s_cmp_ge_u32 s63, 10\n"
    "s_cselect_b32 s1, 10, 0\n"
    "s_sub_u32 s63, s63, s1\n"
    "s_mul_i32 s63, s63, 0x40400\n"
    "v_mfma_f32_16x16x32_bf16 v[104:107], v[232:235], a[108:111], v[104:107]\n"
    "buffer_load_dwordx4 a[160:163], v34, s[36:39], s63 offen offset:0 sc1\n"
    "ds_read_b128 v[212:215], v39 offset:25600\n"
    "v_mfma_f32_16x16x32_bf16 v[76:79], v[236:239], a[104:107], v[76:79]\n"
    "ds_read_b128 v[216:219], v39 offset:28672\n"
    "v_mfma_f32_16x16x32_bf16 v[108:111], v[236:239], a[108:111], v[108:111]\n"
    "ds_read_b128 v[220:223], v39 offset:29696\n"
    "buffer_load_dwordx4 a[164:167], v34, s[36:39], s63 offen offset:256 sc1\n"
    "v_mfma_f32_16x16x32_bf16 v[80:83], v[240:243], a[104:107], v[80:83]\n"
    "v_mfma_f32_16x16x32_bf16 v[112:115], v[240:243], a[108:111], v[112:115]\n"
    "v_mfma_f32_16x16x32_bf16 v[84:87], a[104:107], v[244:247], v[84:87]\n"
    "v_mfma_f32_16x16x32_bf16 v[116:119], a[108:111], v[244:247], v[116:119]\n"
    "buffer_load_dwordx4 a[168:171], v36, s[36:39], s63 offen offset:512 sc1\n"
    "v_mfma_f32_16x16x32_bf16 v[88:91], a[104:107], v[248:251], v[88:91]\n"
    "v_mfma_f32_16x16x32_bf16 v[120:123], a[108:111], v[248:251], v[120:123]\n"
    "v_mfma_f32_16x16x32_bf16 v[92:95], a[104:107], v[252:255], v[92:95]\n"
    "v_mfma_f32_16x16x32_bf16 v[124:127], a[108:111], v[252:255], v[124:127]\n"
    "s_waitcnt lgkmcnt(0)\n"
    "buffer_load_dwordx4 a[172:175], v35, s[36:39], s63 offen offset:0 sc1\n"
    "v_mfma_f32_16x16x32_bf16 v[64:67], v[192:195], a[112:115], v[64:67]\n"
    "ds_read_b128 v[224:227], v39 offset:18432\n"
    "v_mfma_f32_16x16x32_bf16 v[96:99], v[192:195], a[116:119], v[96:99]\n"
    "ds_read_b128 v[228:231], v39 offset:19456\n"
    "v_mfma_f32_16x16x32_bf16 v[68:71], v[196:199], a[112:115], v[68:71]\n"
    "ds_read_b128 v[232:235], v39 offset:22528\n"
    "v_mfma_f32_16x16x32_bf16 v[100:103], v[196:199], a[116:119], v[100:103]\n"
    "ds_read_b128 v[236:239], v39 offset:23552\n"
    "buffer_load_dwordx4 a[176:179], v35, s[36:39], s63 offen offset:256 sc1\n"
    "v_mfma_f32_16x16x32_bf16 v[72:75], v[200:203], a[112:115], v[72:75]\n"
    "ds_read_b128 v[240:243], v39 offset:26624\n"
    "v_mfma_f32_16x16x32_bf16 v[104:107], v[200:203], a[116:119], v[104:107]\n"
    "ds_read_b128 v[244:247], v39 offset:27648\n"
    "v_mfma_f32_16x16x32_bf16 v[76:79], v[204:207], a[112:115], v[76:79]\n"
    "ds_read_b128 v[248:251], v39 offset:30720\n"
    "v_mfma_f32_16x16x32_bf16 v[108:111], v[204:207], a[116:119], v[108:111]\n"
    "ds_read_b128 v[252:255], v39 offset:31744\n"
    "buffer_load_dwordx4 a[180:183], v37, s[36:39], s63 offen offset:512 sc1\n"
    "v_mfma_f32_16x16x32_bf16 v[80:83], v[208:211], a[112:115], v[80:83]\n"
    "ds_read_b64 v[152:153], v14 offset:0\n"
    "v_mfma_f32_16x16x32_bf16 v[112:115], v[208:211], a[116:119], v[112:115]\n"
    "ds_read_b64 v[154:155], v175 offset:0\n"
    "v_mfma_f32_16x16x32_bf16 v[84:87], a[112:115], v[212:215], v[84:87]\n"
    "ds_read_b64 v[156:157], v14 offset:2048\n"
    "v_mfma_f32_16x16x32_bf16 v[116:119], a[116:119], v[212:215], v[116:119]\n"
    "ds_read_b64 v[158:159], v175 offset:2048\n"
    "v_mfma_f32_16x16x32_bf16 v[88:91], a[112:115], v[216:219], v[88:91]\n"
    "s_sub_u32 s63, s63, 0x40400\n"
    "s_cselect_b32 s63, 0x242400, s63\n"
    "ds_read_b64 v[160:161], v14 offset:4096\n"
    "v_mfma_f32_16x16x32_bf16 v[120:123], a[116:119], v[216:219], v[120:123]\n"
    "ds_read_b64 v[162:163], v175 offset:4096\n"
    "buffer_load_dwordx4 a[184:187], v34, s[36:39], s63 offen offset:0 sc1\n"
    "v_mfma_f32_16x16x32_bf16 v[92:95], a[112:115], v[220:223], v[92:95]\n"
    "v_mfma_f32_16x16x32_bf16 v[124:127], a[116:119], v[220:223], v[124:127]\n"
    "s_waitcnt lgkmcnt(6)\n"
    "s_barrier\n"
    "v_mfma_f32_16x16x32_bf16 v[64:67], v[224:227], a[120:123], v[64:67]\n"
    "ds_read_b128 v[192:195], v39 offset:32768\n"
    "buffer_load_dwordx4 a[188:191], v34, s[36:39], s63 offen offset:256 sc1\n"
    "v_mfma_f32_16x16x32_bf16 v[96:99], v[224:227], a[124:127], v[96:99]\n"
    "ds_read_b128 v[196:199], v39 offset:33792\n"
    "v_mfma_f32_16x16x32_bf16 v[68:71], v[228:231], a[120:123], v[68:71]\n"
    "ds_read_b128 v[200:203], v39 offset:36864\n"
    "buffer_load_dwordx4 a[192:195], v36, s[36:39], s63 offen offset:512 sc1\n"
    "v_mfma_f32_16x16x32_bf16 v[100:103], v[228:231], a[124:127], v[100:103]\n"
    "ds_read_b128 v[204:207], v39 offset:37888\n"
    "v_mfma_f32_16x16x32_bf16 v[72:75], v[232:235], a[120:123], v[72:75]\n"
    "ds_read_b128 v[208:211], v39 offset:40960\n"
    "v_mfma_f32_16x16x32_bf16 v[104:107], v[232:235], a[124:127], v[104:107]\n"
    "ds_read_b128 v[212:215], v39 offset:41984\n"
    "v_mfma_f32_16x16x32_bf16 v[76:79], v[236:239], a[120:123], v[76:79]\n"
    "ds_read_b128 v[216:219], v39 offset:45056\n"
    "buffer_load_dwordx4 a[196:199], v35, s[36:39], s63 offen offset:0 sc1\n"
    "v_mfma_f32_16x16x32_bf16 v[108:111], v[236:239], a[124:127], v[108:111]\n"
    "ds_read_b128 v[220:223], v39 offset:46080\n"
    "v_mfma_f32_16x16x32_bf16 v[80:83], v[240:243], a[120:123], v[80:83]\n"
    "v_mfma_f32_16x16x32_bf16 v[112:115], v[240:243], a[124:127], v[112:115]\n"
    "v_mfma_f32_16x16x32_bf16 v[84:87], a[120:123], v[244:247], v[84:87]\n"
    "buffer_load_dwordx4 a[200:203], v35, s[36:39], s63 offen offset:256 sc1\n"
    "v_mfma_f32_16x16x32_bf16 v[116:119], a[124:127], v[244:247], v[116:119]\n"
    "v_mfma_f32_16x16x32_bf16 v[88:91], a[120:123], v[248:251], v[88:91]\n"
    "v_mfma_f32_16x16x32_bf16 v[120:123], a[124:127], v[248:251], v[120:123]\n"
    "v_mfma_f32_16x16x32_bf16 v[92:95], a[120:123], v[252:255], v[92:95]\n"
    "buffer_load_dwordx4 a[204:207], v37, s[36:39], s63 offen offset:512 sc1\n"
    "v_mfma_f32_16x16x32_bf16 v[124:127], a[124:127], v[252:255], v[124:127]\n"
    "s_waitcnt lgkmcnt(0)\n"
    "s_sub_u32 s63, s63, 0x40400\n"
    "s_cselect_b32 s63, 0x242400, s63\n"
    "v_mfma_f32_16x16x32_bf16 v[64:67], v[192:195], a[128:131], v[64:67]\n"
    "ds_read_b128 v[224:227], v39 offset:34816\n"
    "v_mfma_f32_16x16x32_bf16 v[96:99], v[192:195], a[132:135], v[96:99]\n"
    "ds_read_b128 v[228:231], v39 offset:35840\n"
    "v_mfma_f32_16x16x32_bf16 v[68:71], v[196:199], a[128:131], v[68:71]\n"
    "ds_read_b128 v[232:235], v39 offset:38912\n"
    "buffer_load_dwordx4 a[208:211], v34, s[36:39], s63 offen offset:0 sc1\n"
    "v_mfma_f32_16x16x32_bf16 v[100:103], v[196:199], a[132:135], v[100:103]\n"
    "ds_read_b128 v[236:239], v39 offset:39936\n"
    "v_mfma_f32_16x16x32_bf16 v[72:75], v[200:203], a[128:131], v[72:75]\n"
    "ds_read_b128 v[240:243], v39 offset:43008\n"
    "v_mfma_f32_16x16x32_bf16 v[104:107], v[200:203], a[132:135], v[104:107]\n"
    "ds_read_b128 v[244:247], v39 offset:44032\n"
    "v_mfma_f32_16x16x32_bf16 v[76:79], v[204:207], a[128:131], v[76:79]\n"
    "ds_read_b128 v[248:251], v39 offset:47104\n"
    "buffer_load_dwordx4 a[212:215], v34, s[36:39], s63 offen offset:256 sc1\n"
    "v_mfma_f32_16x16x32_bf16 v[108:111], v[204:207], a[132:135], v[108:111]\n"
    "ds_read_b128 v[252:255], v39 offset:48128\n"
    "v_mfma_f32_16x16x32_bf16 v[80:83], v[208:211], a[128:131], v[80:83]\n"
    "v_mfma_f32_16x16x32_bf16 v[112:115], v[208:211], a[132:135], v[112:115]\n"
    "v_mfma_f32_16x16x32_bf16 v[84:87], a[128:131], v[212:215], v[84:87]\n"
    "buffer_load_dwordx4 a[216:219], v36, s[36:39], s63 offen offset:512 sc1\n"
    "v_mfma_f32_16x16x32_bf16 v[116:119], a[132:135], v[212:215], v[116:119]\n"
    "v_mfma_f32_16x16x32_bf16 v[88:91], a[128:131], v[216:219], v[88:91]\n"
    "v_mfma_f32_16x16x32_bf16 v[120:123], a[132:135], v[216:219], v[120:123]\n"
    "v_mfma_f32_16x16x32_bf16 v[92:95], a[128:131], v[220:223], v[92:95]\n"
    "buffer_load_dwordx4 a[220:223], v35, s[36:39], s63 offen offset:0 sc1\n"
    "v_mfma_f32_16x16x32_bf16 v[124:127], a[132:135], v[220:223], v[124:127]\n"
    "s_waitcnt lgkmcnt(0)\n"
    "s_barrier\n"
    "v_mfma_f32_16x16x32_bf16 v[64:67], v[224:227], a[136:139], v[64:67]\n"
    "ds_read_b128 v[192:195], v39 offset:0\n"
    "v_lshlrev_b32 v128, 16, v152\n"
    "v_mfma_f32_16x16x32_bf16 v[96:99], v[224:227], a[140:143], v[96:99]\n"
    "ds_read_b128 v[196:199], v39 offset:1024\n"
    "buffer_load_dwordx4 a[224:227], v35, s[36:39], s63 offen offset:256 sc1\n"
    "v_mfma_f32_16x16x32_bf16 v[68:71], v[228:231], a[136:139], v[68:71]\n"
    "ds_read_b128 v[200:203], v39 offset:4096\n"
    "v_and_b32 v129, 0xffff0000, v152\n"
    "v_mfma_f32_16x16x32_bf16 v[100:103], v[228:231], a[140:143], v[100:103]\n"
    "ds_read_b128 v[204:207], v39 offset:5120\n"
    "v_lshlrev_b32 v130, 16, v153\n"
    "v_mfma_f32_16x16x32_bf16 v[72:75], v[232:235], a[136:139], v[72:75]\n"
    "ds_read_b128 v[208:211], v39 offset:8192\n"
    "v_and_b32 v131, 0xffff0000, v153\n"
    "v_mfma_f32_16x16x32_bf16 v[104:107], v[232:235], a[140:143], v[104:107]\n"
    "ds_read_b128 v[212:215], v39 offset:9216\n"
    "buffer_load_dwordx4 a[228:231], v37, s[36:39], s63 offen offset:512 sc1\n"
    "v_mfma_f32_16x16x32_bf16 v[76:79], v[236:239], a[136:139], v[76:79]\n"
    "ds_read_b128 v[216:219], v39 offset:12288\n"
    "s_add_u32 m0, s56, 0x4000\n"
    "v_mfma_f32_16x16x32_bf16 v[108:111], v[236:239], a[140:143], v[108:111]\n"
    "ds_read_b128 v[220:223], v39 offset:13312\n"
    "s_waitcnt vmcnt(20)\n"
    "v_fma_f32 v128, v128, v177, v176\n"
    "v_mfma_f32_16x16x32_bf16 v[80:83], v[240:243], a[136:139], v[80:83]\n"
    "s_sub_u32 s61, s61, 0x14000\n"
    "s_cselect_b32 s61, 0xb4000, s61\n"
    "v_mfma_f32_16x16x32_bf16 v[112:115], v[240:243], a[140:143], v[112:115]\n"
    "v_fma_f32 v129, v129, v177, v176\n"
    "buffer_load_dwordx4 v20, s[20:23], s61 offen offset:0 lds\n"
    "v_mfma_f32_16x16x32_bf16 v[84:87], a[136:139], v[244:247], v[84:87]\n"
    "v_fma_f32 v130, v130, v177, v176\n"
    "v_fma_f32 v131, v131, v177, v176\n"
    "v_mfma_f32_16x16x32_bf16 v[116:119], a[140:143], v[244:247], v[116:119]\n"
    "v_lshlrev_b32 v140, 16, v154\n"
    "v_and_b32 v141, 0xffff0000, v154\n"
    "v_mfma_f32_16x16x32_bf16 v[88:91], a[136:139], v[248:251], v[88:91]\n"
    "v_lshlrev_b32 v142, 16, v155\n"
    "v_and_b32 v143, 0xffff0000, v155\n"
    "v_mfma_f32_16x16x32_bf16 v[120:123], a[140:143], v[248:251], v[120:123]\n"
    "v_fma_f32 v140, v140, v177, v176\n"
    "buffer_load_dwordx4 v21, s[20:23], s61 offen offset:1024 lds\n"
    "v_mfma_f32_16x16x32_bf16 v[92:95], a[136:139], v[252:255], v[92:95]\n"
    "s_add_u32 m0, s56, 0x6000\n"
    "v_fma_f32 v141, v141, v177, v176\n"
    "v_mfma_f32_16x16x32_bf16 v[124:127], a[140:143], v[252:255], v[124:127]\n"
    "s_waitcnt lgkmcnt(0)\n"
    "s_sub_u32 s62, s62, 0x14000\n"
    "s_cselect_b32 s62, 0xb4000, s62\n"
    "v_mfma_f32_16x16x32_bf16 v[64:67], v[192:195], a[144:147], v[64:67]\n"
    "ds_read_b128 v[224:227], v39 offset:2048\n"
    "v_fma_f32 v142, v142, v177, v176\n"
    "v_mfma_f32_16x16x32_bf16 v[96:99], v[192:195], a[148:151], v[96:99]\n"
    "ds_read_b128 v[228:231], v39 offset:3072\n"
    "buffer_load_dwordx4 v20, s[20:23], s62 offen offset:0 lds\n"
    "v_mfma_f32_16x16x32_bf16 v[68:71], v[196:199], a[144:147], v[68:71]\n"
    "ds_read_b128 v[232:235], v39 offset:6144\n"
    "v_fma_f32 v143, v143, v177, v176\n"
    "v_mfma_f32_16x16x32_bf16 v[100:103], v[196:199], a[148:151], v[100:103]\n"
    "ds_read_b128 v[236:239], v39 offset:7168\n"
    "v_lshlrev_b32 v132, 16, v156\n"
    "v_mfma_f32_16x16x32_bf16 v[72:75], v[200:203], a[144:147], v[72:75]\n"
    "ds_read_b128 v[240:243], v39 offset:10240\n"
    "v_and_b32 v133, 0xffff0000, v156\n"
    "v_mfma_f32_16x16x32_bf16 v[104:107], v[200:203], a[148:151], v[104:107]\n"
    "ds_read_b128 v[244:247], v39 offset:11264\n"
    "buffer_load_dwordx4 v21, s[20:23], s62 offen offset:1024 lds\n"
    "v_mfma_f32_16x16x32_bf16 v[76:79], v[204:207], a[144:147], v[76:79]\n"
    "ds_read_b128 v[248:251], v39 offset:14336\n"
    "v_lshlrev_b32 v134, 16, v157\n"
    "v_mfma_f32_16x16x32_bf16 v[108:111], v[204:207], a[148:151], v[108:111]\n"
    "ds_read_b128 v[252:255], v39 offset:15360\n"
    "v_and_b32 v135, 0xffff0000, v157\n"
    "v_mfma_f32_16x16x32_bf16 v[80:83], v[208:211], a[144:147], v[80:83]\n"
    "s_sub_u32 s63, s63, 0x40400\n"
    "s_cselect_b32 s63, 0x242400, s63\n"
    "v_mfma_f32_16x16x32_bf16 v[112:115], v[208:211], a[148:151], v[112:115]\n"
    "s_waitcnt vmcnt(23)\n"
    "v_fma_f32 v132, v132, v179, v178\n"
    "buffer_load_dwordx4 a[232:235], v34, s[36:39], s63 offen offset:0 sc1\n"
    "v_mfma_f32_16x16x32_bf16 v[84:87], a[144:147], v[212:215], v[84:87]\n"
    "v_fma_f32 v133, v133, v179, v178\n"
    "v_fma_f32 v134, v134, v179, v178\n"
    "v_mfma_f32_16x16x32_bf16 v[116:119], a[148:151], v[212:215], v[116:119]\n"
    "v_fma_f32 v135, v135, v179, v178\n"
    "v_lshlrev_b32 v144, 16, v158\n"
    "v_mfma_f32_16x16x32_bf16 v[88:91], a[144:147], v[216:219], v[88:91]\n"
    "v_and_b32 v145, 0xffff0000, v158\n"
    "v_lshlrev_b32 v146, 16, v159\n"
    "v_mfma_f32_16x16x32_bf16 v[120:123], a[148:151], v[216:219], v[120:123]\n"
    "v_and_b32 v147, 0xffff0000, v159\n"
    "buffer_load_dwordx4 a[236:239], v34, s[36:39], s63 offen offset:256 sc1\n"
    "v_mfma_f32_16x16x32_bf16 v[92:95], a[144:147], v[220:223], v[92:95]\n"
    "v_fma_f32 v144, v144, v179, v178\n"
    "v_fma_f32 v145, v145, v179, v178\n"
    "v_mfma_f32_16x16x32_bf16 v[124:127], a[148:151], v[220:223], v[124:127]\n"
    "s_waitcnt lgkmcnt(0)\n"
    "s_barrier\n"
    "v_mfma_f32_16x16x32_bf16 v[64:67], v[224:227], a[152:155], v[64:67]\n"
    "ds_read_b128 v[192:195], v40 offset:0\n"
    "v_fma_f32 v146, v146, v179, v178\n"
    "v_mfma_f32_16x16x32_bf16 v[96:99], v[224:227], a[156:159], v[96:99]\n"
    "ds_read_b128 v[196:199], v40 offset:1024\n"
    "buffer_load_dwordx4 a[240:243], v36, s[36:39], s63 offen offset:512 sc1\n"
    "v_mfma_f32_16x16x32_bf16 v[68:71], v[228:231], a[152:155], v[68:71]\n"
    "ds_read_b128 v[200:203], v40 offset:2048\n"
    "v_fma_f32 v147, v147, v179, v178\n"
    "v_mfma_f32_16x16x32_bf16 v[100:103], v[228:231], a[156:159], v[100:103]\n"
    "ds_read_b128 v[204:207], v40 offset:3072\n"
    "v_lshlrev_b32 v136, 16, v160\n"
    "v_mfma_f32_16x16x32_bf16 v[72:75], v[232:235], a[152:155], v[72:75]\n"
    "ds_read_b128 v[208:211], v40 offset:4096\n"
    "v_and_b32 v137, 0xffff0000, v160\n"
    "v_mfma_f32_16x16x32_bf16 v[104:107], v[232:235], a[156:159], v[104:107]\n"
    "ds_read_b128 v[212:215], v40 offset:5120\n"
    "buffer_load_dwordx4 a[244:247], v35, s[36:39], s63 offen offset:0 sc1\n"
    "v_mfma_f32_16x16x32_bf16 v[76:79], v[236:239], a[152:155], v[76:79]\n"
    "ds_read_b128 v[216:219], v40 offset:6144\n"
    "v_lshlrev_b32 v138, 16, v161\n"
    "v_mfma_f32_16x16x32_bf16 v[108:111], v[236:239], a[156:159], v[108:111]\n"
    "ds_read_b128 v[220:223], v40 offset:7168\n"
    "v_and_b32 v139, 0xffff0000, v161\n"
    "v_mfma_f32_16x16x32_bf16 v[80:83], v[240:243], a[152:155], v[80:83]\n"
    "s_waitcnt vmcnt(26)\n"
    "v_fma_f32 v136, v136, v181, v180\n"
    "v_fma_f32 v137, v137, v181, v180\n"
    "v_mfma_f32_16x16x32_bf16 v[112:115], v[240:243], a[156:159], v[112:115]\n"
    "buffer_load_dwordx4 a[248:251], v35, s[36:39], s63 offen offset:256 sc1\n"
    "v_fma_f32 v138, v138, v181, v180\n"
    "v_mfma_f32_16x16x32_bf16 v[84:87], a[152:155], v[244:247], v[84:87]\n"
    "v_fma_f32 v139, v139, v181, v180\n"
    "v_lshlrev_b32 v148, 16, v162\n"
    "v_mfma_f32_16x16x32_bf16 v[116:119], a[156:159], v[244:247], v[116:119]\n"
    "v_and_b32 v149, 0xffff0000, v162\n"
    "v_lshlrev_b32 v150, 16, v163\n"
    "v_mfma_f32_16x16x32_bf16 v[88:91], a[152:155], v[248:251], v[88:91]\n"
    "v_and_b32 v151, 0xffff0000, v163\n"
    "v_fma_f32 v148, v148, v181, v180\n"
    "v_mfma_f32_16x16x32_bf16 v[120:123], a[156:159], v[248:251], v[120:123]\n"
    "buffer_load_dwordx4 a[252:255], v37, s[36:39], s63 offen offset:512 sc1\n"
    "v_fma_f32 v149, v149, v181, v180\n"
    "v_mfma_f32_16x16x32_bf16 v[92:95], a[152:155], v[252:255], v[92:95]\n"
    "s_sub_u32 s63, s63, 0x40400\n"
    "s_cselect_b32 s63, 0x242400, s63\n"
    "v_mfma_f32_16x16x32_bf16 v[124:127], a[156:159], v[252:255], v[124:127]\n"
    "s_waitcnt lgkmcnt(0)\n"
    "v_fma_f32 v150, v150, v181, v180\n"
    "v_fma_f32 v151, v151, v181, v180\n"
    "v_mfma_f32_16x16x32_bf16 v[64:67], v[192:195], v[184:187], v[64:67]\n"
    "v_mfma_f32_16x16x32_bf16 v[96:99], v[192:195], v[188:191], v[96:99]\n"
    "buffer_load_dwordx4 a[0:3], v34, s[36:39], s63 offen offset:0 sc1\n"
    "v_mfma_f32_16x16x32_bf16 v[68:71], v[196:199], v[184:187], v[68:71]\n"
    "v_mfma_f32_16x16x32_bf16 v[100:103], v[196:199], v[188:191], v[100:103]\n"
    "v_mfma_f32_16x16x32_bf16 v[72:75], v[200:203], v[184:187], v[72:75]\n"
    "v_mfma_f32_16x16x32_bf16 v[104:107], v[200:203], v[188:191], v[104:107]\n"
    "buffer_load_dwordx4 a[4:7], v34, s[36:39], s63 offen offset:256 sc1\n"
    "v_mfma_f32_16x16x32_bf16 v[76:79], v[204:207], v[184:187], v[76:79]\n"
    "v_mfma_f32_16x16x32_bf16 v[108:111], v[204:207], v[188:191], v[108:111]\n"
    "v_mfma_f32_16x16x32_bf16 v[80:83], v[208:211], v[184:187], v[80:83]\n"
    "v_mfma_f32_16x16x32_bf16 v[112:115], v[208:211], v[188:191], v[112:115]\n"
    "buffer_load_dwordx4 a[8:11], v36, s[36:39], s63 offen offset:512 sc1\n"
    "v_mfma_f32_16x16x32_bf16 v[84:87], v[184:187], v[212:215], v[84:87]\n"
    "v_mfma_f32_16x16x32_bf16 v[116:119], v[188:191], v[212:215], v[116:119]\n"
    "v_mfma_f32_16x16x32_bf16 v[88:91], v[184:187], v[216:219], v[88:91]\n"
    "v_mfma_f32_16x16x32_bf16 v[120:123], v[188:191], v[216:219], v[120:123]\n"
    "buffer_load_dwordx4 a[12:15], v35, s[36:39], s63 offen offset:0 sc1\n"
    "v_mfma_f32_16x16x32_bf16 v[92:95], v[184:187], v[220:223], v[92:95]\n"
    "v_mfma_f32_16x16x32_bf16 v[124:127], v[188:191], v[220:223], v[124:127]\n"
    "s_nop 1\n"
    "v_pk_mul_f32 v[84:85], v[84:85], v[164:165] op_sel:[0,0] op_sel_hi:[1,1]\n"
    "v_pk_mul_f32 v[86:87], v[86:87], v[166:167] op_sel:[0,0] op_sel_hi:[1,1]\n"
    "v_pk_mul_f32 v[88:89], v[88:89], v[164:165] op_sel:[0,0] op_sel_hi:[1,1]\n"
    "v_pk_mul_f32 v[90:91], v[90:91], v[166:167] op_sel:[0,0] op_sel_hi:[1,1]\n"
    "s_nop 0\n"
    "v_pk_mul_f32 v[92:93], v[92:93], v[164:165] op_sel:[0,0] op_sel_hi:[1,1]\n"
    "v_pk_mul_f32 v[94:95], v[94:95], v[166:167] op_sel:[0,0] op_sel_hi:[1,1]\n"
    "v_pk_mul_f32 v[116:117], v[116:117], v[168:169] op_sel:[0,0] op_sel_hi:[1,1]\n"
    "v_pk_mul_f32 v[118:119], v[118:119], v[170:171] op_sel:[0,0] op_sel_hi:[1,1]\n"
    "v_pk_mul_f32 v[120:121], v[120:121], v[168:169] op_sel:[0,0] op_sel_hi:[1,1]\n"
    "v_pk_mul_f32 v[122:123], v[122:123], v[170:171] op_sel:[0,0] op_sel_hi:[1,1]\n"
    "v_pk_mul_f32 v[124:125], v[124:125], v[168:169] op_sel:[0,0] op_sel_hi:[1,1]\n"
    "v_pk_mul_f32 v[126:127], v[126:127], v[170:171] op_sel:[0,0] op_sel_hi:[1,1]\n"
    "v_mul_f32 v58, 0x3ea52b5c, v53\n"
    "s_mov_b32 s90, -1\n"
    "s_mov_b32 s91, 0\n"
    "v_cndmask_b32 v58, v53, v58, s[90:91]\n"
    "buffer_load_dwordx4 a[16:19], v35, s[36:39], s63 offen offset:256 sc1\n"
    "buffer_load_dwordx4 a[20:23], v37, s[36:39], s63 offen offset:512 sc1\n"
    "v_cvt_pk_bf16_f32 v234, v116, v117\n"
    "v_cvt_pk_bf16_f32 v235, v118, v119\n"
    "v_cvt_pk_bf16_f32 v240, v120, v121\n"
    "v_cvt_pk_bf16_f32 v241, v122, v123\n"
    "v_cvt_pk_bf16_f32 v246, v124, v125\n"
    "v_cvt_pk_bf16_f32 v247, v126, v127\n"
    "v_pk_mul_f32 v[104:105], v[104:105], v[58:59] op_sel:[0,0] op_sel_hi:[1,0]\n"
    "v_pk_mul_f32 v[106:107], v[106:107], v[58:59] op_sel:[0,0] op_sel_hi:[1,0]\n"
    "v_pk_mul_f32 v[108:109], v[108:109], v[52:53] op_sel:[0,1] op_sel_hi:[1,1]\n"
    "v_pk_mul_f32 v[110:111], v[110:111], v[52:53] op_sel:[0,1] op_sel_hi:[1,1]\n"
    "v_pk_mul_f32 v[112:113], v[112:113], v[52:53] op_sel:[0,1] op_sel_hi:[1,1]\n"
    "v_pk_mul_f32 v[114:115], v[114:115], v[52:53] op_sel:[0,1] op_sel_hi:[1,1]\n"
    "v_cvt_pk_bf16_f32 v228, v104, v105\n"
    "v_cvt_pk_bf16_f32 v229, v106, v107\n"
    "v_mov_b32 v190, v228\n"
    "v_mov_b32 v191, v229\n"
    "v_cvt_pk_bf16_f32 v224, v108, v109\n"
    "v_cvt_pk_bf16_f32 v225, v110, v111\n"
    "v_cvt_pk_bf16_f32 v226, v112, v113\n"
    "v_cvt_pk_bf16_f32 v227, v114, v115\n"
    "v_permlane32_swap_b32 v190, v228\n"
    "v_permlane32_swap_b32 v191, v229\n"
    "v_mov_b32 v104, v228\n"
    "v_mov_b32 v105, v229\n"
    "v_mov_b32 v106, v234\n"
    "v_mov_b32 v107, v235\n"
    "v_mov_b32 v108, v240\n"
    "v_mov_b32 v109, v241\n"
    "s_add_u32 m0, s56, 0x14000\n"
    "s_mul_i32 s59, s74, 0x36000\n"
    "buffer_load_dwordx4 v22, s[24:27], s59 offen offset:0 lds\n"
    "buffer_load_dwordx4 v23, s[24:27], s59 offen offset:1024 lds\n"
    "v_mov_b32 v110, v246\n"
    "v_mov_b32 v111, v247\n"
    "s_barrier\n"
    "ds_read_b64_tr_b16 v[192:193], v26 offset:0\n"
    "ds_read_b64_tr_b16 v[194:195], v26 offset:512\n"
    "ds_read_b64_tr_b16 v[196:197], v27 offset:0\n"
    "ds_read_b64_tr_b16 v[198:199], v27 offset:512\n"
    "ds_read_b64_tr_b16 v[200:201], v28 offset:0\n"
    "ds_read_b64_tr_b16 v[202:203], v28 offset:512\n"
    "ds_read_b64_tr_b16 v[204:205], v29 offset:0\n"
    "ds_read_b64_tr_b16 v[206:207], v29 offset:512\n"
    "s_waitcnt vmcnt(33) lgkmcnt(4)\n"
    "v_mfma_f32_16x16x32_bf16 v[128:131], v[192:195], a[160:163], v[128:131]\n"
    "ds_read_b64_tr_b16 v[208:209], v26 offset:1024\n"
    "s_add_u32 m0, s56, 0x16000\n"
    "v_mfma_f32_16x16x32_bf16 v[132:135], v[192:195], a[164:167], v[132:135]\n"
    "ds_read_b64_tr_b16 v[210:211], v26 offset:1536\n"
    "v_mfma_f32_16x16x32_bf16 v[136:139], v[192:195], a[168:171], v[136:139]\n"
    "ds_read_b64_tr_b16 v[212:213], v27 offset:1024\n"
    "s_sub_u32 s59, s59, 0x36000\n"
    "s_cselect_b32 s59, 0x1e6000, s59\n"
    "v_mfma_f32_16x16x32_bf16 v[140:143], v[196:199], a[160:163], v[140:143]\n"
    "ds_read_b64_tr_b16 v[214:215], v27 offset:1536\n"
    "buffer_load_dwordx4 v22, s[24:27], s59 offen offset:0 lds\n"
    "v_mfma_f32_16x16x32_bf16 v[144:147], v[196:199], a[164:167], v[144:147]\n"
    "ds_write_b128 v16, v[224:227] offset:3072\n"
    "v_mfma_f32_16x16x32_bf16 v[148:151], v[196:199], a[168:171], v[148:151]\n"
    "buffer_store_dwordx4 v[224:227], v38, s[36:39], s95 offen offset:0 sc1\n"
    "ds_write_b128 v16, v[104:107] offset:4096\n"
    "buffer_store_dwordx4 v[104:107], v38, s[36:39], s95 offen offset:1024 sc1\n"
    "ds_write_b128 v16, v[108:111] offset:5120\n"
    "buffer_store_dwordx4 v[108:111], v38, s[36:39], s95 offen offset:2048 sc1\n"
    "s_waitcnt lgkmcnt(7)\n"
    "v_mfma_f32_16x16x32_bf16 v[164:167], v[200:203], a[160:163], 0\n"
    "ds_read_b64_tr_b16 v[216:217], v28 offset:1024\n"
    "v_mfma_f32_16x16x32_bf16 v[168:171], v[200:203], a[164:167], 0\n"
    "ds_read_b64_tr_b16 v[218:219], v28 offset:1536\n"
    "v_mfma_f32_16x16x32_bf16 v[172:175], v[200:203], a[168:171], 0\n"
    "ds_read_b64_tr_b16 v[220:221], v29 offset:1024\n"
    "v_mfma_f32_16x16x32_bf16 v[176:179], v[204:207], a[160:163], 0\n"
    "ds_read_b64_tr_b16 v[222:223], v29 offset:1536\n"
    "v_mfma_f32_16x16x32_bf16 v[180:183], v[204:207], a[164:167], 0\n"
    "v_mfma_f32_16x16x32_bf16 v[184:187], v[204:207], a[168:171], 0\n"
    "s_waitcnt vmcnt(34) lgkmcnt(7)\n"
    "v_mfma_f32_16x16x32_bf16 v[128:131], v[208:211], a[172:175], v[128:131]\n"
    "ds_read_b64_tr_b16 v[192:193], v30 offset:49152\n"
    "v_mfma_f32_16x16x32_bf16 v[132:135], v[208:211], a[176:179], v[132:135]\n"
    "ds_read_b64_tr_b16 v[194:195], v30 offset:49664\n"
    "v_mfma_f32_16x16x32_bf16 v[136:139], v[208:211], a[180:183], v[136:139]\n"
    "ds_read_b64_tr_b16 v[196:197], v31 offset:49152\n"
    "buffer_load_dwordx4 v23, s[24:27], s59 offen offset:1024 lds\n"
    "v_mfma_f32_16x16x32_bf16 v[140:143], v[212:215], a[172:175], v[140:143]\n"
    "ds_read_b64_tr_b16 v[198:199], v31 offset:49664\n"
    "s_add_u32 m0, s56, 0x18000\n"
    "v_mfma_f32_16x16x32_bf16 v[144:147], v[212:215], a[176:179], v[144:147]\n"
    "s_sub_u32 s59, s59, 0x36000\n"
    "s_cselect_b32 s59, 0x1e6000, s59\n"
    "v_mfma_f32_16x16x32_bf16 v[148:151], v[212:215], a[180:183], v[148:151]\n"
    "s_waitcnt lgkmcnt(4)\n"
    "v_mfma_f32_16x16x32_bf16 v[164:167], v[216:219], a[172:175], v[164:167]\n"
    "ds_read_b64_tr_b16 v[200:201], v32 offset:49152\n"
    "buffer_load_dwordx4 v22, s[24:27], s59 offen offset:0 lds\n"
    "v_mfma_f32_16x16x32_bf16 v[168:171], v[216:219], a[176:179], v[168:171]\n"
    "ds_read_b64_tr_b16 v[202:203], v32 offset:49664\n"
    "v_mfma_f32_16x16x32_bf16 v[172:175], v[216:219], a[180:183], v[172:175]\n"
    "ds_read_b64_tr_b16 v[204:205], v33 offset:49152\n"
    "v_mfma_f32_16x16x32_bf16 v[176:179], v[220:223], a[172:175], v[176:179]\n"
    "ds_read_b64_tr_b16 v[206:207], v33 offset:49664\n"
    "v_mfma_f32_16x16x32_bf16 v[180:183], v[220:223], a[176:179], v[180:183]\n"
    "buffer_load_dwordx4 v23, s[24:27], s59 offen offset:1024 lds\n"
    "s_add_u32 m0, s56, 0x1a000\n"
    "v_mfma_f32_16x16x32_bf16 v[184:187], v[220:223], a[180:183], v[184:187]\n"
    "s_waitcnt vmcnt(34) lgkmcnt(4)\n"
    "s_sub_u32 s59, s59, 0x36000\n"
    "s_cselect_b32 s59, 0x1e6000, s59\n"
    "v_mfma_f32_16x16x32_bf16 v[128:131], v[192:195], a[184:187], v[128:131]\n"
    "ds_read_b64_tr_b16 v[208:209], v30 offset:50176\n"
    "v_mfma_f32_16x16x32_bf16 v[132:135], v[192:195], a[188:191], v[132:135]\n"
    "ds_read_b64_tr_b16 v[210:211], v30 offset:50688\n"
    "v_mfma_f32_16x16x32_bf16 v[136:139], v[192:195], a[192:195], v[136:139]\n"
    "ds_read_b64_tr_b16 v[212:213], v31 offset:50176\n"
    "buffer_load_dwordx4 v22, s[24:27], s59 offen offset:0 lds\n"
    "v_mfma_f32_16x16x32_bf16 v[140:143], v[196:199], a[184:187], v[140:143]\n"
    "ds_read_b64_tr_b16 v[214:215], v31 offset:50688\n"
    "v_mfma_f32_16x16x32_bf16 v[144:147], v[196:199], a[188:191], v[144:147]\n"
    "v_mfma_f32_16x16x32_bf16 v[148:151], v[196:199], a[192:195], v[148:151]\n"
    "s_waitcnt lgkmcnt(4)\n"
    "v_mfma_f32_16x16x32_bf16 v[164:167], v[200:203], a[184:187], v[164:167]\n"
    "ds_read_b64_tr_b16 v[216:217], v32 offset:50176\n"
    "buffer_load_dwordx4 v23, s[24:27], s59 offen offset:1024 lds\n"
    "v_mfma_f32_16x16x32_bf16 v[168:171], v[200:203], a[188:191], v[168:171]\n"
    "ds_read_b64_tr_b16 v[218:219], v32 offset:50688\n"
    "s_add_u32 m0, s56, 0x10000\n"
    "v_mfma_f32_16x16x32_bf16 v[172:175], v[200:203], a[192:195], v[172:175]\n"
    "ds_read_b64_tr_b16 v[220:221], v33 offset:50176\n"
    "v_mfma_f32_16x16x32_bf16 v[176:179], v[204:207], a[184:187], v[176:179]\n"
    "ds_read_b64_tr_b16 v[222:223], v33 offset:50688\n"
    "v_mfma_f32_16x16x32_bf16 v[180:183], v[204:207], a[188:191], v[180:183]\n"
    "s_sub_u32 s59, s59, 0x36000\n"
    "s_cselect_b32 s59, 0x1e6000, s59\n"
    "v_mfma_f32_16x16x32_bf16 v[184:187], v[204:207], a[192:195], v[184:187]\n"
    "s_waitcnt vmcnt(5) lgkmcnt(4)\n"
    "s_barrier\n"
    "v_mfma_f32_16x16x32_bf16 v[128:131], v[208:211], a[196:199], v[128:131]\n"
    "buffer_store_dwordx2 v[4:5], v9, s[36:39], s85 offen offset:0 sc1\n"
    "buffer_load_dwordx4 v22, s[24:27], s59 offen offset:0 lds\n"
    "v_mfma_f32_16x16x32_bf16 v[132:135], v[208:211], a[200:203], v[132:135]\n"
    "ds_read_b64_tr_b16 v[192:193], v26 offset:32768\n"
    "v_mfma_f32_16x16x32_bf16 v[136:139], v[208:211], a[204:207], v[136:139]\n"
    "ds_read_b64_tr_b16 v[194:195], v26 offset:33280\n"
    "v_mfma_f32_16x16x32_bf16 v[140:143], v[212:215], a[196:199], v[140:143]\n"
    "ds_read_b64_tr_b16 v[196:197], v27 offset:32768\n"
    "buffer_load_dwordx4 v23, s[24:27], s59 offen offset:1024 lds\n"
    "v_mfma_f32_16x16x32_bf16 v[144:147], v[212:215], a[200:203], v[144:147]\n"
    "ds_read_b64_tr_b16 v[198:199], v27 offset:33280\n"
    "s_add_u32 m0, s56, 0x12000\n"
    "v_mfma_f32_16x16x32_bf16 v[148:151], v[212:215], a[204:207], v[148:151]\n"
    "ds_read_b128 v[248:251], v16 offset:0\n"
    "ds_read_b64 v[252:253], v16 offset:1024\n"
    "ds_read_b64 v[230:231], v16 offset:1032\n"
    "ds_read_b64 v[236:237], v16 offset:2048\n"
    "ds_read_b64 v[242:243], v16 offset:2056\n"
    "s_waitcnt lgkmcnt(9)\n"
    "v_mfma_f32_16x16x32_bf16 v[164:167], v[216:219], a[196:199], v[164:167]\n"
    "ds_read_b64_tr_b16 v[200:201], v28 offset:32768\n"
    "v_mfma_f32_16x16x32_bf16 v[168:171], v[216:219], a[200:203], v[168:171]\n"
    "ds_read_b64_tr_b16 v[202:203], v28 offset:33280\n"
    "s_sub_u32 s59, s59, 0x36000\n"
    "s_cselect_b32 s59, 0x1e6000, s59\n"
    "v_mfma_f32_16x16x32_bf16 v[172:175], v[216:219], a[204:207], v[172:175]\n"
    "ds_read_b64_tr_b16 v[204:205], v29 offset:32768\n"
    "buffer_load_dwordx4 v22, s[24:27], s59 offen offset:0 lds\n"
    "v_mfma_f32_16x16x32_bf16 v[176:179], v[220:223], a[196:199], v[176:179]\n"
    "ds_read_b64_tr_b16 v[206:207], v29 offset:33280\n"
    "v_mfma_f32_16x16x32_bf16 v[180:183], v[220:223], a[200:203], v[180:183]\n"
    "buffer_load_dwordx4 v23, s[24:27], s59 offen offset:1024 lds\n"
    "s_add_u32 m0, s56, 0\n"
    "v_mfma_f32_16x16x32_bf16 v[184:187], v[220:223], a[204:207], v[184:187]\n"
    "s_waitcnt lgkmcnt(9)\n"
    "s_sub_u32 s59, s59, 0x36000\n"
    "s_cselect_b32 s59, 0x1e6000, s59\n"
    "v_mfma_f32_16x16x32_bf16 v[128:131], v[192:195], a[208:211], v[128:131]\n"
    "ds_read_b64_tr_b16 v[208:209], v26 offset:33792\n"
    "v_mfma_f32_16x16x32_bf16 v[132:135], v[192:195], a[212:215], v[132:135]\n"
    "ds_read_b64_tr_b16 v[210:211], v26 offset:34304\n"
    "v_mfma_f32_16x16x32_bf16 v[136:139], v[192:195], a[216:219], v[136:139]\n"
    "ds_read_b64_tr_b16 v[212:213], v27 offset:33792\n"
    "buffer_load_dwordx4 v22, s[24:27], s59 offen offset:0 lds\n"
    "v_mfma_f32_16x16x32_bf16 v[140:143], v[196:199], a[208:211], v[140:143]\n"
    "ds_read_b64_tr_b16 v[214:215], v27 offset:34304\n"
    "v_mfma_f32_16x16x32_bf16 v[144:147], v[196:199], a[212:215], v[144:147]\n"
    "v_mfma_f32_16x16x32_bf16 v[148:151], v[196:199], a[216:219], v[148:151]\n"
    "s_waitcnt lgkmcnt(4)\n"
    "v_mfma_f32_16x16x32_bf16 v[164:167], v[200:203], a[208:211], v[164:167]\n"
    "ds_read_b64_tr_b16 v[216:217], v28 offset:33792\n"
    "buffer_load_dwordx4 v23, s[24:27], s59 offen offset:1024 lds\n"
    "v_mfma_f32_16x16x32_bf16 v[168:171], v[200:203], a[212:215], v[168:171]\n"
    "ds_read_b64_tr_b16 v[218:219], v28 offset:34304\n"
    "s_add_u32 m0, s56, 0x2000\n"
    "v_mfma_f32_16x16x32_bf16 v[172:175], v[200:203], a[216:219], v[172:175]\n"
    "ds_read_b64_tr_b16 v[220:221], v29 offset:33792\n"
    "v_mfma_f32_16x16x32_bf16 v[176:179], v[204:207], a[208:211], v[176:179]\n"
    "ds_read_b64_tr_b16 v[222:223], v29 offset:34304\n"
    "v_mfma_f32_16x16x32_bf16 v[180:183], v[204:207], a[212:215], v[180:183]\n"
    "s_sub_u32 s59, s59, 0x36000\n"
    "s_cselect_b32 s59, 0x1e6000, s59\n"
    "v_mfma_f32_16x16x32_bf16 v[184:187], v[204:207], a[216:219], v[184:187]\n"
    "s_waitcnt lgkmcnt(0)\n"
    "s_barrier\n"
    "v_mfma_f32_16x16x32_bf16 v[128:131], v[208:211], a[220:223], v[128:131]\n"
    "buffer_load_dwordx4 v22, s[24:27], s59 offen offset:0 lds\n"
    "ds_read_b64_tr_b16 v[192:193], v28 offset:49152\n"
    "v_mfma_f32_16x16x32_bf16 v[132:135], v[208:211], a[224:227], v[132:135]\n"
    "ds_read_b64_tr_b16 v[194:195], v28 offset:49664\n"
    "v_mfma_f32_16x16x32_bf16 v[136:139], v[208:211], a[228:231], v[136:139]\n"
    "ds_read_b64_tr_b16 v[196:197], v29 offset:49152\n"
    "buffer_load_dwordx4 v23, s[24:27], s59 offen offset:1024 lds\n"
    "v_mfma_f32_16x16x32_bf16 v[140:143], v[212:215], a[220:223], v[140:143]\n"
    "ds_read_b64_tr_b16 v[198:199], v29 offset:49664\n"
    "s_add_u32 m0, s56, 0x1c000\n"
    "v_mfma_f32_16x16x32_bf16 v[144:147], v[212:215], a[224:227], v[144:147]\n"
    "s_sub_u32 s59, s59, 0x36000\n"
    "s_cselect_b32 s59, 0x1e6000, s59\n"
    "v_mfma_f32_16x16x32_bf16 v[148:151], v[212:215], a[228:231], v[148:151]\n"
    "v_mfma_f32_16x16x32_bf16 v[164:167], v[216:219], a[220:223], v[164:167]\n"
    "ds_read_b64_tr_b16 v[200:201], v28 offset:50176\n"
    "buffer_load_dwordx4 v22, s[24:27], s59 offen offset:0 lds\n"
    "v_mfma_f32_16x16x32_bf16 v[168:171], v[216:219], a[224:227], v[168:171]\n"
    "ds_read_b64_tr_b16 v[202:203], v28 offset:50688\n"
    "v_mfma_f32_16x16x32_bf16 v[172:175], v[216:219], a[228:231], v[172:175]\n"
    "ds_read_b64_tr_b16 v[204:205], v29 offset:50176\n"
    "v_mfma_f32_16x16x32_bf16 v[176:179], v[220:223], a[220:223], v[176:179]\n"
    "ds_read_b64_tr_b16 v[206:207], v29 offset:50688\n"
    "v_mfma_f32_16x16x32_bf16 v[180:183], v[220:223], a[224:227], v[180:183]\n"
    "buffer_load_dwordx4 v23, s[24:27], s59 offen offset:1024 lds\n"
    "s_add_u32 m0, s56, 0x1e000\n"
    "v_mfma_f32_16x16x32_bf16 v[184:187], v[220:223], a[228:231], v[184:187]\n"
    "s_waitcnt lgkmcnt(4)\n"
    "s_sub_u32 s59, s59, 0x36000\n"
    "s_cselect_b32 s59, 0x1e6000, s59\n"
    "v_mfma_f32_16x16x32_bf16 v[164:167], v[192:195], a[232:235], v[164:167]\n"
    "ds_read_b64_tr_b16 v[208:209], v28 offset:16384\n"
    "v_mfma_f32_16x16x32_bf16 v[168:171], v[192:195], a[236:239], v[168:171]\n"
    "ds_read_b64_tr_b16 v[210:211], v28 offset:16896\n"
    "v_mfma_f32_16x16x32_bf16 v[172:175], v[192:195], a[240:243], v[172:175]\n"
    "ds_read_b64_tr_b16 v[212:213], v29 offset:16384\n"
    "buffer_load_dwordx4 v22, s[24:27], s59 offen offset:0 lds\n"
    "v_mfma_f32_16x16x32_bf16 v[176:179], v[196:199], a[232:235], v[176:179]\n"
    "ds_read_b64_tr_b16 v[214:215], v29 offset:16896\n"
    "v_mfma_f32_16x16x32_bf16 v[180:183], v[196:199], a[236:239], v[180:183]\n"
    "v_mfma_f32_16x16x32_bf16 v[184:187], v[196:199], a[240:243], v[184:187]\n"
    "s_waitcnt lgkmcnt(4)\n"
    "v_mfma_f32_16x16x32_bf16 v[164:167], v[200:203], a[244:247], v[164:167]\n"
    "ds_read_b64_tr_b16 v[216:217], v28 offset:17408\n"
    "buffer_load_dwordx4 v23, s[24:27], s59 offen offset:1024 lds\n"
    "v_mfma_f32_16x16x32_bf16 v[168:171], v[200:203], a[248:251], v[168:171]\n"
    "ds_read_b64_tr_b16 v[218:219], v28 offset:17920\n"
    "s_add_u32 m0, s56, 0x8000\n"
    "v_mfma_f32_16x16x32_bf16 v[172:175], v[200:203], a[252:255], v[172:175]\n"
    "ds_read_b64_tr_b16 v[220:221], v29 offset:17408\n"
    "s_mul_i32 s59, s74, 0x36000\n"
    "v_mfma_f32_16x16x32_bf16 v[176:179], v[204:207], a[244:247], v[176:179]\n"
    "ds_read_b64_tr_b16 v[222:223], v29 offset:17920\n"
    "v_add_u32 v22, 0x80, v22\n"
    "v_mfma_f32_16x16x32_bf16 v[180:183], v[204:207], a[248:251], v[180:183]\n"
    "v_add_u32 v23, 0x80, v23\n"
    "buffer_load_dwordx4 v22, s[24:27], s59 offen offset:0 lds\n"
    "v_mfma_f32_16x16x32_bf16 v[184:187], v[204:207], a[252:255], v[184:187]\n"
    "s_waitcnt lgkmcnt(4)\n"
    "v_mfma_f32_16x16x32_bf16 v[164:167], v[208:211], a[0:3], v[164:167]\n"
    "ds_read_b64_tr_b16 v[192:193], v26 offset:49152\n"
    "v_mfma_f32_16x16x32_bf16 v[168:171], v[208:211], a[4:7], v[168:171]\n"
    "ds_read_b64_tr_b16 v[194:195], v26 offset:49664\n"
    "v_mfma_f32_16x16x32_bf16 v[172:175], v[208:211], a[8:11], v[172:175]\n"
    "ds_read_b64_tr_b16 v[196:197], v27 offset:49152\n"
    "buffer_load_dwordx4 v23, s[24:27], s59 offen offset:1024 lds\n"
    "v_mfma_f32_16x16x32_bf16 v[176:179], v[212:215], a[0:3], v[176:179]\n"
    "ds_read_b64_tr_b16 v[198:199], v27 offset:49664\n"
    "s_add_u32 m0, s56, 0xa000\n"
    "v_mfma_f32_16x16x32_bf16 v[180:183], v[212:215], a[4:7], v[180:183]\n"
    "s_sub_u32 s59, s59, 0x36000\n"
    "s_cselect_b32 s59, 0x1e6000, s59\n"
    "v_mfma_f32_16x16x32_bf16 v[184:187], v[212:215], a[8:11], v[184:187]\n"
    "s_waitcnt lgkmcnt(4)\n"
    "v_mfma_f32_16x16x32_bf16 v[164:167], v[216:219], a[12:15], v[164:167]\n"
    "ds_read_b64_tr_b16 v[200:201], v26 offset:50176\n"
    "buffer_load_dwordx4 v22, s[24:27], s59 offen offset:0 lds\n"
    "v_mfma_f32_16x16x32_bf16 v[168:171], v[216:219], a[16:19], v[168:171]\n"
    "ds_read_b64_tr_b16 v[202:203], v26 offset:50688\n"
    "v_mfma_f32_16x16x32_bf16 v[172:175], v[216:219], a[20:23], v[172:175]\n"
    "ds_read_b64_tr_b16 v[204:205], v27 offset:50176\n"
    "v_mfma_f32_16x16x32_bf16 v[176:179], v[220:223], a[12:15], v[176:179]\n"
    "ds_read_b64_tr_b16 v[206:207], v27 offset:50688\n"
    "v_mfma_f32_16x16x32_bf16 v[180:183], v[220:223], a[16:19], v[180:183]\n"
    "buffer_load_dwordx4 v23, s[24:27], s59 offen offset:1024 lds\n"
    "v_mfma_f32_16x16x32_bf16 v[184:187], v[220:223], a[20:23], v[184:187]\n"
    "s_waitcnt lgkmcnt(4)\n"
    "v_mfma_f32_16x16x32_bf16 v[128:131], v[192:195], a[232:235], v[128:131]\n"
    "ds_read_b64_tr_b16 v[208:209], v26 offset:16384\n"
    "v_mfma_f32_16x16x32_bf16 v[132:135], v[192:195], a[236:239], v[132:135]\n"
    "ds_read_b64_tr_b16 v[210:211], v26 offset:16896\n"
    "v_mfma_f32_16x16x32_bf16 v[136:139], v[192:195], a[240:243], v[136:139]\n"
    "ds_read_b64_tr_b16 v[212:213], v27 offset:16384\n"
    "v_mfma_f32_16x16x32_bf16 v[140:143], v[196:199], a[232:235], v[140:143]\n"
    "ds_read_b64_tr_b16 v[214:215], v27 offset:16896\n"
    "v_mfma_f32_16x16x32_bf16 v[144:147], v[196:199], a[236:239], v[144:147]\n"
    "ds_write_b128 v56, v[164:167] offset:0\n"
    "v_mfma_f32_16x16x32_bf16 v[148:151], v[196:199], a[240:243], v[148:151]\n"
    "s_waitcnt lgkmcnt(5)\n"
    "v_mfma_f32_16x16x32_bf16 v[128:131], v[200:203], a[244:247], v[128:131]\n"
    "ds_read_b64_tr_b16 v[216:217], v26 offset:17408\n"
    "v_mfma_f32_16x16x32_bf16 v[132:135], v[200:203], a[248:251], v[132:135]\n"
    "ds_read_b64_tr_b16 v[218:219], v26 offset:17920\n"
    "v_mfma_f32_16x16x32_bf16 v[136:139], v[200:203], a[252:255], v[136:139]\n"
    "ds_read_b64_tr_b16 v[220:221], v27 offset:17408\n"
    "v_mfma_f32_16x16x32_bf16 v[140:143], v[204:207], a[244:247], v[140:143]\n"
    "ds_read_b64_tr_b16 v[222:223], v27 offset:17920\n"
    "v_mfma_f32_16x16x32_bf16 v[144:147], v[204:207], a[248:251], v[144:147]\n"
    "ds_write_b128 v56, v[168:171] offset:1024\n"
    "v_mfma_f32_16x16x32_bf16 v[148:151], v[204:207], a[252:255], v[148:151]\n"
    "ds_write_b128 v56, v[172:175] offset:2048\n"
    "s_waitcnt lgkmcnt(7)\n"
    "v_mfma_f32_16x16x32_bf16 v[128:131], v[208:211], a[0:3], v[128:131]\n"
    "v_mfma_f32_16x16x32_bf16 v[132:135], v[208:211], a[4:7], v[132:135]\n"
    "v_mfma_f32_16x16x32_bf16 v[136:139], v[208:211], a[8:11], v[136:139]\n"
    "ds_write_b128 v56, v[176:179] offset:3072\n"
    "v_mfma_f32_16x16x32_bf16 v[140:143], v[212:215], a[0:3], v[140:143]\n"
    "v_mfma_f32_16x16x32_bf16 v[144:147], v[212:215], a[4:7], v[144:147]\n"
    "v_mfma_f32_16x16x32_bf16 v[148:151], v[212:215], a[8:11], v[148:151]\n"
    "ds_write_b128 v56, v[180:183] offset:4096\n"
    "s_waitcnt lgkmcnt(4)\n"
    "v_mfma_f32_16x16x32_bf16 v[128:131], v[216:219], a[12:15], v[128:131]\n"
    "v_mfma_f32_16x16x32_bf16 v[132:135], v[216:219], a[16:19], v[132:135]\n"
    "v_mfma_f32_16x16x32_bf16 v[136:139], v[216:219], a[20:23], v[136:139]\n"
    "ds_write_b128 v56, v[184:187] offset:5120\n"
    "v_mfma_f32_16x16x32_bf16 v[140:143], v[220:223], a[12:15], v[140:143]\n"
    "v_mfma_f32_16x16x32_bf16 v[144:147], v[220:223], a[16:19], v[144:147]\n"
    "v_mfma_f32_16x16x32_bf16 v[148:151], v[220:223], a[20:23], v[148:151]\n"
    "s_waitcnt lgkmcnt(0)\n"
    "s_barrier\n"
    "s_add_u32 m0, s56, 0xc000\n"
    "s_sub_u32 s59, s59, 0x36000\n"
    "s_cselect_b32 s59, 0x1e6000, s59\n"
    "buffer_load_dwordx4 v22, s[24:27], s59 offen offset:0 lds\n"
    "ds_read_b128 v[192:195], v57 offset:0\n"
    "ds_read_b128 v[204:207], v57 offset:3072\n"
    "ds_read_b128 v[196:199], v57 offset:1024\n"
    "ds_read_b128 v[208:211], v57 offset:4096\n"
    "ds_read_b128 v[200:203], v57 offset:2048\n"
    "ds_read_b128 v[212:215], v57 offset:5120\n"
    "s_sub_u32 s92, s85, 0x800\n"
    "buffer_load_dwordx2 v[6:7], v8, s[36:39], s92 offen offset:0 sc1\n"
    "s_waitcnt lgkmcnt(5)\n"
    "v_pk_add_f32 v[128:129], v[128:129], v[192:193] op_sel:[0,0] op_sel_hi:[1,1]\n"
    "v_pk_add_f32 v[130:131], v[130:131], v[194:195] op_sel:[0,0] op_sel_hi:[1,1]\n"
    "s_waitcnt lgkmcnt(4)\n"
    "v_pk_add_f32 v[140:141], v[140:141], v[204:205] op_sel:[0,0] op_sel_hi:[1,1]\n"
    "v_pk_add_f32 v[142:143], v[142:143], v[206:207] op_sel:[0,0] op_sel_hi:[1,1]\n"
    "v_cvt_pk_bf16_f32 v164, v128, v129\n"
    "v_cvt_pk_bf16_f32 v165, v130, v131\n"
    "v_cvt_pk_bf16_f32 v166, v140, v141\n"
    "v_cvt_pk_bf16_f32 v167, v142, v143\n"
    "v_dot2_f32_bf16 v176, v164, v48, 0\n"
    "v_dot2_f32_bf16 v177, v164, v164, 0\n"
    "v_dot2_f32_bf16 v176, v165, v48, v176\n"
    "v_dot2_f32_bf16 v177, v165, v165, v177\n"
    "v_dot2_f32_bf16 v176, v166, v48, v176\n"
    "v_dot2_f32_bf16 v177, v166, v166, v177\n"
    "v_dot2_f32_bf16 v176, v167, v48, v176\n"
    "buffer_load_dwordx4 v23, s[24:27], s59 offen offset:1024 lds\n"
    "s_add_u32 m0, s56, 0xe000\n"
    "s_sub_u32 s59, s59, 0x36000\n"
    "s_cselect_b32 s59, 0x1e6000, s59\n"
    "buffer_load_dwordx4 v22, s[24:27], s59 offen offset:0 lds\n"
    "v_dot2_f32_bf16 v177, v167, v167, v177\n"
    "v_permlane16_swap_b32 v164, v166\n"
    "v_permlane16_swap_b32 v165, v167\n"
    "buffer_store_dwordx4 v[164:167], v12, s[36:39], 0 offen offset:0 sc1\n"
    "s_waitcnt lgkmcnt(3)\n"
    "v_pk_add_f32 v[132:133], v[132:133], v[196:197] op_sel:[0,0] op_sel_hi:[1,1]\n"
    "v_pk_add_f32 v[134:135], v[134:135], v[198:199] op_sel:[0,0] op_sel_hi:[1,1]\n"
    "s_waitcnt lgkmcnt(2)\n"
    "v_pk_add_f32 v[144:145], v[144:145], v[208:209] op_sel:[0,0] op_sel_hi:[1,1]\n"
    "v_pk_add_f32 v[146:147], v[146:147], v[210:211] op_sel:[0,0] op_sel_hi:[1,1]\n"
    "v_cvt_pk_bf16_f32 v168, v132, v133\n"
    "v_cvt_pk_bf16_f32 v169, v134, v135\n"
    "v_cvt_pk_bf16_f32 v170, v144, v145\n"
    "v_cvt_pk_bf16_f32 v171, v146, v147\n"
    "v_dot2_f32_bf16 v178, v168, v48, 0\n"
    "v_dot2_f32_bf16 v179, v168, v168, 0\n"
    "v_dot2_f32_bf16 v178, v169, v48, v178\n"
    "v_dot2_f32_bf16 v179, v169, v169, v179\n"
    "v_dot2_f32_bf16 v178, v170, v48, v178\n"
    "v_dot2_f32_bf16 v179, v170, v170, v179\n"
    "v_dot2_f32_bf16 v178, v171, v48, v178\n"
    "v_dot2_f32_bf16 v179, v171, v171, v179\n"
    "v_permlane16_swap_b32 v168, v170\n"
    "v_permlane16_swap_b32 v169, v171\n"
    "buffer_store_dwordx4 v[168:171], v12, s[36:39], 0 offen offset:256 sc1\n"
    "s_waitcnt lgkmcnt(1)\n"
    "v_pk_add_f32 v[136:137], v[136:137], v[200:201] op_sel:[0,0] op_sel_hi:[1,1]\n"
    "v_pk_add_f32 v[138:139], v[138:139], v[202:203] op_sel:[0,0] op_sel_hi:[1,1]\n"
    "s_waitcnt lgkmcnt(0)\n"
    "v_pk_add_f32 v[148:149], v[148:149], v[212:213] op_sel:[0,0] op_sel_hi:[1,1]\n"
    "v_pk_add_f32 v[150:151], v[150:151], v[214:215] op_sel:[0,0] op_sel_hi:[1,1]\n"
    "v_cvt_pk_bf16_f32 v172, v136, v137\n"
    "v_cvt_pk_bf16_f32 v173, v138, v139\n"
    "v_cvt_pk_bf16_f32 v174, v148, v149\n"
    "v_cvt_pk_bf16_f32 v175, v150, v151\n"
    "v_dot2_f32_bf16 v180, v172, v48, 0\n"
    "v_dot2_f32_bf16 v181, v172, v172, 0\n"
    "v_dot2_f32_bf16 v180, v173, v48, v180\n"
    "v_dot2_f32_bf16 v181, v173, v173, v181\n"
    "v_dot2_f32_bf16 v180, v174, v48, v180\n"
    "v_dot2_f32_bf16 v181, v174, v174, v181\n"
    "v_dot2_f32_bf16 v180, v175, v48, v180\n"
    "v_dot2_f32_bf16 v181, v175, v175, v181\n"
    "v_permlane16_swap_b32 v172, v174\n"
    "v_permlane16_swap_b32 v173, v175\n"
    "buffer_store_dwordx4 v[172:175], v12, s[36:39], s94 offen offset:512 sc1\n"
    "v_permlane16_swap_b32 v176, v177\n"
    "v_permlane16_swap_b32 v178, v179\n"
    "v_permlane16_swap_b32 v180, v181\n"
    "v_add_f32 v176, v176, v177\n"
    "v_add_f32 v178, v178, v179\n"
    "v_add_f32 v180, v180, v181\n"
    "v_mov_b32 v177, v176\n"
    "v_mov_b32 v179, v178\n"
    "v_mov_b32 v181, v180\n"
    "v_permlane32_swap_b32 v176, v177\n"
    "v_permlane32_swap_b32 v178, v179\n"
    "v_permlane32_swap_b32 v180, v181\n"
    "v_add_f32 v176, v176, v177\n"
    "buffer_store_dword v176, v13, s[36:39], 0 offen offset:0 sc1\n"
    "v_add_f32 v178, v178, v179\n"
    "buffer_store_dword v178, v13, s[36:39], 0 offen offset:128 sc1\n"
    "v_add_f32 v180, v180, v181\n"
    "buffer_store_dword v180, v13, s[36:39], s94 offen offset:256 sc1\n"
    "v_mul_f32 v58, 0x3ea52b5c, v52\n"
    "s_mov_b32 s90, -1\n"
    "s_mov_b32 s91, 0\n"
    "v_cndmask_b32 v58, v52, v58, s[90:91]\n"
    "v_cvt_pk_bf16_f32 v232, v84, v85\n"
    "v_cvt_pk_bf16_f32 v233, v86, v87\n"
    "v_cvt_pk_bf16_f32 v238, v88, v89\n"
    "v_cvt_pk_bf16_f32 v239, v90, v91\n"
    "v_cvt_pk_bf16_f32 v244, v92, v93\n"
    "v_cvt_pk_bf16_f32 v245, v94, v95\n"
    "v_pk_mul_f32 v[72:73], v[72:73], v[58:59] op_sel:[0,0] op_sel_hi:[1,0]\n"
    "v_pk_mul_f32 v[74:75], v[74:75], v[58:59] op_sel:[0,0] op_sel_hi:[1,0]\n"
    "v_pk_mul_f32 v[76:77], v[76:77], v[52:53] op_sel:[0,0] op_sel_hi:[1,0]\n"
    "v_pk_mul_f32 v[78:79], v[78:79], v[52:53] op_sel:[0,0] op_sel_hi:[1,0]\n"
    "v_pk_mul_f32 v[80:81], v[80:81], v[52:53] op_sel:[0,0] op_sel_hi:[1,0]\n"
    "buffer_load_dwordx4 v23, s[24:27], s59 offen offset:1024 lds\n"
    "s_add_u32 m0, s56, 0x4000\n"
    "s_sub_u32 s59, s59, 0x36000\n"
    "s_cselect_b32 s59, 0x1e6000, s59\n"
    "buffer_load_dwordx4 v22, s[24:27], s59 offen offset:0 lds\n"
    "v_pk_mul_f32 v[82:83], v[82:83], v[52:53] op_sel:[0,0] op_sel_hi:[1,0]\n"
    "v_cvt_pk_bf16_f32 v196, v72, v73\n"
    "v_cvt_pk_bf16_f32 v197, v74, v75\n"
    "v_mov_b32 v188, v196\n"
    "v_mov_b32 v189, v197\n"
    "v_cvt_pk_bf16_f32 v192, v76, v77\n"
    "v_cvt_pk_bf16_f32 v193, v78, v79\n"
    "v_cvt_pk_bf16_f32 v194, v80, v81\n"
    "v_cvt_pk_bf16_f32 v195, v82, v83\n"
    "v_permlane32_swap_b32 v188, v196\n"
    "v_permlane32_swap_b32 v189, v197\n"
    "v_mul_f32 v58, 0x3ea52b5c, v52\n"
    "v_pk_mul_f32 v[64:65], v[64:65], v[58:59] op_sel:[0,0] op_sel_hi:[1,0]\n"
    "v_pk_mul_f32 v[66:67], v[66:67], v[58:59] op_sel:[0,0] op_sel_hi:[1,0]\n"
    "v_pk_mul_f32 v[68:69], v[68:69], v[58:59] op_sel:[0,0] op_sel_hi:[1,0]\n"
    "v_pk_mul_f32 v[70:71], v[70:71], v[58:59] op_sel:[0,0] op_sel_hi:[1,0]\n"
    "v_cvt_pk_bf16_f32 v200, v64, v65\n"
    "v_cvt_pk_bf16_f32 v201, v66, v67\n"
    "v_cvt_pk_bf16_f32 v59, v68, v69\n"
    "v_cvt_pk_bf16_f32 v60, v70, v71\n"
    "s_mov_b32 s90, 0xffff\n"
    "s_mov_b32 s91, 0\n"
    "v_cndmask_b32 v202, 0, v59, s[90:91]\n"
    "v_cndmask_b32 v203, 0, v60, s[90:91]\n"
    "v_cndmask_b32 v204, v59, 0, s[90:91]\n"
    "buffer_load_dwordx4 v23, s[24:27], s59 offen offset:1024 lds\n"
    "s_add_u32 m0, s56, 0x6000\n"
    "s_sub_u32 s59, s59, 0x36000\n"
    "s_cselect_b32 s59, 0x1e6000, s59\n"
    "buffer_load_dwordx4 v22, s[24:27], s59 offen offset:0 lds\n"
    "v_cndmask_b32 v205, v60, 0, s[90:91]\n"
    "s_mov_b32 s90, -1\n"
    "v_cndmask_b32 v206, 0, v188, s[90:91]\n"
    "v_cndmask_b32 v207, 0, v189, s[90:91]\n"
    "v_mul_f32 v58, 0x3ea52b5c, v53\n"
    "v_pk_mul_f32 v[96:97], v[96:97], v[58:59] op_sel:[0,0] op_sel_hi:[1,0]\n"
    "v_pk_mul_f32 v[98:99], v[98:99], v[58:59] op_sel:[0,0] op_sel_hi:[1,0]\n"
    "v_pk_mul_f32 v[100:101], v[100:101], v[58:59] op_sel:[0,0] op_sel_hi:[1,0]\n"
    "v_pk_mul_f32 v[102:103], v[102:103], v[58:59] op_sel:[0,0] op_sel_hi:[1,0]\n"
    "v_cvt_pk_bf16_f32 v208, v96, v97\n"
    "v_cvt_pk_bf16_f32 v209, v98, v99\n"
    "v_cvt_pk_bf16_f32 v59, v100, v101\n"
    "v_cvt_pk_bf16_f32 v60, v102, v103\n"
    "s_mov_b32 s90, 0xffff\n"
    "s_mov_b32 s91, 0\n"
    "v_cndmask_b32 v210, 0, v59, s[90:91]\n"
    "v_cndmask_b32 v211, 0, v60, s[90:91]\n"
    "v_cndmask_b32 v212, v59, 0, s[90:91]\n"
    "v_cndmask_b32 v213, v60, 0, s[90:91]\n"
    "s_mov_b32 s90, -1\n"
    "v_cndmask_b32 v214, 0, v190, s[90:91]\n"
    "v_cndmask_b32 v215, 0, v191, s[90:91]\n"
    "s_add_u32 s64, s45, 15\n"
    "s_and_b32 s65, s64, 3\n"
    "s_lshr_b32 s64, s64, 2\n"
    "s_and_b32 s64, s64, 3\n"
    "s_lshl_b32 s64, s64, 1\n"
    "buffer_load_dwordx4 v23, s[24:27], s59 offen offset:1024 lds\n"
    "s_and_b32 s67, s46, 7\n"
    "s_cmp_lt_u32 s67, 5\n"
    "s_cselect_b32 s68, 5, 3\n"
    "s_cselect_b32 s69, 0, 15\n"
    "s_mul_i32 s65, s65, s68\n"
    "s_add_u32 s65, s65, s69\n"
    "s_add_u32 s65, s65, s67\n"
    "s_lshl_b32 s65, s65, 3\n"
    "s_add_u32 s64, s64, s65\n"
    "s_add_u32 s64, s64, s44\n"
    "s_mul_i32 s64, s64, 0xc00\n"
    "s_add_u32 s64, s64, 0x97000\n"
    "s_cmp_eq_u32 s45, 0\n"
    "s_cselect_b32 s64, 0x40000000, s64\n"
    "s_cmp_eq_u32 s41, 0\n"
    "s_cselect_b32 s64, s64, 0x40000000\n"
    "s_waitcnt vmcnt(13)\n"
    "v_cmp_eq_u64 vcc, v[6:7], v[4:5]\n"
    "s_or_b32 vcc_lo, vcc_lo, s78\n"
    "s_cmp_eq_u32 vcc_lo, -1\n"
    "s_cbranch_scc0 .Lspin_KV\n"
    ".Ldone_KV:\n"
    "buffer_load_dwordx4 v[64:67], v38, s[36:39], s64 offen offset:0 sc1\n"
    "buffer_load_dwordx4 v[68:71], v38, s[36:39], s64 offen offset:1024 sc1\n"
    "buffer_load_dwordx4 v[72:75], v38, s[36:39], s64 offen offset:2048 sc1\n"
    "s_waitcnt vmcnt(8)\n"
    "s_barrier\n"
    "buffer_store_dwordx2 v[4:5], v9, s[36:39], s86 offen offset:0 sc1\n"
    "v_lshl_add_u32 v46, v1, 4, s56\n"
    "v_add_u32 v47, 0x10000, v46\n"
    "s_add_u32 s87, s85, 0x10000\n"
    "s_mul_i32 s69, s52, 14\n"
    "s_add_u32 s69, s69, s53\n"
    "s_lshl_b32 s69, s69, 7\n"
    "s_add_u32 s88, s69, 0x29000\n"
    "s_cmp_lt_u32 s42, 28\n"
    "s_cselect_b32 s88, s88, 0x40000000\n"
    "s_add_u32 s89, s88, 0x8000\n"
    "s_mov_b32 s69, 0xffffffff\n"
    "s_cmp_eq_u32 s3, 0\n"
    "s_cselect_b32 s69, 0xffff, s69\n"
    "s_cmp_eq_u32 s3, 2\n"
    "s_cselect_b32 s69, 0xffff0000, s69\n"
    "s_cmp_lt_u32 s42, 30\n"
    "s_cselect_b32 s69, s69, 0\n"
    "s_cmp_eq_u32 s41, 0\n"
    "s_cselect_b32 s69, s69, 0\n"
    "s_not_b32 s80, s69\n"
    "s_mov_b32 s69, 0xfffffff\n"
    "s_cmp_eq_u32 s3, 0\n"
    "s_cselect_b32 s69, 0x3fff, s69\n"
    "s_cmp_eq_u32 s3, 2\n"
    "s_cselect_b32 s69, 0xfffc000, s69\n"
    "s_cmp_lt_u32 s42, 30\n"
    "s_cselect_b32 s69, s69, 0\n"
    "s_cmp_eq_u32 s41, 0\n"
    "s_cselect_b32 s69, s69, 0\n"
    "s_not_b32 s81, s69\n"
    "s_mov_b32 s69, 0x7ffdfff\n"
    "s_cmp_eq_u32 s3, 0\n"
    "s_cselect_b32 s69, 0x1fff, s69\n"
    "s_cmp_eq_u32 s3, 2\n"
    "s_cselect_b32 s69, 0x7ffc000, s69\n"
    "s_cmp_lt_u32 s42, 30\n"
    "s_cselect_b32 s69, s69, 0\n"
    "s_cmp_eq_u32 s41, 0\n"
    "s_cselect_b32 s69, s69, 0\n"
    "s_not_b32 s82, s69\n"
    "v_lshlrev_b32 v58, 2, v2\n"
    "v_sub_u32 v58, v3, v58\n"
    "v_mov_b32 v60, 0xf149f2ca\n"
    "s_add_u32 m0, s56, 0x22000\n"
    "s_sub_u32 s59, s59, 0x36000\n"
    "s_cselect_b32 s59, 0x1e6000, s59\n"
    "buffer_load_dwordx4 v22, s[24:27], s59 offen offset:0 lds\n"
    "buffer_load_dwordx4 v23, s[24:27], s59 offen offset:1024 lds\n"
    "v_add_u32 v59, 16, v58\n"
    "v_and_b32 v59, 0xfffffff1, v59\n"
    "v_cmp_eq_u32 vcc, 0, v59\n"
    "s_nop 1\n"
    "v_cndmask_b32 v112, v60, 0, vcc\n"
    "v_add_u32 v59, 15, v58\n"
    "v_and_b32 v59, 0xfffffff1, v59\n"
    "v_cmp_eq_u32 vcc, 0, v59\n"
    "s_nop 1\n"
    "v_cndmask_b32 v113, v60, 0, vcc\n"
    "v_add_u32 v59, 14, v58\n"
    "v_and_b32 v59, 0xfffffff1, v59\n"
    "v_cmp_eq_u32 vcc, 0, v59\n"
    "s_nop 1\n"
    "v_cndmask_b32 v114, v60, 0, vcc\n"
    "v_add_u32 v59, 13, v58\n"
    "v_and_b32 v59, 0xfffffff1, v59\n"
    "v_cmp_eq_u32 vcc, 0, v59\n"
    "s_nop 1\n"
    "v_cndmask_b32 v115, v60, 0, vcc\n"
    "v_add_u32 v59, 0, v58\n"
    "v_and_b32 v59, 0xfffffff1, v59\n"
    "v_cmp_eq_u32 vcc, 0, v59\n"
    "s_nop 1\n"
    "v_cndmask_b32 v116, v60, 0, vcc\n"
    "v_add_u32 v59, -1, v58\n"
    "v_and_b32 v59, 0xfffffff1, v59\n"
    "v_cmp_eq_u32 vcc, 0, v59\n"
    "s_nop 1\n"
    "v_cndmask_b32 v117, v60, 0, vcc\n"
    "v_add_u32 v59, -2, v58\n"
    "v_and_b32 v59, 0xfffffff1, v59\n"
    "v_cmp_eq_u32 vcc, 0, v59\n"
    "s_nop 1\n"
    "v_cndmask_b32 v118, v60, 0, vcc\n"
    "v_add_u32 v59, -3, v58\n"
    "v_and_b32 v59, 0xfffffff1, v59\n"
    "s_add_u32 m0, s56, 0x24000\n"
    "s_sub_u32 s59, s59, 0x36000\n"
    "s_cselect_b32 s59, 0x1e6000, s59\n"
    "buffer_load_dwordx4 v22, s[24:27], s59 offen offset:0 lds\n"
    "buffer_load_dwordx4 v23, s[24:27], s59 offen offset:1024 lds\n"
    "v_cmp_eq_u32 vcc, 0, v59\n"
    "s_nop 1\n"
    "v_cndmask_b32 v119, v60, 0, vcc\n"
    "s_or_b32 s65, s45, s41\n"
    "s_cmp_eq_u32 s65, 0\n"
    "s_cselect_b64 s[90:91], -1, 0\n"
    "v_cndmask_b32 v120, v112, v60, s[90:91]\n"
    "v_cndmask_b32 v121, v113, v60, s[90:91]\n"
    "v_cndmask_b32 v122, v114, v60, s[90:91]\n"
    "v_cndmask_b32 v123, v115, v60, s[90:91]\n"
    "s_mul_i32 s64, s46, 0x28280\n"
    "s_lshl_b32 s65, s45, 7\n"
    "s_lshl_b32 s67, s41, 5\n"
    "s_add_u32 s65, s65, s67\n"
    "s_lshl_b32 s65, s65, 4\n"
    "s_add_u32 s64, s64, s65\n"
    "s_add_u32 s64, s64, 0x8f0c00\n"
    "v_and_b32 v58, 1, v2\n"
    "v_lshrrev_b32 v59, 1, v2\n"
    "v_lshl_add_u32 v58, v58, 1, v59\n"
    "v_mul_u32_u24 v58, 0x8080, v58\n"
    "v_lshl_add_u32 v58, v3, 4, v58\n"
    "v_add_u32 v44, s64, v58\n"
    "s_add_u32 s64, s64, 0x20200\n"
    "v_lshlrev_b32 v58, 4, v3\n"
    "v_lshl_add_u32 v58, v2, 3, v58\n"
    "v_add_u32 v58, s64, v58\n"
    "v_cmp_gt_u32 vcc, 32, v1\n"
    "v_mov_b32 v59, 0x40000000\n"
    "s_nop 0\n"
    "v_cndmask_b32 v45, v59, v58, vcc\n"
    "s_cmp_eq_u32 s41, 0\n"
    "s_cselect_b64 s[90:91], -1, 0\n"
    "s_mul_i32 s61, s75, 0x14000\n"
    "buffer_load_dwordx4 a[160:163], v20, s[12:15], s61 offen offset:0\n"
    "buffer_load_dwordx4 a[164:167], v21, s[12:15], s61 offen offset:1024\n"
    "s_waitcnt vmcnt(9)\n"
    "v_cndmask_b32 v248, v248, v64, s[90:91]\n"
    "v_cndmask_b32 v249, v249, v65, s[90:91]\n"
    "v_cndmask_b32 v250, v250, v66, s[90:91]\n"
    "v_cndmask_b32 v251, v251, v67, s[90:91]\n"
    "s_waitcnt vmcnt(8)\n"
    "v_cndmask_b32 v252, v252, v68, s[90:91]\n"
    "v_cndmask_b32 v253, v253, v69, s[90:91]\n"
    "v_cndmask_b32 v230, v230, v70, s[90:91]\n"
    "v_cndmask_b32 v231, v231, v71, s[90:91]\n"
    "s_waitcnt vmcnt(7)\n"
    "v_cndmask_b32 v236, v236, v72, s[90:91]\n"
    "v_cndmask_b32 v237, v237, v73, s[90:91]\n"
    "v_cndmask_b32 v242, v242, v74, s[90:91]\n"
    "v_cndmask_b32 v243, v243, v75, s[90:91]\n"
    "v_mfma_f32_16x16x32_bf16 v[64:67], v[248:251], v[200:203], v[120:123]\n"
    "s_add_u32 s62, s61, 0x64000\n"
    "v_mfma_f32_16x16x32_bf16 v[68:71], v[192:195], v[200:203], v[116:119]\n"
    "s_cmp_ge_u32 s62, 0xc8000\n"
    "s_cselect_b32 s0, 0xc8000, 0\n"
    "v_mfma_f32_16x16x32_bf16 v[72:75], v[250:253], v[204:207], v[120:123]\n"
    "s_sub_u32 s62, s62, s0\n"
    "buffer_load_dwordx4 a[168:171], v20, s[12:15], s62 offen offset:0\n"
    "v_mfma_f32_16x16x32_bf16 v[76:79], v[194:197], v[204:207], v[116:119]\n"
    "v_mfma_f32_16x16x32_bf16 v[80:83], v[192:195], v[208:211], v[112:115]\n"
    "v_mfma_f32_16x16x32_bf16 v[84:87], v[224:227], v[208:211], v[116:119]\n"
    "v_mfma_f32_16x16x32_bf16 v[88:91], v[194:197], v[212:215], v[112:115]\n"
    "buffer_load_dwordx4 a[172:175], v21, s[12:15], s62 offen offset:1024\n"
    "v_mfma_f32_16x16x32_bf16 v[92:95], v[226:229], v[212:215], v[116:119]\n"
    "v_max3_f32 v124, v64, v65, v66\n"
    "v_max3_f32 v125, v72, v73, v74\n"
    "s_nop 1\n"
    "v_max3_f32 v126, v80, v81, v82\n"
    "s_nop 0\n"
    "v_max3_f32 v127, v88, v89, v90\n"
    "v_max3_f32 v124, v124, v67, v68\n"
    "v_max3_f32 v125, v125, v75, v76\n"
    "v_max3_f32 v126, v126, v83, v84\n"
    "v_max3_f32 v127, v127, v91, v92\n"
    "v_max3_f32 v124, v124, v69, v70\n"
    "v_max3_f32 v125, v125, v77, v78\n"
    "v_max3_f32 v126, v126, v85, v86\n"
    "v_max3_f32 v127, v127, v93, v94\n"
    "v_max_f32 v124, v124, v71\n"
    "v_max_f32 v125, v125, v79\n"
    "v_max_f32 v126, v126, v87\n"
    "v_max_f32 v127, v127, v95\n"
    "v_mov_b32 v200, v124\n"
    "v_mov_b32 v201, v125\n"
    "v_mov_b32 v202, v126\n"
    "v_mov_b32 v203, v127\n"
    "s_sub_u32 s61, s61, 0x14000\n"
    "s_cselect_b32 s61, 0xb4000, s61\n"
    "buffer_load_dwordx4 a[176:179], v20, s[12:15], s61 offen offset:0\n"
    "buffer_load_dwordx4 a[180:183], v21, s[12:15], s61 offen offset:1024\n"
    "v_permlane16_swap_b32 v124, v200\n"
    "v_permlane16_swap_b32 v125, v201\n"
    "v_permlane16_swap_b32 v126, v202\n"
    "v_permlane16_swap_b32 v127, v203\n"
    "v_max_f32 v124, v124, v200\n"
    "v_max_f32 v125, v125, v201\n"
    "v_max_f32 v126, v126, v202\n"
    "v_max_f32 v127, v127, v203\n"
    "v_mov_b32 v200, v124\n"
    "v_mov_b32 v201, v125\n"
    "v_mov_b32 v202, v126\n"
    "v_mov_b32 v203, v127\n"
    "v_permlane32_swap_b32 v124, v200\n"
    "v_permlane32_swap_b32 v125, v201\n"
    "v_permlane32_swap_b32 v126, v202\n"
    "v_permlane32_swap_b32 v127, v203\n"
    "v_max_f32 v124, v124, v200\n"
    "v_max_f32 v125, v125, v201\n"
    "v_max_f32 v126, v126, v202\n"
    "v_max_f32 v127, v127, v203\n"
    "v_pk_add_f32 v[64:65], v[64:65], v[124:125] op_sel:[0,0] op_sel_hi:[1,0] neg_lo:[0,1] neg_hi:[0,1]\n"
    "v_pk_add_f32 v[66:67], v[66:67], v[124:125] op_sel:[0,0] op_sel_hi:[1,0] neg_lo:[0,1] neg_hi:[0,1]\n"
    "s_sub_u32 s62, s62, 0x14000\n"
    "s_cselect_b32 s62, 0xb4000, s62\n"
    "buffer_load_dwordx4 a[184:187], v20, s[12:15], s62 offen offset:0\n"
    "buffer_load_dwordx4 a[188:191], v21, s[12:15], s62 offen offset:1024\n"
    "v_pk_add_f32 v[68:69], v[68:69], v[124:125] op_sel:[0,0] op_sel_hi:[1,0] neg_lo:[0,1] neg_hi:[0,1]\n"
    "v_pk_add_f32 v[70:71], v[70:71], v[124:125] op_sel:[0,0] op_sel_hi:[1,0] neg_lo:[0,1] neg_hi:[0,1]\n"
    "v_pk_add_f32 v[72:73], v[72:73], v[124:125] op_sel:[0,1] op_sel_hi:[1,1] neg_lo:[0,1] neg_hi:[0,1]\n"
    "v_pk_add_f32 v[74:75], v[74:75], v[124:125] op_sel:[0,1] op_sel_hi:[1,1] neg_lo:[0,1] neg_hi:[0,1]\n"
    "v_pk_add_f32 v[76:77], v[76:77], v[124:125] op_sel:[0,1] op_sel_hi:[1,1] neg_lo:[0,1] neg_hi:[0,1]\n"
    "v_pk_add_f32 v[78:79], v[78:79], v[124:125] op_sel:[0,1] op_sel_hi:[1,1] neg_lo:[0,1] neg_hi:[0,1]\n"
    "v_pk_add_f32 v[80:81], v[80:81], v[126:127] op_sel:[0,0] op_sel_hi:[1,0] neg_lo:[0,1] neg_hi:[0,1]\n"
    "v_pk_add_f32 v[82:83], v[82:83], v[126:127] op_sel:[0,0] op_sel_hi:[1,0] neg_lo:[0,1] neg_hi:[0,1]\n"
    "v_pk_add_f32 v[84:85], v[84:85], v[126:127] op_sel:[0,0] op_sel_hi:[1,0] neg_lo:[0,1] neg_hi:[0,1]\n"
    "v_pk_add_f32 v[86:87], v[86:87], v[126:127] op_sel:[0,0] op_sel_hi:[1,0] neg_lo:[0,1] neg_hi:[0,1]\n"
    "v_pk_add_f32 v[88:89], v[88:89], v[126:127] op_sel:[0,1] op_sel_hi:[1,1] neg_lo:[0,1] neg_hi:[0,1]\n"
    "v_pk_add_f32 v[90:91], v[90:91], v[126:127] op_sel:[0,1] op_sel_hi:[1,1] neg_lo:[0,1] neg_hi:[0,1]\n"
    "v_pk_add_f32 v[92:93], v[92:93], v[126:127] op_sel:[0,1] op_sel_hi:[1,1] neg_lo:[0,1] neg_hi:[0,1]\n"
    "v_pk_add_f32 v[94:95], v[94:95], v[126:127] op_sel:[0,1] op_sel_hi:[1,1] neg_lo:[0,1] neg_hi:[0,1]\n"
    "v_exp_f32 v64, v64\n"
    "v_exp_f32 v65, v65\n"
    "v_exp_f32 v66, v66\n"
    "v_exp_f32 v67, v67\n"
    "v_exp_f32 v68, v68\n"
    "v_exp_f32 v69, v69\n"
    "v_exp_f32 v70, v70\n"
    "v_exp_f32 v71, v71\n"
    "s_sub_u32 s61, s61, 0x14000\n"
    "s_cselect_b32 s61, 0xb4000, s61\n"
    "buffer_load_dwordx4 a[192:195], v20, s[12:15], s61 offen offset:0\n"
    "buffer_load_dwordx4 a[196:199], v21, s[12:15], s61 offen offset:1024\n"
    "v_exp_f32 v72, v72\n"
    "v_exp_f32 v73, v73\n"
    "v_exp_f32 v74, v74\n"
    "v_exp_f32 v75, v75\n"
    "v_exp_f32 v76, v76\n"
    "v_exp_f32 v77, v77\n"
    "v_exp_f32 v78, v78\n"
    "v_exp_f32 v79, v79\n"
    "v_exp_f32 v80, v80\n"
    "v_exp_f32 v81, v81\n"
    "v_exp_f32 v82, v82\n"
    "v_exp_f32 v83, v83\n"
    "v_exp_f32 v84, v84\n"
    "v_exp_f32 v85, v85\n"
    "v_exp_f32 v86, v86\n"
    "s_sub_u32 s62, s62, 0x14000\n"
    "s_cselect_b32 s62, 0xb4000, s62\n"
    "buffer_load_dwordx4 a[200:203], v20, s[12:15], s62 offen offset:0\n"
    "buffer_load_dwordx4 a[204:207], v21, s[12:15], s62 offen offset:1024\n"
    "v_exp_f32 v87, v87\n"
    "v_exp_f32 v88, v88\n"
    "v_exp_f32 v89, v89\n"
    "v_exp_f32 v90, v90\n"
    "v_exp_f32 v91, v91\n"
    "v_exp_f32 v92, v92\n"
    "v_exp_f32 v93, v93\n"
    "v_exp_f32 v94, v94\n"
    "v_exp_f32 v95, v95\n"
    "v_cvt_pk_bf16_f32 v96, v64, v65\n"
    "v_cvt_pk_bf16_f32 v97, v66, v67\n"
    "v_cvt_pk_bf16_f32 v98, v68, v69\n"
    "v_cvt_pk_bf16_f32 v99, v70, v71\n"
    "v_cvt_pk_bf16_f32 v100, v72, v73\n"
    "v_cvt_pk_bf16_f32 v101, v74, v75\n"
    "v_cvt_pk_bf16_f32 v102, v76, v77\n"
    "v_cvt_pk_bf16_f32 v103, v78, v79\n"
    "v_cvt_pk_bf16_f32 v104, v80, v81\n"
    "v_cvt_pk_bf16_f32 v105, v82, v83\n"
    "v_cvt_pk_bf16_f32 v106, v84, v85\n"
    "v_cvt_pk_bf16_f32 v107, v86, v87\n"
    "s_sub_u32 s61, s61, 0x14000\n"
    "s_cselect_b32 s61, 0xb4000, s61\n"
    "buffer_load_dwordx4 a[208:211], v20, s[12:15], s61 offen offset:0\n"
    "buffer_load_dwordx4 a[212:215], v21, s[12:15], s61 offen offset:1024\n"
    "v_cvt_pk_bf16_f32 v108, v88, v89\n"
    "v_cvt_pk_bf16_f32 v109, v90, v91\n"
    "v_cvt_pk_bf16_f32 v110, v92, v93\n"
    "v_cvt_pk_bf16_f32 v111, v94, v95\n"
    "v_pk_add_f32 v[64:65], v[64:65], v[68:69] op_sel:[0,0] op_sel_hi:[1,1]\n"
    "v_pk_add_f32 v[72:73], v[72:73], v[76:77] op_sel:[0,0] op_sel_hi:[1,1]\n"
    "v_pk_add_f32 v[80:81], v[80:81], v[84:85] op_sel:[0,0] op_sel_hi:[1,1]\n"
    "v_pk_add_f32 v[88:89], v[88:89], v[92:93] op_sel:[0,0] op_sel_hi:[1,1]\n"
    "v_pk_add_f32 v[66:67], v[66:67], v[70:71] op_sel:[0,0] op_sel_hi:[1,1]\n"
    "v_pk_add_f32 v[74:75], v[74:75], v[78:79] op_sel:[0,0] op_sel_hi:[1,1]\n"
    "v_pk_add_f32 v[82:83], v[82:83], v[86:87] op_sel:[0,0] op_sel_hi:[1,1]\n"
    "v_pk_add_f32 v[90:91], v[90:91], v[94:95] op_sel:[0,0] op_sel_hi:[1,1]\n"
    "v_pk_add_f32 v[64:65], v[64:65], v[66:67] op_sel:[0,0] op_sel_hi:[1,1]\n"
    "v_pk_add_f32 v[72:73], v[72:73], v[74:75] op_sel:[0,0] op_sel_hi:[1,1]\n"
    "v_pk_add_f32 v[80:81], v[80:81], v[82:83] op_sel:[0,0] op_sel_hi:[1,1]\n"
    "v_pk_add_f32 v[88:89], v[88:89], v[90:91] op_sel:[0,0] op_sel_hi:[1,1]\n"
    "v_add_f32 v204, v64, v65\n"
    "v_add_f32 v205, v72, v73\n"
    "v_add_f32 v206, v80, v81\n"
    "v_add_f32 v207, v88, v89\n"
    "v_mfma_f32_16x16x32_bf16 v[64:67], v[230:233], v[96:99], 0\n"
    "s_sub_u32 s62, s62, 0x14000\n"
    "s_cselect_b32 s62, 0xb4000, s62\n"
    "v_mfma_f32_16x16x32_bf16 v[68:71], v[236:239], v[96:99], 0\n"
    "buffer_load_dwordx4 a[216:219], v20, s[12:15], s62 offen offset:0\n"
    "v_mfma_f32_16x16x32_bf16 v[72:75], v[236:239], v[100:103], 0\n"
    "v_mfma_f32_16x16x32_bf16 v[76:79], v[242:245], v[100:103], 0\n"
    "v_mfma_f32_16x16x32_bf16 v[80:83], v[232:235], v[104:107], 0\n"
    "buffer_load_dwordx4 a[220:223], v21, s[12:15], s62 offen offset:1024\n"
    "v_mfma_f32_16x16x32_bf16 v[84:87], v[238:241], v[104:107], 0\n"
    "s_sub_u32 s61, s61, 0x14000\n"
    "s_cselect_b32 s61, 0xb4000, s61\n"
    "v_mfma_f32_16x16x32_bf16 v[88:91], v[238:241], v[108:111], 0\n"
    "v_mfma_f32_16x16x32_bf16 v[92:95], v[244:247], v[108:111], 0\n"
    "v_mov_b32 v200, v204\n"
    "v_mov_b32 v201, v205\n"
    "v_mov_b32 v202, v206\n"
    "v_mov_b32 v203, v207\n"
    "v_permlane16_swap_b32 v204, v200\n"
    "v_permlane16_swap_b32 v205, v201\n"
    "v_permlane16_swap_b32 v206, v202\n"
    "v_permlane16_swap_b32 v207, v203\n"
    "buffer_load_dwordx4 a[224:227], v20, s[12:15], s61 offen offset:0\n"
    "buffer_load_dwordx4 a[228:231], v21, s[12:15], s61 offen offset:1024\n"
    "v_add_f32 v204, v204, v200\n"
    "v_add_f32 v205, v205, v201\n"
    "v_add_f32 v206, v206, v202\n"
    "v_add_f32 v207, v207, v203\n"
    "v_mov_b32 v200, v204\n"
    "v_mov_b32 v201, v205\n"
    "v_mov_b32 v202, v206\n"
    "v_mov_b32 v203, v207\n"
    "v_permlane32_swap_b32 v204, v200\n"
    "v_permlane32_swap_b32 v205, v201\n"
    "v_permlane32_swap_b32 v206, v202\n"
    "v_permlane32_swap_b32 v207, v203\n"
    "v_add_f32 v204, v204, v200\n"
    "v_add_f32 v205, v205, v201\n"
    "v_add_f32 v206, v206, v202\n"
    "v_add_f32 v207, v207, v203\n"
    "v_rcp_f32 v204, v204\n"
    "v_rcp_f32 v205, v205\n"
    "v_rcp_f32 v206, v206\n"
    "v_rcp_f32 v207, v207\n"
    "v_pk_mul_f32 v[64:65], v[64:65], v[204:205] op_sel:[0,0] op_sel_hi:[1,0]\n"
    "v_pk_mul_f32 v[66:67], v[66:67], v[204:205] op_sel:[0,0] op_sel_hi:[1,0]\n"
    "s_sub_u32 s62, s62, 0x14000\n"
    "s_cselect_b32 s62, 0xb4000, s62\n"
    "buffer_load_dwordx4 a[232:235], v20, s[12:15], s62 offen offset:0\n"
    "buffer_load_dwordx4 a[236:239], v21, s[12:15], s62 offen offset:1024\n"
    "v_pk_mul_f32 v[68:69], v[68:69], v[204:205] op_sel:[0,0] op_sel_hi:[1,0]\n"
    "v_pk_mul_f32 v[70:71], v[70:71], v[204:205] op_sel:[0,0] op_sel_hi:[1,0]\n"
    "v_pk_mul_f32 v[72:73], v[72:73], v[204:205] op_sel:[0,1] op_sel_hi:[1,1]\n"
    "v_pk_mul_f32 v[74:75], v[74:75], v[204:205] op_sel:[0,1] op_sel_hi:[1,1]\n"
    "v_pk_mul_f32 v[76:77], v[76:77], v[204:205] op_sel:[0,1] op_sel_hi:[1,1]\n"
    "v_pk_mul_f32 v[78:79], v[78:79], v[204:205] op_sel:[0,1] op_sel_hi:[1,1]\n"
    "v_pk_mul_f32 v[80:81], v[80:81], v[206:207] op_sel:[0,0] op_sel_hi:[1,0]\n"
    "v_pk_mul_f32 v[82:83], v[82:83], v[206:207] op_sel:[0,0] op_sel_hi:[1,0]\n"
    "v_pk_mul_f32 v[84:85], v[84:85], v[206:207] op_sel:[0,0] op_sel_hi:[1,0]\n"
    "v_pk_mul_f32 v[86:87], v[86:87], v[206:207] op_sel:[0,0] op_sel_hi:[1,0]\n"
    "v_pk_mul_f32 v[88:89], v[88:89], v[206:207] op_sel:[0,1] op_sel_hi:[1,1]\n"
    "v_pk_mul_f32 v[90:91], v[90:91], v[206:207] op_sel:[0,1] op_sel_hi:[1,1]\n"
    "v_pk_mul_f32 v[92:93], v[92:93], v[206:207] op_sel:[0,1] op_sel_hi:[1,1]\n"
    "v_pk_mul_f32 v[94:95], v[94:95], v[206:207] op_sel:[0,1] op_sel_hi:[1,1]\n"
    "s_mov_b32 s90, 0xffff\n"
    "s_mov_b32 s91, 0\n"
    "buffer_load_dwordx2 v[6:7], v10, s[36:39], 0 offen offset:0 sc1\n"
    "v_cndmask_b32 v72, v72, v68, s[90:91]\n"
    "v_cndmask_b32 v73, v73, v69, s[90:91]\n"
    "v_cndmask_b32 v74, v74, v70, s[90:91]\n"
    "v_cndmask_b32 v75, v75, v71, s[90:91]\n"
    "v_cvt_pk_bf16_f32 v96, v64, v65\n"
    "v_cvt_pk_bf16_f32 v97, v66, v67\n"
    "v_cvt_pk_bf16_f32 v98, v72, v73\n"
    "v_cvt_pk_bf16_f32 v99, v74, v75\n"
    "v_cvt_pk_bf16_f32 v100, v76, v77\n"
    "v_cvt_pk_bf16_f32 v101, v78, v79\n"
    "v_permlane16_swap_b32 v96, v98\n"
    "v_permlane16_swap_b32 v97, v99\n"
    "buffer_store_dwordx4 v[96:99], v44, s[36:39], 0 offen offset:0 sc1\n"
    "buffer_store_dwordx2 v[100:101], v45, s[36:39], 0 offen offset:0 sc1\n"
    "v_cndmask_b32 v88, v88, v84, s[90:91]\n"
    "v_cndmask_b32 v89, v89, v85, s[90:91]\n"
    "v_cndmask_b32 v90, v90, v86, s[90:91]\n"
    "v_cndmask_b32 v91, v91, v87, s[90:91]\n"
    "v_cvt_pk_bf16_f32 v104, v80, v81\n"
    "v_cvt_pk_bf16_f32 v105, v82, v83\n"
    "v_cvt_pk_bf16_f32 v106, v88, v89\n"
    "v_cvt_pk_bf16_f32 v107, v90, v91\n"
    "v_cvt_pk_bf16_f32 v108, v92, v93\n"
    "v_cvt_pk_bf16_f32 v109, v94, v95\n"
    "v_permlane16_swap_b32 v104, v106\n"
    "v_permlane16_swap_b32 v105, v107\n"
    "buffer_store_dwordx4 v[104:107], v44, s[36:39], 0 offen offset:256 sc1\n"
    "buffer_store_dwordx2 v[108:109], v45, s[36:39], 0 offen offset:256 sc1\n"
    "s_nop 7\n"
    "s_or_b32 s64, s40, s41\n"
    "s_cmp_eq_u32 s64, 0\n"
    "s_cbranch_scc1 .Lgen0\n"
    ".Lgen9:\n"
    "s_lshl_b32 s64, s52, 7\n"
    "s_lshl_b32 s65, s41, 5\n"
    "s_add_u32 s64, s64, s65\n"
    "v_and_b32 v58, 31, v1\n"
    "v_lshrrev_b32 v59, 5, v1\n"
    "s_lshl_b32 s65, s64, 4\n"
    "s_add_u32 s67, s65, 0x66e000\n"
    "s_cmp_lt_u32 s42, 28\n"
    "s_cselect_b32 s67, s67, 0x40000000\n"
    "v_mul_u32_u24 v60, 0x8080, v59\n"
    "v_lshl_add_u32 v60, v58, 4, v60\n"
    "v_add_u32 v38, s67, v60\n"
    "v_add_u32 v39, 0x10100, v38\n"
    "v_add_u32 v40, 0x20200, v38\n"
    "v_add_u32 v41, 0x30300, v38\n"
    "s_mul_i32 s67, s53, 0x80800\n"
    "s_add_u32 s67, s67, s65\n"
    "s_add_u32 s67, s67, 0xb73800\n"
    "s_cmp_lt_u32 s42, 28\n"
    "s_cselect_b32 s67, s67, 0x40000000\n"
    "v_add_u32 v42, s67, v60\n"
    "s_mov_b32 s67, 0x40400\n"
    "s_cmp_eq_u32 s53, 13\n"
    "s_cselect_b32 s67, 0x40000000, s67\n"
    "v_add_u32 v43, s67, v42\n"
    "s_lshl_b32 s65, s64, 3\n"
    "s_add_u32 s65, s65, 0x47000\n"
    "s_cmp_lt_u32 s42, 28\n"
    "s_cselect_b32 s65, s65, 0x40000000\n"
    "v_mul_u32_u24 v59, 0x28000, v59\n"
    "v_lshl_add_u32 v58, v58, 3, v59\n"
    "v_add_u32 v45, s65, v58\n"
    "v_bfe_u32 v58, v1, 3, 1\n"
    "v_lshlrev_b32 v58, 1, v58\n"
    "v_lshrrev_b32 v59, 5, v1\n"
    "v_or_b32 v58, v58, v59\n"
    "v_and_b32 v60, 1, v2\n"
    "v_xor_b32 v58, v58, v60\n"
    "v_lshlrev_b32 v58, 5, v58\n"
    "v_lshl_add_u32 v58, v59, 11, v58\n"
    "v_bfe_u32 v59, v1, 2, 2\n"
    "v_lshl_add_u32 v58, v59, 7, v58\n"
    "v_and_b32 v59, 3, v1\n"
    "v_lshl_add_u32 v26, v59, 3, v58\n"
    "v_xor_b32 v27, 64, v26\n"
    "v_add_u32 v28, 0x10000, v26\n"
    "v_add_u32 v29, 0x10000, v27\n"
    "v_add_u32 v30, 0x20000, v26\n"
    "v_add_u32 v31, 0x20000, v27\n"
    "s_mul_i32 s58, s74, 0x40400\n"
    "s_mov_b32 s69, 0x10100\n"
    "s_mov_b32 s70, 0x20200\n"
    "s_mov_b32 s71, 0x30300\n"
    "s_waitcnt vmcnt(4)\n"
    "v_cmp_eq_u64 vcc, v[6:7], v[4:5]\n"
    "s_or_b32 vcc_lo, vcc_lo, s79\n"
    "s_cmp_eq_u32 vcc_lo, -1\n"
    "s_cbranch_scc0 .Lspin_X1\n"
    ".Ldone_X1:\n"
    "s_barrier\n"
    "buffer_load_dwordx4 a[0:3], v38, s[36:39], s58 offen offset:0 sc1\n"
    "buffer_load_dwordx4 a[4:7], v39, s[36:39], s58 offen offset:0 sc1\n"
    "buffer_load_dwordx4 a[8:11], v40, s[36:39], s58 offen offset:0 sc1\n"
    "buffer_load_dwordx4 a[12:15], v41, s[36:39], s58 offen offset:0 sc1\n"
    "s_sub_u32 s58, s58, 0x40400\n"
    "s_cselect_b32 s58, 0x242400, s58\n"
    "buffer_load_dwordx4 a[16:19], v38, s[36:39], s58 offen offset:0 sc1\n"
    "buffer_load_dwordx4 a[20:23], v39, s[36:39], s58 offen offset:0 sc1\n"
    "buffer_load_dwordx4 a[24:27], v40, s[36:39], s58 offen offset:0 sc1\n"
    "buffer_load_dwordx4 a[28:31], v41, s[36:39], s58 offen offset:0 sc1\n"
    "s_sub_u32 s58, s58, 0x40400\n"
    "s_cselect_b32 s58, 0x242400, s58\n"
    "buffer_load_dwordx4 a[32:35], v38, s[36:39], s58 offen offset:0 sc1\n"
    "buffer_load_dwordx4 a[36:39], v39, s[36:39], s58 offen offset:0 sc1\n"
    "buffer_load_dwordx4 a[40:43], v40, s[36:39], s58 offen offset:0 sc1\n"
    "buffer_load_dwordx4 a[44:47], v41, s[36:39], s58 offen offset:0 sc1\n"
    "s_sub_u32 s58, s58, 0x40400\n"
    "s_cselect_b32 s58, 0x242400, s58\n"
    "buffer_load_dwordx4 a[48:51], v38, s[36:39], s58 offen offset:0 sc1\n"
    "ds_read_b64_tr_b16 v[192:193], v28 offset:16384\n"
    "ds_read_b64_tr_b16 v[194:195], v28 offset:16896\n"
    "ds_read_b64_tr_b16 v[196:197], v29 offset:16384\n"
    "ds_read_b64_tr_b16 v[198:199], v29 offset:16896\n"
    "ds_read_b64_tr_b16 v[200:201], v28 offset:20480\n"
    "ds_read_b64_tr_b16 v[202:203], v28 offset:20992\n"
    "ds_read_b64_tr_b16 v[204:205], v29 offset:20480\n"
    "ds_read_b64_tr_b16 v[206:207], v29 offset:20992\n"
    "s_waitcnt vmcnt(12) lgkmcnt(4)\n"
    "v_mfma_f32_32x32x16_bf16 v[64:79], v[192:195], a[0:3], 0\n"
    "ds_read_b64_tr_b16 v[208:209], v28 offset:17408\n"
    "ds_read_b64_tr_b16 v[210:211], v28 offset:17920\n"
    "ds_read_b64_tr_b16 v[212:213], v29 offset:17408\n"
    "buffer_load_dwordx4 a[52:55], v39, s[36:39], s58 offen offset:0 sc1\n"
    "v_mfma_f32_32x32x16_bf16 v[80:95], v[196:199], a[0:3], 0\n"
    "ds_read_b64_tr_b16 v[214:215], v29 offset:17920\n"
    "ds_read_b64_tr_b16 v[216:217], v28 offset:21504\n"
    "ds_read_b64_tr_b16 v[218:219], v28 offset:22016\n"
    "s_waitcnt vmcnt(12) lgkmcnt(8)\n"
    "v_mfma_f32_32x32x16_bf16 v[64:79], v[200:203], a[4:7], v[64:79]\n"
    "ds_read_b64_tr_b16 v[220:221], v29 offset:21504\n"
    "buffer_load_dwordx4 a[56:59], v40, s[36:39], s58 offen offset:0 sc1\n"
    "ds_read_b64_tr_b16 v[222:223], v29 offset:22016\n"
    "s_waitcnt lgkmcnt(8)\n"
    "v_mfma_f32_32x32x16_bf16 v[80:95], v[204:207], a[4:7], v[80:95]\n"
    "s_waitcnt vmcnt(12) lgkmcnt(4)\n"
    "v_mfma_f32_32x32x16_bf16 v[64:79], v[208:211], a[8:11], v[64:79]\n"
    "ds_read_b64_tr_b16 v[192:193], v28 offset:24576\n"
    "ds_read_b64_tr_b16 v[194:195], v28 offset:25088\n"
    "ds_read_b64_tr_b16 v[196:197], v29 offset:24576\n"
    "buffer_load_dwordx4 a[60:63], v41, s[36:39], s58 offen offset:0 sc1\n"
    "s_sub_u32 s58, s58, 0x40400\n"
    "s_cselect_b32 s58, 0x242400, s58\n"
    "v_mfma_f32_32x32x16_bf16 v[80:95], v[212:215], a[8:11], v[80:95]\n"
    "ds_read_b64_tr_b16 v[198:199], v29 offset:25088\n"
    "ds_read_b64_tr_b16 v[200:201], v28 offset:28672\n"
    "ds_read_b64_tr_b16 v[202:203], v28 offset:29184\n"
    "buffer_load_dwordx4 a[64:67], v38, s[36:39], s58 offen offset:0 sc1\n"
    "s_waitcnt vmcnt(13) lgkmcnt(8)\n"
    "v_mfma_f32_32x32x16_bf16 v[64:79], v[216:219], a[12:15], v[64:79]\n"
    "ds_read_b64_tr_b16 v[204:205], v29 offset:28672\n"
    "ds_read_b64_tr_b16 v[206:207], v29 offset:29184\n"
    "s_waitcnt lgkmcnt(8)\n"
    "v_mfma_f32_32x32x16_bf16 v[80:95], v[220:223], a[12:15], v[80:95]\n"
    "s_waitcnt vmcnt(12) lgkmcnt(4)\n"
    "v_mfma_f32_32x32x16_bf16 v[64:79], v[192:195], a[16:19], v[64:79]\n"
    "ds_read_b64_tr_b16 v[208:209], v28 offset:25600\n"
    "ds_read_b64_tr_b16 v[210:211], v28 offset:26112\n"
    "ds_read_b64_tr_b16 v[212:213], v29 offset:25600\n"
    "buffer_load_dwordx4 a[68:71], v39, s[36:39], s58 offen offset:0 sc1\n"
    "v_mfma_f32_32x32x16_bf16 v[80:95], v[196:199], a[16:19], v[80:95]\n"
    "ds_read_b64_tr_b16 v[214:215], v29 offset:26112\n"
    "ds_read_b64_tr_b16 v[216:217], v28 offset:29696\n"
    "ds_read_b64_tr_b16 v[218:219], v28 offset:30208\n"
    "buffer_load_dwordx4 a[72:75], v40, s[36:39], s58 offen offset:0 sc1\n"
    "s_waitcnt vmcnt(13) lgkmcnt(8)\n"
    "v_mfma_f32_32x32x16_bf16 v[64:79], v[200:203], a[20:23], v[64:79]\n"
    "ds_read_b64_tr_b16 v[220:221], v29 offset:29696\n"
    "ds_read_b64_tr_b16 v[222:223], v29 offset:30208\n"
    "s_waitcnt lgkmcnt(8)\n"
    "v_mfma_f32_32x32x16_bf16 v[80:95], v[204:207], a[20:23], v[80:95]\n"
    "s_waitcnt vmcnt(12) lgkmcnt(4)\n"
    "s_barrier\n"
    "v_mfma_f32_32x32x16_bf16 v[64:79], v[208:211], a[24:27], v[64:79]\n"
    "buffer_store_dwordx2 v[4:5], v9, s[36:39], s87 offen offset:0 sc1\n"
    "ds_read_b64_tr_b16 v[192:193], v28 offset:32768\n"
    "ds_read_b64_tr_b16 v[194:195], v28 offset:33280\n"
    "buffer_load_dwordx4 a[76:79], v41, s[36:39], s58 offen offset:0 sc1\n"
    "s_sub_u32 s58, s58, 0x40400\n"
    "s_cselect_b32 s58, 0x242400, s58\n"
    "v_mfma_f32_32x32x16_bf16 v[80:95], v[212:215], a[24:27], v[80:95]\n"
    "ds_read_b64_tr_b16 v[196:197], v29 offset:32768\n"
    "ds_read_b64_tr_b16 v[198:199], v29 offset:33280\n"
    "ds_read_b64_tr_b16 v[200:201], v28 offset:36864\n"
    "buffer_load_dwordx4 a[80:83], v38, s[36:39], s58 offen offset:0 sc1\n"
    "s_waitcnt vmcnt(14) lgkmcnt(7)\n"
    "v_mfma_f32_32x32x16_bf16 v[64:79], v[216:219], a[28:31], v[64:79]\n"
    "ds_read_b64_tr_b16 v[202:203], v28 offset:37376\n"
    "ds_read_b64_tr_b16 v[204:205], v29 offset:36864\n"
    "ds_read_b64_tr_b16 v[206:207], v29 offset:37376\n"
    "s_waitcnt lgkmcnt(8)\n"
    "v_mfma_f32_32x32x16_bf16 v[80:95], v[220:223], a[28:31], v[80:95]\n"
    "s_waitcnt vmcnt(13) lgkmcnt(4)\n"
    "v_mfma_f32_32x32x16_bf16 v[64:79], v[192:195], a[32:35], v[64:79]\n"
    "ds_read_b64_tr_b16 v[208:209], v28 offset:33792\n"
    "ds_read_b64_tr_b16 v[210:211], v28 offset:34304\n"
    "ds_read_b64_tr_b16 v[212:213], v29 offset:33792\n"
    "buffer_load_dwordx4 a[84:87], v39, s[36:39], s58 offen offset:0 sc1\n"
    "v_mfma_f32_32x32x16_bf16 v[80:95], v[196:199], a[32:35], v[80:95]\n"
    "ds_read_b64_tr_b16 v[214:215], v29 offset:34304\n"
    "ds_read_b64_tr_b16 v[216:217], v28 offset:37888\n"
    "ds_read_b64_tr_b16 v[218:219], v28 offset:38400\n"
    "s_waitcnt vmcnt(13) lgkmcnt(8)\n"
    "v_mfma_f32_32x32x16_bf16 v[64:79], v[200:203], a[36:39], v[64:79]\n"
    "ds_read_b64_tr_b16 v[220:221], v29 offset:37888\n"
    "buffer_load_dwordx4 a[88:91], v40, s[36:39], s58 offen offset:0 sc1\n"
    "ds_read_b64_tr_b16 v[222:223], v29 offset:38400\n"
    "s_waitcnt lgkmcnt(8)\n"
    "v_mfma_f32_32x32x16_bf16 v[80:95], v[204:207], a[36:39], v[80:95]\n"
    "s_waitcnt vmcnt(13) lgkmcnt(4)\n"
    "v_mfma_f32_32x32x16_bf16 v[64:79], v[208:211], a[40:43], v[64:79]\n"
    "ds_read_b64_tr_b16 v[192:193], v28 offset:40960\n"
    "ds_read_b64_tr_b16 v[194:195], v28 offset:41472\n"
    "ds_read_b64_tr_b16 v[196:197], v29 offset:40960\n"
    "buffer_load_dwordx4 a[92:95], v41, s[36:39], s58 offen offset:0 sc1\n"
    "s_mov_b32 s1, 0\n"
    "v_mfma_f32_32x32x16_bf16 v[80:95], v[212:215], a[40:43], v[80:95]\n"
    "ds_read_b64_tr_b16 v[198:199], v29 offset:41472\n"
    "ds_read_b64_tr_b16 v[200:201], v28 offset:45056\n"
    "ds_read_b64_tr_b16 v[202:203], v28 offset:45568\n"
    "buffer_load_dwordx2 v[224:225], v45, s[36:39], s1 offen offset:0 sc1\n"
    "s_mov_b32 s1, 0x4000\n"
    "buffer_load_dwordx2 v[226:227], v45, s[36:39], s1 offen offset:0 sc1\n"
    "s_mov_b32 s1, 0x8000\n"
    "buffer_load_dwordx2 v[228:229], v45, s[36:39], s1 offen offset:0 sc1\n"
    "s_mov_b32 s1, 0xc000\n"
    "buffer_load_dwordx2 v[230:231], v45, s[36:39], s1 offen offset:0 sc1\n"
    "s_mov_b32 s1, 0x10000\n"
    "buffer_load_dwordx2 v[232:233], v45, s[36:39], s1 offen offset:0 sc1\n"
    "s_mov_b32 s1, 0x14000\n"
    "buffer_load_dwordx2 v[234:235], v45, s[36:39], s1 offen offset:0 sc1\n"
    "s_mov_b32 s1, 0x18000\n"
    "buffer_load_dwordx2 v[236:237], v45, s[36:39], s1 offen offset:0 sc1\n"
    "s_mov_b32 s1, 0x1c000\n"
    "buffer_load_dwordx2 v[238:239], v45, s[36:39], s1 offen offset:0 sc1\n"
    "s_mov_b32 s1, 0x20000\n"
    "buffer_load_dwordx2 v[240:241], v45, s[36:39], s1 offen offset:0 sc1\n"
    "s_mov_b32 s1, 0x24000\n"
    "buffer_load_dwordx2 v[242:243], v45, s[36:39], s1 offen offset:0 sc1\n"
    "s_sub_u32 s58, s58, 0x40400\n"
    "s_cselect_b32 s58, 0x242400, s58\n"
    "buffer_load_dwordx4 a[96:99], v38, s[36:39], s58 offen offset:0 sc1\n"
    "s_waitcnt vmcnt(24) lgkmcnt(8)\n"
    "v_mfma_f32_32x32x16_bf16 v[64:79], v[216:219], a[44:47], v[64:79]\n"
    "ds_read_b64_tr_b16 v[204:205], v29 offset:45056\n"
    "ds_read_b64_tr_b16 v[206:207], v29 offset:45568\n"
    "s_waitcnt lgkmcnt(8)\n"
    "v_mfma_f32_32x32x16_bf16 v[80:95], v[220:223], a[44:47], v[80:95]\n"
    "s_waitcnt vmcnt(23) lgkmcnt(4)\n"
    "v_mfma_f32_32x32x16_bf16 v[64:79], v[192:195], a[48:51], v[64:79]\n"
    "ds_read_b64_tr_b16 v[208:209], v28 offset:41984\n"
    "ds_read_b64_tr_b16 v[210:211], v28 offset:42496\n"
    "ds_read_b64_tr_b16 v[212:213], v29 offset:41984\n"
    "buffer_load_dwordx4 a[100:103], v39, s[36:39], s58 offen offset:0 sc1\n"
    "v_mfma_f32_32x32x16_bf16 v[80:95], v[196:199], a[48:51], v[80:95]\n"
    "ds_read_b64_tr_b16 v[214:215], v29 offset:42496\n"
    "ds_read_b64_tr_b16 v[216:217], v28 offset:46080\n"
    "ds_read_b64_tr_b16 v[218:219], v28 offset:46592\n"
    "buffer_load_dwordx4 a[104:107], v40, s[36:39], s58 offen offset:0 sc1\n"
    "s_waitcnt vmcnt(24) lgkmcnt(8)\n"
    "v_mfma_f32_32x32x16_bf16 v[64:79], v[200:203], a[52:55], v[64:79]\n"
    "ds_read_b64_tr_b16 v[220:221], v29 offset:46080\n"
    "ds_read_b64_tr_b16 v[222:223], v29 offset:46592\n"
    "s_waitcnt lgkmcnt(8)\n"
    "v_mfma_f32_32x32x16_bf16 v[80:95], v[204:207], a[52:55], v[80:95]\n"
    "s_waitcnt vmcnt(23) lgkmcnt(0)\n"
    "s_barrier\n"
    "v_mfma_f32_32x32x16_bf16 v[64:79], v[208:211], a[56:59], v[64:79]\n"
    "ds_read_b64_tr_b16 v[192:193], v28 offset:0\n"
    "ds_read_b64_tr_b16 v[194:195], v28 offset:512\n"
    "ds_read_b64_tr_b16 v[196:197], v29 offset:0\n"
    "buffer_load_dwordx4 a[108:111], v41, s[36:39], s58 offen offset:0 sc1\n"
    "s_sub_u32 s58, s58, 0x40400\n"
    "s_cselect_b32 s58, 0x242400, s58\n"
    "v_mfma_f32_32x32x16_bf16 v[80:95], v[212:215], a[56:59], v[80:95]\n"
    "ds_read_b64_tr_b16 v[198:199], v29 offset:512\n"
    "ds_read_b64_tr_b16 v[200:201], v28 offset:4096\n"
    "ds_read_b64_tr_b16 v[202:203], v28 offset:4608\n"
    "ds_write_b128 v47, a[160:163] offset:32768\n"
    "s_waitcnt vmcnt(23)\n"
    "v_mfma_f32_32x32x16_bf16 v[64:79], v[216:219], a[60:63], v[64:79]\n"
    "ds_read_b64_tr_b16 v[204:205], v29 offset:4096\n"
    "buffer_load_dwordx4 a[112:115], v38, s[36:39], s58 offen offset:0 sc1\n"
    "ds_read_b64_tr_b16 v[206:207], v29 offset:4608\n"
    "ds_write_b128 v47, a[164:167] offset:33792\n"
    "v_mfma_f32_32x32x16_bf16 v[80:95], v[220:223], a[60:63], v[80:95]\n"
    "s_waitcnt vmcnt(23) lgkmcnt(6)\n"
    "ds_write_b128 v47, a[168:171] offset:40960\n"
    "ds_write_b128 v47, a[172:175] offset:41984\n"
    "v_mfma_f32_32x32x16_bf16 v[64:79], v[192:195], a[64:67], v[64:79]\n"
    "ds_read_b64_tr_b16 v[208:209], v28 offset:1024\n"
    "ds_read_b64_tr_b16 v[210:211], v28 offset:1536\n"
    "ds_read_b64_tr_b16 v[212:213], v29 offset:1024\n"
    "buffer_load_dwordx4 a[116:119], v39, s[36:39], s58 offen offset:0 sc1\n"
    "v_mfma_f32_32x32x16_bf16 v[80:95], v[196:199], a[64:67], v[80:95]\n"
    "ds_read_b64_tr_b16 v[214:215], v29 offset:1536\n"
    "ds_read_b64_tr_b16 v[216:217], v28 offset:5120\n"
    "ds_read_b64_tr_b16 v[218:219], v28 offset:5632\n"
    "s_waitcnt vmcnt(23) lgkmcnt(12)\n"
    "v_mfma_f32_32x32x16_bf16 v[64:79], v[200:203], a[68:71], v[64:79]\n"
    "ds_read_b64_tr_b16 v[220:221], v29 offset:5120\n"
    "buffer_load_dwordx4 a[120:123], v40, s[36:39], s58 offen offset:0 sc1\n"
    "ds_read_b64_tr_b16 v[222:223], v29 offset:5632\n"
    "s_waitcnt lgkmcnt(11)\n"
    "v_mfma_f32_32x32x16_bf16 v[80:95], v[204:207], a[68:71], v[80:95]\n"
    "s_waitcnt vmcnt(23) lgkmcnt(4)\n"
    "v_mfma_f32_32x32x16_bf16 v[64:79], v[208:211], a[72:75], v[64:79]\n"
    "ds_read_b64_tr_b16 v[192:193], v28 offset:8192\n"
    "ds_read_b64_tr_b16 v[194:195], v28 offset:8704\n"
    "ds_read_b64_tr_b16 v[196:197], v29 offset:8192\n"
    "buffer_load_dwordx4 a[124:127], v41, s[36:39], s58 offen offset:0 sc1\n"
    "s_sub_u32 s58, s58, 0x40400\n"
    "s_cselect_b32 s58, 0x242400, s58\n"
    "v_mfma_f32_32x32x16_bf16 v[80:95], v[212:215], a[72:75], v[80:95]\n"
    "ds_read_b64_tr_b16 v[198:199], v29 offset:8704\n"
    "ds_read_b64_tr_b16 v[200:201], v28 offset:12288\n"
    "ds_read_b64_tr_b16 v[202:203], v28 offset:12800\n"
    "buffer_load_dwordx4 a[128:131], v38, s[36:39], s58 offen offset:0 sc1\n"
    "s_waitcnt vmcnt(23) lgkmcnt(8)\n"
    "v_mfma_f32_32x32x16_bf16 v[64:79], v[216:219], a[76:79], v[64:79]\n"
    "ds_read_b64_tr_b16 v[204:205], v29 offset:12288\n"
    "ds_read_b64_tr_b16 v[206:207], v29 offset:12800\n"
    "s_waitcnt lgkmcnt(8)\n"
    "v_mfma_f32_32x32x16_bf16 v[80:95], v[220:223], a[76:79], v[80:95]\n"
    "s_waitcnt vmcnt(22) lgkmcnt(4)\n"
    "v_mfma_f32_32x32x16_bf16 v[64:79], v[192:195], a[80:83], v[64:79]\n"
    "ds_read_b64_tr_b16 v[208:209], v28 offset:9216\n"
    "ds_read_b64_tr_b16 v[210:211], v28 offset:9728\n"
    "ds_read_b64_tr_b16 v[212:213], v29 offset:9216\n"
    "buffer_load_dwordx4 a[132:135], v39, s[36:39], s58 offen offset:0 sc1\n"
    "v_mfma_f32_32x32x16_bf16 v[80:95], v[196:199], a[80:83], v[80:95]\n"
    "ds_read_b64_tr_b16 v[214:215], v29 offset:9728\n"
    "ds_read_b64_tr_b16 v[216:217], v28 offset:13312\n"
    "ds_read_b64_tr_b16 v[218:219], v28 offset:13824\n"
    "buffer_load_dwordx4 a[136:139], v40, s[36:39], s58 offen offset:0 sc1\n"
    "s_add_u32 m0, s56, 0x20000\n"
    "s_waitcnt vmcnt(23) lgkmcnt(8)\n"
    "v_mfma_f32_32x32x16_bf16 v[64:79], v[200:203], a[84:87], v[64:79]\n"
    "ds_read_b64_tr_b16 v[220:221], v29 offset:13312\n"
    "ds_read_b64_tr_b16 v[222:223], v29 offset:13824\n"
    "s_waitcnt lgkmcnt(8)\n"
    "v_mfma_f32_32x32x16_bf16 v[80:95], v[204:207], a[84:87], v[80:95]\n"
    "s_waitcnt vmcnt(22) lgkmcnt(0)\n"
    "s_barrier\n"
    "v_mfma_f32_32x32x16_bf16 v[64:79], v[208:211], a[88:91], v[64:79]\n"
    "ds_read_b64_tr_b16 v[192:193], v26 offset:0\n"
    "ds_read_b64_tr_b16 v[194:195], v26 offset:512\n"
    "buffer_load_dwordx4 v24, s[36:39], 0 offen offset:0 sc1 lds\n"
    "buffer_load_dwordx4 v25, s[36:39], 0 offen offset:1024 sc1 lds\n"
    "ds_read_b64_tr_b16 v[196:197], v27 offset:0\n"
    "buffer_load_dwordx4 a[140:143], v41, s[36:39], s58 offen offset:0 sc1\n"
    "v_mfma_f32_32x32x16_bf16 v[80:95], v[212:215], a[88:91], v[80:95]\n"
    "ds_read_b64_tr_b16 v[198:199], v27 offset:512\n"
    "ds_read_b64_tr_b16 v[200:201], v26 offset:4096\n"
    "ds_read_b64_tr_b16 v[202:203], v26 offset:4608\n"
    "s_sub_u32 s58, s58, 0x40400\n"
    "s_cselect_b32 s58, 0x242400, s58\n"
    "buffer_load_dwordx4 a[144:147], v38, s[36:39], s58 offen offset:0 sc1\n"
    "s_waitcnt vmcnt(25)\n"
    "v_mfma_f32_32x32x16_bf16 v[64:79], v[216:219], a[92:95], v[64:79]\n"
    "ds_read_b64_tr_b16 v[204:205], v27 offset:4096\n"
    "ds_read_b64_tr_b16 v[206:207], v27 offset:4608\n"
    "ds_write_b128 v47, a[176:179] offset:0\n"
    "v_mfma_f32_32x32x16_bf16 v[80:95], v[220:223], a[92:95], v[80:95]\n"
    "s_waitcnt vmcnt(14) lgkmcnt(5)\n"
    "ds_write_b128 v47, a[180:183] offset:1024\n"
    "ds_write_b128 v47, a[184:187] offset:8192\n"
    "v_mfma_f32_32x32x16_bf16 v[64:79], v[192:195], a[96:99], v[64:79]\n"
    "ds_read_b64_tr_b16 v[208:209], v26 offset:1024\n"
    "ds_read_b64_tr_b16 v[210:211], v26 offset:1536\n"
    "ds_read_b64_tr_b16 v[212:213], v27 offset:1024\n"
    "buffer_load_dwordx4 a[148:151], v39, s[36:39], s58 offen offset:0 sc1\n"
    "ds_write_b128 v47, a[188:191] offset:9216\n"
    "v_mfma_f32_32x32x16_bf16 v[80:95], v[196:199], a[96:99], v[80:95]\n"
    "ds_read_b64_tr_b16 v[214:215], v27 offset:1536\n"
    "ds_read_b64_tr_b16 v[216:217], v26 offset:5120\n"
    "ds_read_b64_tr_b16 v[218:219], v26 offset:5632\n"
    "s_waitcnt vmcnt(14) lgkmcnt(12)\n"
    "v_mfma_f32_32x32x16_bf16 v[64:79], v[200:203], a[100:103], v[64:79]\n"
    "ds_read_b64_tr_b16 v[220:221], v27 offset:5120\n"
    "ds_read_b64_tr_b16 v[222:223], v27 offset:5632\n"
    "s_waitcnt lgkmcnt(12)\n"
    "v_mfma_f32_32x32x16_bf16 v[80:95], v[204:207], a[100:103], v[80:95]\n"
    "buffer_load_dwordx4 a[152:155], v40, s[36:39], s58 offen offset:0 sc1\n"
    "s_waitcnt vmcnt(14) lgkmcnt(4)\n"
    "v_mfma_f32_32x32x16_bf16 v[64:79], v[208:211], a[104:107], v[64:79]\n"
    "ds_read_b64_tr_b16 v[192:193], v26 offset:8192\n"
    "ds_read_b64_tr_b16 v[194:195], v26 offset:8704\n"
    "ds_read_b64_tr_b16 v[196:197], v27 offset:8192\n"
    "v_mfma_f32_32x32x16_bf16 v[80:95], v[212:215], a[104:107], v[80:95]\n"
    "ds_read_b64_tr_b16 v[198:199], v27 offset:8704\n"
    "ds_read_b64_tr_b16 v[200:201], v26 offset:12288\n"
    "buffer_load_dwordx4 a[156:159], v41, s[36:39], s58 offen offset:0 sc1\n"
    "ds_read_b64_tr_b16 v[202:203], v26 offset:12800\n"
    "s_add_u32 m0, s56, 0x14000\n"
    "s_waitcnt vmcnt(14) lgkmcnt(8)\n"
    "v_mfma_f32_32x32x16_bf16 v[64:79], v[216:219], a[108:111], v[64:79]\n"
    "ds_read_b64_tr_b16 v[204:205], v27 offset:12288\n"
    "ds_read_b64_tr_b16 v[206:207], v27 offset:12800\n"
    "s_sub_u32 s59, s59, 0x36000\n"
    "s_cselect_b32 s59, 0x1e6000, s59\n"
    "s_waitcnt lgkmcnt(8)\n"
    "v_mfma_f32_32x32x16_bf16 v[80:95], v[220:223], a[108:111], v[80:95]\n"
    "s_mul_i32 s92, s93, 0x1000\n"
    "s_add_u32 s92, s92, 0x21000\n"
    "buffer_load_dwordx2 v[6:7], v8, s[36:39], s92 offen offset:0 sc1\n"
    "s_waitcnt vmcnt(14) lgkmcnt(4)\n"
    "v_mfma_f32_32x32x16_bf16 v[64:79], v[192:195], a[112:115], v[64:79]\n"
    "ds_read_b64_tr_b16 v[208:209], v26 offset:9216\n"
    "ds_read_b64_tr_b16 v[210:211], v26 offset:9728\n"
    "ds_read_b64_tr_b16 v[212:213], v27 offset:9216\n"
    "v_mfma_f32_32x32x16_bf16 v[80:95], v[196:199], a[112:115], v[80:95]\n"
    "ds_read_b64_tr_b16 v[214:215], v27 offset:9728\n"
    "ds_read_b64_tr_b16 v[216:217], v26 offset:13312\n"
    "ds_read_b64_tr_b16 v[218:219], v26 offset:13824\n"
    "s_waitcnt vmcnt(13) lgkmcnt(8)\n"
    "v_mfma_f32_32x32x16_bf16 v[64:79], v[200:203], a[116:119], v[64:79]\n"
    "ds_read_b64_tr_b16 v[220:221], v27 offset:13312\n"
    "ds_read_b64_tr_b16 v[222:223], v27 offset:13824\n"
    "s_waitcnt lgkmcnt(8)\n"
    "v_mfma_f32_32x32x16_bf16 v[80:95], v[204:207], a[116:119], v[80:95]\n"
    "s_waitcnt vmcnt(12) lgkmcnt(0)\n"
    "s_barrier\n"
    "v_mfma_f32_32x32x16_bf16 v[64:79], v[208:211], a[120:123], v[64:79]\n"
    "ds_read_b64_tr_b16 v[192:193], v28 offset:49152\n"
    "ds_read_b64_tr_b16 v[194:195], v28 offset:49664\n"
    "ds_read_b64_tr_b16 v[196:197], v29 offset:49152\n"
    "ds_write_b128 v46, a[192:195] offset:0\n"
    "v_mfma_f32_32x32x16_bf16 v[80:95], v[212:215], a[120:123], v[80:95]\n"
    "ds_read_b64_tr_b16 v[198:199], v29 offset:49664\n"
    "ds_read_b64_tr_b16 v[200:201], v28 offset:53248\n"
    "ds_read_b64_tr_b16 v[202:203], v28 offset:53760\n"
    "ds_write_b128 v46, a[196:199] offset:1024\n"
    "s_waitcnt vmcnt(11)\n"
    "v_mfma_f32_32x32x16_bf16 v[64:79], v[216:219], a[124:127], v[64:79]\n"
    "ds_read_b64_tr_b16 v[204:205], v29 offset:53248\n"
    "ds_read_b64_tr_b16 v[206:207], v29 offset:53760\n"
    "ds_write_b128 v46, a[200:203] offset:8192\n"
    "v_mfma_f32_32x32x16_bf16 v[80:95], v[220:223], a[124:127], v[80:95]\n"
    "s_waitcnt vmcnt(10) lgkmcnt(6)\n"
    "ds_write_b128 v46, a[204:207] offset:9216\n"
    "v_add_f32 v224, v224, v226\n"
    "v_add_f32 v225, v225, v227\n"
    "v_mfma_f32_32x32x16_bf16 v[64:79], v[192:195], a[128:131], v[64:79]\n"
    "ds_read_b64_tr_b16 v[208:209], v28 offset:50176\n"
    "ds_read_b64_tr_b16 v[210:211], v28 offset:50688\n"
    "ds_read_b64_tr_b16 v[212:213], v29 offset:50176\n"
    "v_add_f32 v224, v224, v228\n"
    "v_add_f32 v225, v225, v229\n"
    "v_add_f32 v224, v224, v230\n"
    "v_mfma_f32_32x32x16_bf16 v[80:95], v[196:199], a[128:131], v[80:95]\n"
    "ds_read_b64_tr_b16 v[214:215], v29 offset:50688\n"
    "ds_read_b64_tr_b16 v[216:217], v28 offset:54272\n"
    "ds_read_b64_tr_b16 v[218:219], v28 offset:54784\n"
    "v_add_f32 v225, v225, v231\n"
    "v_add_f32 v224, v224, v232\n"
    "v_add_f32 v225, v225, v233\n"
    "s_waitcnt vmcnt(9) lgkmcnt(11)\n"
    "v_mfma_f32_32x32x16_bf16 v[64:79], v[200:203], a[132:135], v[64:79]\n"
    "ds_read_b64_tr_b16 v[220:221], v29 offset:54272\n"
    "ds_read_b64_tr_b16 v[222:223], v29 offset:54784\n"
    "v_add_f32 v224, v224, v234\n"
    "v_add_f32 v225, v225, v235\n"
    "v_add_f32 v224, v224, v236\n"
    "v_add_f32 v225, v225, v237\n"
    "s_waitcnt lgkmcnt(10)\n"
    "v_mfma_f32_32x32x16_bf16 v[80:95], v[204:207], a[132:135], v[80:95]\n"
    "s_waitcnt vmcnt(8) lgkmcnt(4)\n"
    "v_add_f32 v224, v224, v238\n"
    "v_add_f32 v225, v225, v239\n"
    "v_add_f32 v224, v224, v240\n"
    "v_add_f32 v225, v225, v241\n"
    "v_add_f32 v224, v224, v242\n"
    "v_add_f32 v225, v225, v243\n"
    "v_mfma_f32_32x32x16_bf16 v[64:79], v[208:211], a[136:139], v[64:79]\n"
    "ds_read_b64_tr_b16 v[192:193], v28 offset:57344\n"
    "ds_read_b64_tr_b16 v[194:195], v28 offset:57856\n"
    "ds_read_b64_tr_b16 v[196:197], v29 offset:57344\n"
    "buffer_load_dwordx4 v22, s[24:27], s59 offen offset:0 lds\n"
    "v_mov_b32 v58, v224\n"
    "v_mov_b32 v59, v225\n"
    "v_mfma_f32_32x32x16_bf16 v[80:95], v[212:215], a[136:139], v[80:95]\n"
    "ds_read_b64_tr_b16 v[198:199], v29 offset:57856\n"
    "ds_read_b64_tr_b16 v[200:201], v28 offset:61440\n"
    "ds_read_b64_tr_b16 v[202:203], v28 offset:61952\n"
    "v_permlane32_swap_b32 v224, v58\n"
    "buffer_load_dwordx4 v23, s[24:27], s59 offen offset:1024 lds\n"
    "s_waitcnt vmcnt(7) lgkmcnt(8)\n"
    "v_mfma_f32_32x32x16_bf16 v[64:79], v[216:219], a[140:143], v[64:79]\n"
    "ds_read_b64_tr_b16 v[204:205], v29 offset:61440\n"
    "ds_read_b64_tr_b16 v[206:207], v29 offset:61952\n"
    "s_add_u32 m0, s56, 0x16000\n"
    "s_sub_u32 s59, s59, 0x36000\n"
    "s_cselect_b32 s59, 0x1e6000, s59\n"
    "s_waitcnt lgkmcnt(8)\n"
    "v_mfma_f32_32x32x16_bf16 v[80:95], v[220:223], a[140:143], v[80:95]\n"
    "s_waitcnt vmcnt(6) lgkmcnt(4)\n"
    "v_permlane32_swap_b32 v225, v59\n"
    "v_add_f32 v58, v58, v224\n"
    "buffer_load_dwordx4 v22, s[24:27], s59 offen offset:0 lds\n"
    "v_add_f32 v59, v59, v225\n"
    "v_mul_f32 v58, 0xbacccccd, v58\n"
    "v_mfma_f32_32x32x16_bf16 v[64:79], v[192:195], a[144:147], v[64:79]\n"
    "ds_read_b64_tr_b16 v[208:209], v28 offset:58368\n"
    "ds_read_b64_tr_b16 v[210:211], v28 offset:58880\n"
    "ds_read_b64_tr_b16 v[212:213], v29 offset:58368\n"
    "v_mul_f32 v59, 0x3acccccd, v59\n"
    "v_fma_f32 v59, -v58, v58, v59\n"
    "v_max_f32 v59, 0, v59\n"
    "v_mfma_f32_32x32x16_bf16 v[80:95], v[196:199], a[144:147], v[80:95]\n"
    "ds_read_b64_tr_b16 v[214:215], v29 offset:58880\n"
    "ds_read_b64_tr_b16 v[216:217], v28 offset:62464\n"
    "ds_read_b64_tr_b16 v[218:219], v28 offset:62976\n"
    "buffer_load_dwordx4 v23, s[24:27], s59 offen offset:1024 lds\n"
    "v_add_f32 v59, 0x3727c5ac, v59\n"
    "s_waitcnt vmcnt(7) lgkmcnt(8)\n"
    "v_mfma_f32_32x32x16_bf16 v[64:79], v[200:203], a[148:151], v[64:79]\n"
    "ds_read_b64_tr_b16 v[220:221], v29 offset:62464\n"
    "ds_read_b64_tr_b16 v[222:223], v29 offset:62976\n"
    "v_rsq_f32 v60, v59\n"
    "v_cvt_pk_bf16_f32 v184, v58, v58\n"
    "v_mov_b32 v185, v184\n"
    "s_waitcnt lgkmcnt(8)\n"
    "v_mfma_f32_32x32x16_bf16 v[80:95], v[204:207], a[148:151], v[80:95]\n"
    "s_waitcnt vmcnt(4)\n"
    "v_cmp_eq_u64 vcc, v[6:7], v[4:5]\n"
    "s_or_b32 vcc_lo, vcc_lo, s80\n"
    "s_cmp_eq_u32 vcc_lo, -1\n"
    "s_cbranch_scc0 .Lspin_O\n"
    ".Ldone_O:\n"
    "s_waitcnt lgkmcnt(4)\n"
    "s_barrier\n"
    "s_mul_i32 s63, s54, 5\n"
    "s_add_u32 s63, s63, s75\n"
    "s_cmp_ge_u32 s63, 10\n"
    "s_cselect_b32 s1, 10, 0\n"
    "s_sub_u32 s63, s63, s1\n"
    "s_mul_i32 s63, s63, 0x40400\n"
    "v_add_u32 v34, 0x505800, v34\n"
    "v_add_u32 v35, 0x505800, v35\n"
    "v_add_u32 v36, 0x505800, v36\n"
    "v_add_u32 v37, 0x505800, v37\n"
    "buffer_load_dwordx4 a[160:163], v34, s[36:39], s63 offen offset:0 sc1\n"
    "v_mfma_f32_32x32x16_bf16 v[64:79], v[208:211], a[152:155], v[64:79]\n"
    "ds_read_b64_tr_b16 v[192:193], v30 offset:0\n"
    "ds_read_b64_tr_b16 v[194:195], v30 offset:512\n"
    "ds_read_b64_tr_b16 v[196:197], v31 offset:0\n"
    "buffer_load_dwordx4 a[164:167], v34, s[36:39], s63 offen offset:256 sc1\n"
    "v_mov_b32 v186, v184\n"
    "v_mov_b32 v187, v184\n"
    "v_mfma_f32_32x32x16_bf16 v[80:95], v[212:215], a[152:155], v[80:95]\n"
    "ds_read_b64_tr_b16 v[198:199], v31 offset:512\n"
    "v_mul_f32 v181, v60, v60\n"
    "v_mul_f32 v180, 0xc0135761, v60\n"
    "v_mul_f32 v182, v59, v60\n"
    "v_mul_f32 v181, 0x3d372713, v181\n"
    "v_mul_f32 v181, v181, v180\n"
    "s_waitcnt lgkmcnt(6)\n"
    "v_mfma_f32_32x32x16_bf16 v[64:79], v[216:219], a[156:159], v[64:79]\n"
    "buffer_load_dwordx4 a[168:171], v36, s[36:39], s63 offen offset:512 sc1\n"
    "ds_write_b128 v47, a[208:211] offset:49152\n"
    "s_waitcnt lgkmcnt(5)\n"
    "v_mfma_f32_32x32x16_bf16 v[80:95], v[220:223], a[156:159], v[80:95]\n"
    "s_waitcnt lgkmcnt(1)\n"
    "s_barrier\n"
    "v_mfma_f32_32x32x16_bf16 v[64:79], v[192:195], v[184:187], v[64:79]\n"
    "ds_read_b64_tr_b16 v[208:209], v26 offset:32768\n"
    "ds_read_b64_tr_b16 v[210:211], v26 offset:33280\n"
    "ds_read_b64_tr_b16 v[212:213], v27 offset:32768\n"
    "buffer_load_dwordx4 a[172:175], v35, s[36:39], s63 offen offset:0 sc1\n"
    "ds_write_b128 v47, a[212:215] offset:50176\n"
    "v_mfma_f32_32x32x16_bf16 v[80:95], v[196:199], v[184:187], v[80:95]\n"
    "ds_read_b64_tr_b16 v[214:215], v27 offset:33280\n"
    "ds_read_b64_tr_b16 v[216:217], v26 offset:36864\n"
    "ds_read_b64_tr_b16 v[218:219], v26 offset:37376\n"
    "ds_read_b64_tr_b16 v[220:221], v27 offset:36864\n"
    "ds_read_b64_tr_b16 v[222:223], v27 offset:37376\n"
    "s_nop 0\n"
    "v_pk_mul_f32 v[224:225], v[64:65], v[64:65] op_sel:[0,0] op_sel_hi:[1,1]\n"
    "v_pk_mul_f32 v[226:227], v[66:67], v[66:67] op_sel:[0,0] op_sel_hi:[1,1]\n"
    "v_pk_mul_f32 v[228:229], v[68:69], v[68:69] op_sel:[0,0] op_sel_hi:[1,1]\n"
    "v_pk_mul_f32 v[230:231], v[70:71], v[70:71] op_sel:[0,0] op_sel_hi:[1,1]\n"
    "v_pk_mul_f32 v[232:233], v[72:73], v[72:73] op_sel:[0,0] op_sel_hi:[1,1]\n"
    "v_pk_mul_f32 v[234:235], v[74:75], v[74:75] op_sel:[0,0] op_sel_hi:[1,1]\n"
    "v_pk_mul_f32 v[236:237], v[76:77], v[76:77] op_sel:[0,0] op_sel_hi:[1,1]\n"
    "v_pk_mul_f32 v[238:239], v[78:79], v[78:79] op_sel:[0,0] op_sel_hi:[1,1]\n"
    "buffer_load_dwordx4 a[176:179], v35, s[36:39], s63 offen offset:256 sc1\n"
    "buffer_load_dwordx4 a[180:183], v37, s[36:39], s63 offen offset:512 sc1\n"
    "v_pk_fma_f32 v[224:225], v[224:225], v[180:181], v[180:181] op_sel:[0,1,0] op_sel_hi:[1,1,0]\n"
    "v_pk_fma_f32 v[226:227], v[226:227], v[180:181], v[180:181] op_sel:[0,1,0] op_sel_hi:[1,1,0]\n"
    "v_pk_fma_f32 v[228:229], v[228:229], v[180:181], v[180:181] op_sel:[0,1,0] op_sel_hi:[1,1,0]\n"
    "v_pk_fma_f32 v[230:231], v[230:231], v[180:181], v[180:181] op_sel:[0,1,0] op_sel_hi:[1,1,0]\n"
    "v_pk_fma_f32 v[232:233], v[232:233], v[180:181], v[180:181] op_sel:[0,1,0] op_sel_hi:[1,1,0]\n"
    "v_pk_fma_f32 v[234:235], v[234:235], v[180:181], v[180:181] op_sel:[0,1,0] op_sel_hi:[1,1,0]\n"
    "v_pk_fma_f32 v[236:237], v[236:237], v[180:181], v[180:181] op_sel:[0,1,0] op_sel_hi:[1,1,0]\n"
    "v_pk_fma_f32 v[238:239], v[238:239], v[180:181], v[180:181] op_sel:[0,1,0] op_sel_hi:[1,1,0]\n"
    "v_pk_mul_f32 v[224:225], v[224:225], v[64:65] op_sel:[0,0] op_sel_hi:[1,1]\n"
    "v_pk_mul_f32 v[226:227], v[226:227], v[66:67] op_sel:[0,0] op_sel_hi:[1,1]\n"
    "v_pk_mul_f32 v[228:229], v[228:229], v[68:69] op_sel:[0,0] op_sel_hi:[1,1]\n"
    "v_pk_mul_f32 v[230:231], v[230:231], v[70:71] op_sel:[0,0] op_sel_hi:[1,1]\n"
    "v_pk_mul_f32 v[232:233], v[232:233], v[72:73] op_sel:[0,0] op_sel_hi:[1,1]\n"
    "v_pk_mul_f32 v[234:235], v[234:235], v[74:75] op_sel:[0,0] op_sel_hi:[1,1]\n"
    "v_pk_mul_f32 v[236:237], v[236:237], v[76:77] op_sel:[0,0] op_sel_hi:[1,1]\n"
    "v_pk_mul_f32 v[238:239], v[238:239], v[78:79] op_sel:[0,0] op_sel_hi:[1,1]\n"
    "v_exp_f32 v224, v224\n"
    "v_exp_f32 v225, v225\n"
    "v_exp_f32 v226, v226\n"
    "v_exp_f32 v227, v227\n"
    "v_exp_f32 v228, v228\n"
    "v_exp_f32 v229, v229\n"
    "s_sub_u32 s63, s63, 0x40400\n"
    "s_cselect_b32 s63, 0x242400, s63\n"
    "buffer_load_dwordx4 a[184:187], v34, s[36:39], s63 offen offset:0 sc1\n"
    "buffer_load_dwordx4 a[188:191], v34, s[36:39], s63 offen offset:256 sc1\n"
    "v_exp_f32 v230, v230\n"
    "v_exp_f32 v231, v231\n"
    "v_exp_f32 v232, v232\n"
    "v_exp_f32 v233, v233\n"
    "v_exp_f32 v234, v234\n"
    "v_exp_f32 v235, v235\n"
    "v_exp_f32 v236, v236\n"
    "v_exp_f32 v237, v237\n"
    "v_exp_f32 v238, v238\n"
    "v_exp_f32 v239, v239\n"
    "v_pk_fma_f32 v[224:225], v[224:225], v[182:183], v[182:183] op_sel:[0,0,0] op_sel_hi:[1,0,0]\n"
    "v_pk_fma_f32 v[226:227], v[226:227], v[182:183], v[182:183] op_sel:[0,0,0] op_sel_hi:[1,0,0]\n"
    "v_pk_fma_f32 v[228:229], v[228:229], v[182:183], v[182:183] op_sel:[0,0,0] op_sel_hi:[1,0,0]\n"
    "v_pk_fma_f32 v[230:231], v[230:231], v[182:183], v[182:183] op_sel:[0,0,0] op_sel_hi:[1,0,0]\n"
    "v_pk_fma_f32 v[232:233], v[232:233], v[182:183], v[182:183] op_sel:[0,0,0] op_sel_hi:[1,0,0]\n"
    "v_pk_fma_f32 v[234:235], v[234:235], v[182:183], v[182:183] op_sel:[0,0,0] op_sel_hi:[1,0,0]\n"
    "v_pk_fma_f32 v[236:237], v[236:237], v[182:183], v[182:183] op_sel:[0,0,0] op_sel_hi:[1,0,0]\n"
    "buffer_load_dwordx4 a[192:195], v36, s[36:39], s63 offen offset:512 sc1\n"
    "buffer_load_dwordx4 a[196:199], v35, s[36:39], s63 offen offset:0 sc1\n"
    "v_pk_fma_f32 v[238:239], v[238:239], v[182:183], v[182:183] op_sel:[0,0,0] op_sel_hi:[1,0,0]\n"
    "v_rcp_f32 v224, v224\n"
    "v_rcp_f32 v225, v225\n"
    "v_rcp_f32 v226, v226\n"
    "v_rcp_f32 v227, v227\n"
    "v_rcp_f32 v228, v228\n"
    "v_rcp_f32 v229, v229\n"
    "v_rcp_f32 v230, v230\n"
    "v_rcp_f32 v231, v231\n"
    "v_rcp_f32 v232, v232\n"
    "v_rcp_f32 v233, v233\n"
    "v_rcp_f32 v234, v234\n"
    "v_rcp_f32 v235, v235\n"
    "v_rcp_f32 v236, v236\n"
    "buffer_load_dwordx4 a[200:203], v35, s[36:39], s63 offen offset:256 sc1\n"
    "buffer_load_dwordx4 a[204:207], v37, s[36:39], s63 offen offset:512 sc1\n"
    "v_rcp_f32 v237, v237\n"
    "v_rcp_f32 v238, v238\n"
    "v_rcp_f32 v239, v239\n"
    "v_pk_mul_f32 v[64:65], v[64:65], v[224:225] op_sel:[0,0] op_sel_hi:[1,1]\n"
    "v_pk_mul_f32 v[66:67], v[66:67], v[226:227] op_sel:[0,0] op_sel_hi:[1,1]\n"
    "v_pk_mul_f32 v[68:69], v[68:69], v[228:229] op_sel:[0,0] op_sel_hi:[1,1]\n"
    "v_pk_mul_f32 v[70:71], v[70:71], v[230:231] op_sel:[0,0] op_sel_hi:[1,1]\n"
    "v_pk_mul_f32 v[72:73], v[72:73], v[232:233] op_sel:[0,0] op_sel_hi:[1,1]\n"
    "v_pk_mul_f32 v[74:75], v[74:75], v[234:235] op_sel:[0,0] op_sel_hi:[1,1]\n"
    "v_pk_mul_f32 v[76:77], v[76:77], v[236:237] op_sel:[0,0] op_sel_hi:[1,1]\n"
    "v_pk_mul_f32 v[78:79], v[78:79], v[238:239] op_sel:[0,0] op_sel_hi:[1,1]\n"
    "v_cvt_pk_bf16_f32 v240, v64, v65\n"
    "v_cvt_pk_bf16_f32 v241, v66, v67\n"
    "v_cvt_pk_bf16_f32 v242, v68, v69\n"
    "v_cvt_pk_bf16_f32 v243, v70, v71\n"
    "v_cvt_pk_bf16_f32 v244, v72, v73\n"
    "v_cvt_pk_bf16_f32 v245, v74, v75\n"
    "v_cvt_pk_bf16_f32 v246, v76, v77\n"
    "v_cvt_pk_bf16_f32 v247, v78, v79\n"
    "v_permlane32_swap_b32 v240, v242\n"
    "v_permlane32_swap_b32 v241, v243\n"
    "buffer_store_dwordx4 v[240:243], v42, s[36:39], 0 offen offset:0 sc1\n"
    "v_permlane32_swap_b32 v244, v246\n"
    "v_permlane32_swap_b32 v245, v247\n"
    "buffer_store_dwordx4 v[244:247], v42, s[36:39], s69 offen offset:0 sc1\n"
    "v_pk_mul_f32 v[224:225], v[80:81], v[80:81] op_sel:[0,0] op_sel_hi:[1,1]\n"
    "v_pk_mul_f32 v[226:227], v[82:83], v[82:83] op_sel:[0,0] op_sel_hi:[1,1]\n"
    "v_pk_mul_f32 v[228:229], v[84:85], v[84:85] op_sel:[0,0] op_sel_hi:[1,1]\n"
    "v_pk_mul_f32 v[230:231], v[86:87], v[86:87] op_sel:[0,0] op_sel_hi:[1,1]\n"
    "v_pk_mul_f32 v[232:233], v[88:89], v[88:89] op_sel:[0,0] op_sel_hi:[1,1]\n"
    "v_pk_mul_f32 v[234:235], v[90:91], v[90:91] op_sel:[0,0] op_sel_hi:[1,1]\n"
    "v_pk_mul_f32 v[236:237], v[92:93], v[92:93] op_sel:[0,0] op_sel_hi:[1,1]\n"
    "v_pk_mul_f32 v[238:239], v[94:95], v[94:95] op_sel:[0,0] op_sel_hi:[1,1]\n"
    "v_pk_fma_f32 v[224:225], v[224:225], v[180:181], v[180:181] op_sel:[0,1,0] op_sel_hi:[1,1,0]\n"
    "v_pk_fma_f32 v[226:227], v[226:227], v[180:181], v[180:181] op_sel:[0,1,0] op_sel_hi:[1,1,0]\n"
    "v_pk_fma_f32 v[228:229], v[228:229], v[180:181], v[180:181] op_sel:[0,1,0] op_sel_hi:[1,1,0]\n"
    "v_pk_fma_f32 v[230:231], v[230:231], v[180:181], v[180:181] op_sel:[0,1,0] op_sel_hi:[1,1,0]\n"
    "v_pk_fma_f32 v[232:233], v[232:233], v[180:181], v[180:181] op_sel:[0,1,0] op_sel_hi:[1,1,0]\n"
    "v_pk_fma_f32 v[234:235], v[234:235], v[180:181], v[180:181] op_sel:[0,1,0] op_sel_hi:[1,1,0]\n"
    "v_pk_fma_f32 v[236:237], v[236:237], v[180:181], v[180:181] op_sel:[0,1,0] op_sel_hi:[1,1,0]\n"
    "v_pk_fma_f32 v[238:239], v[238:239], v[180:181], v[180:181] op_sel:[0,1,0] op_sel_hi:[1,1,0]\n"
    "v_pk_mul_f32 v[224:225], v[224:225], v[80:81] op_sel:[0,0] op_sel_hi:[1,1]\n"
    "v_pk_mul_f32 v[226:227], v[226:227], v[82:83] op_sel:[0,0] op_sel_hi:[1,1]\n"
    "v_pk_mul_f32 v[228:229], v[228:229], v[84:85] op_sel:[0,0] op_sel_hi:[1,1]\n"
    "v_pk_mul_f32 v[230:231], v[230:231], v[86:87] op_sel:[0,0] op_sel_hi:[1,1]\n"
    "v_pk_mul_f32 v[232:233], v[232:233], v[88:89] op_sel:[0,0] op_sel_hi:[1,1]\n"
    "v_pk_mul_f32 v[234:235], v[234:235], v[90:91] op_sel:[0,0] op_sel_hi:[1,1]\n"
    "v_pk_mul_f32 v[236:237], v[236:237], v[92:93] op_sel:[0,0] op_sel_hi:[1,1]\n"
    "v_pk_mul_f32 v[238:239], v[238:239], v[94:95] op_sel:[0,0] op_sel_hi:[1,1]\n"
    "v_exp_f32 v224, v224\n"
    "v_exp_f32 v225, v225\n"
    "v_exp_f32 v226, v226\n"
    "v_exp_f32 v227, v227\n"
    "v_exp_f32 v228, v228\n"
    "v_exp_f32 v229, v229\n"
    "v_exp_f32 v230, v230\n"
    "v_exp_f32 v231, v231\n"
    "v_exp_f32 v232, v232\n"
    "v_exp_f32 v233, v233\n"
    "v_exp_f32 v234, v234\n"
    "v_exp_f32 v235, v235\n"
    "v_exp_f32 v236, v236\n"
    "v_exp_f32 v237, v237\n"
    "v_exp_f32 v238, v238\n"
    "v_exp_f32 v239, v239\n"
    "v_pk_fma_f32 v[224:225], v[224:225], v[182:183], v[182:183] op_sel:[0,0,0] op_sel_hi:[1,0,0]\n"
    "v_pk_fma_f32 v[226:227], v[226:227], v[182:183], v[182:183] op_sel:[0,0,0] op_sel_hi:[1,0,0]\n"
    "v_pk_fma_f32 v[228:229], v[228:229], v[182:183], v[182:183] op_sel:[0,0,0] op_sel_hi:[1,0,0]\n"
    "v_pk_fma_f32 v[230:231], v[230:231], v[182:183], v[182:183] op_sel:[0,0,0] op_sel_hi:[1,0,0]\n"
    "v_pk_fma_f32 v[232:233], v[232:233], v[182:183], v[182:183] op_sel:[0,0,0] op_sel_hi:[1,0,0]\n"
    "v_pk_fma_f32 v[234:235], v[234:235], v[182:183], v[182:183] op_sel:[0,0,0] op_sel_hi:[1,0,0]\n"
    "v_pk_fma_f32 v[236:237], v[236:237], v[182:183], v[182:183] op_sel:[0,0,0] op_sel_hi:[1,0,0]\n"
    "v_pk_fma_f32 v[238:239], v[238:239], v[182:183], v[182:183] op_sel:[0,0,0] op_sel_hi:[1,0,0]\n"
    "v_rcp_f32 v224, v224\n"
    "v_rcp_f32 v225, v225\n"
    "v_rcp_f32 v226, v226\n"
    "v_rcp_f32 v227, v227\n"
    "v_rcp_f32 v228, v228\n"
    "v_rcp_f32 v229, v229\n"
    "v_rcp_f32 v230, v230\n"
    "v_rcp_f32 v231, v231\n"
    "v_rcp_f32 v232, v232\n"
    "v_rcp_f32 v233, v233\n"
    "v_rcp_f32 v234, v234\n"
    "v_rcp_f32 v235, v235\n"
    "v_rcp_f32 v236, v236\n"
    "v_rcp_f32 v237, v237\n"
    "v_rcp_f32 v238, v238\n"
    "v_rcp_f32 v239, v239\n"
    "v_pk_mul_f32 v[80:81], v[80:81], v[224:225] op_sel:[0,0] op_sel_hi:[1,1]\n"
    "v_pk_mul_f32 v[82:83], v[82:83], v[226:227] op_sel:[0,0] op_sel_hi:[1,1]\n"
    "v_pk_mul_f32 v[84:85], v[84:85], v[228:229] op_sel:[0,0] op_sel_hi:[1,1]\n"
    "v_pk_mul_f32 v[86:87], v[86:87], v[230:231] op_sel:[0,0] op_sel_hi:[1,1]\n"
    "v_pk_mul_f32 v[88:89], v[88:89], v[232:233] op_sel:[0,0] op_sel_hi:[1,1]\n"
    "v_pk_mul_f32 v[90:91], v[90:91], v[234:235] op_sel:[0,0] op_sel_hi:[1,1]\n"
    "v_pk_mul_f32 v[92:93], v[92:93], v[236:237] op_sel:[0,0] op_sel_hi:[1,1]\n"
    "v_pk_mul_f32 v[94:95], v[94:95], v[238:239] op_sel:[0,0] op_sel_hi:[1,1]\n"
    "v_cvt_pk_bf16_f32 v248, v80, v81\n"
    "v_cvt_pk_bf16_f32 v249, v82, v83\n"
    "v_cvt_pk_bf16_f32 v250, v84, v85\n"
    "v_cvt_pk_bf16_f32 v251, v86, v87\n"
    "v_cvt_pk_bf16_f32 v252, v88, v89\n"
    "v_cvt_pk_bf16_f32 v253, v90, v91\n"
    "v_cvt_pk_bf16_f32 v254, v92, v93\n"
    "v_cvt_pk_bf16_f32 v255, v94, v95\n"
    "v_permlane32_swap_b32 v248, v250\n"
    "v_permlane32_swap_b32 v249, v251\n"
    "buffer_store_dwordx4 v[248:251], v42, s[36:39], s70 offen offset:0 sc1\n"
    "v_permlane32_swap_b32 v252, v254\n"
    "v_permlane32_swap_b32 v253, v255\n"
    "buffer_store_dwordx4 v[252:255], v42, s[36:39], s71 offen offset:0 sc1\n"
    "s_waitcnt lgkmcnt(4)\n"
    "v_mfma_f32_32x32x16_bf16 v[96:111], v[208:211], a[0:3], 0\n"
    "ds_read_b64_tr_b16 v[192:193], v26 offset:33792\n"
    "ds_read_b64_tr_b16 v[194:195], v26 offset:34304\n"
    "ds_read_b64_tr_b16 v[196:197], v27 offset:33792\n"
    "ds_write_b128 v47, a[216:219] offset:57344\n"
    "v_mfma_f32_32x32x16_bf16 v[112:127], v[212:215], a[0:3], 0\n"
    "ds_read_b64_tr_b16 v[198:199], v27 offset:34304\n"
    "ds_read_b64_tr_b16 v[200:201], v26 offset:37888\n"
    "ds_read_b64_tr_b16 v[202:203], v26 offset:38400\n"
    "ds_write_b128 v47, a[220:223] offset:58368\n"
    "s_waitcnt lgkmcnt(10)\n"
    "v_mfma_f32_32x32x16_bf16 v[96:111], v[216:219], a[4:7], v[96:111]\n"
    "ds_read_b64_tr_b16 v[204:205], v27 offset:37888\n"
    "ds_read_b64_tr_b16 v[206:207], v27 offset:38400\n"
    "s_waitcnt lgkmcnt(10)\n"
    "v_mfma_f32_32x32x16_bf16 v[112:127], v[220:223], a[4:7], v[112:127]\n"
    "s_waitcnt lgkmcnt(5)\n"
    "v_mfma_f32_32x32x16_bf16 v[96:111], v[192:195], a[8:11], v[96:111]\n"
    "ds_read_b64_tr_b16 v[208:209], v26 offset:40960\n"
    "ds_read_b64_tr_b16 v[210:211], v26 offset:41472\n"
    "ds_read_b64_tr_b16 v[212:213], v27 offset:40960\n"
    "v_mfma_f32_32x32x16_bf16 v[112:127], v[196:199], a[8:11], v[112:127]\n"
    "ds_read_b64_tr_b16 v[214:215], v27 offset:41472\n"
    "ds_read_b64_tr_b16 v[216:217], v26 offset:45056\n"
    "ds_read_b64_tr_b16 v[218:219], v26 offset:45568\n"
    "s_waitcnt lgkmcnt(9)\n"
    "v_mfma_f32_32x32x16_bf16 v[96:111], v[200:203], a[12:15], v[96:111]\n"
    "ds_read_b64_tr_b16 v[220:221], v27 offset:45056\n"
    "ds_read_b64_tr_b16 v[222:223], v27 offset:45568\n"
    "s_waitcnt lgkmcnt(8)\n"
    "v_mfma_f32_32x32x16_bf16 v[112:127], v[204:207], a[12:15], v[112:127]\n"
    "s_waitcnt lgkmcnt(4)\n"
    "v_mfma_f32_32x32x16_bf16 v[96:111], v[208:211], a[16:19], v[96:111]\n"
    "ds_read_b64_tr_b16 v[192:193], v26 offset:41984\n"
    "ds_read_b64_tr_b16 v[194:195], v26 offset:42496\n"
    "ds_read_b64_tr_b16 v[196:197], v27 offset:41984\n"
    "v_mfma_f32_32x32x16_bf16 v[112:127], v[212:215], a[16:19], v[112:127]\n"
    "ds_read_b64_tr_b16 v[198:199], v27 offset:42496\n"
    "ds_read_b64_tr_b16 v[200:201], v26 offset:46080\n"
    "ds_read_b64_tr_b16 v[202:203], v26 offset:46592\n"
    "s_waitcnt lgkmcnt(8)\n"
    "v_mfma_f32_32x32x16_bf16 v[96:111], v[216:219], a[20:23], v[96:111]\n"
    "ds_read_b64_tr_b16 v[204:205], v27 offset:46080\n"
    "ds_read_b64_tr_b16 v[206:207], v27 offset:46592\n"
    "s_waitcnt lgkmcnt(8)\n"
    "v_mfma_f32_32x32x16_bf16 v[112:127], v[220:223], a[20:23], v[112:127]\n"
    "s_waitcnt lgkmcnt(0)\n"
    "s_barrier\n"
    "v_mfma_f32_32x32x16_bf16 v[96:111], v[192:195], a[24:27], v[96:111]\n"
    "ds_read_b64_tr_b16 v[208:209], v26 offset:49152\n"
    "ds_read_b64_tr_b16 v[210:211], v26 offset:49664\n"
    "ds_read_b64_tr_b16 v[212:213], v27 offset:49152\n"
    "ds_write_b128 v46, a[224:227] offset:32768\n"
    "v_mfma_f32_32x32x16_bf16 v[112:127], v[196:199], a[24:27], v[112:127]\n"
    "ds_read_b64_tr_b16 v[214:215], v27 offset:49664\n"
    "ds_read_b64_tr_b16 v[216:217], v26 offset:53248\n"
    "ds_read_b64_tr_b16 v[218:219], v26 offset:53760\n"
    "ds_write_b128 v46, a[228:231] offset:33792\n"
    "v_mfma_f32_32x32x16_bf16 v[96:111], v[200:203], a[28:31], v[96:111]\n"
    "ds_read_b64_tr_b16 v[220:221], v27 offset:53248\n"
    "ds_read_b64_tr_b16 v[222:223], v27 offset:53760\n"
    "ds_write_b128 v46, a[232:235] offset:40960\n"
    "v_mfma_f32_32x32x16_bf16 v[112:127], v[204:207], a[28:31], v[112:127]\n"
    "s_waitcnt lgkmcnt(6)\n"
    "ds_write_b128 v46, a[236:239] offset:41984\n"
    "v_mfma_f32_32x32x16_bf16 v[96:111], v[208:211], a[32:35], v[96:111]\n"
    "ds_read_b64_tr_b16 v[192:193], v26 offset:50176\n"
    "ds_read_b64_tr_b16 v[194:195], v26 offset:50688\n"
    "ds_read_b64_tr_b16 v[196:197], v27 offset:50176\n"
    "v_mfma_f32_32x32x16_bf16 v[112:127], v[212:215], a[32:35], v[112:127]\n"
    "ds_read_b64_tr_b16 v[198:199], v27 offset:50688\n"
    "ds_read_b64_tr_b16 v[200:201], v26 offset:54272\n"
    "ds_read_b64_tr_b16 v[202:203], v26 offset:54784\n"
    "s_waitcnt lgkmcnt(11)\n"
    "v_mfma_f32_32x32x16_bf16 v[96:111], v[216:219], a[36:39], v[96:111]\n"
    "ds_read_b64_tr_b16 v[204:205], v27 offset:54272\n"
    "ds_read_b64_tr_b16 v[206:207], v27 offset:54784\n"
    "s_waitcnt lgkmcnt(10)\n"
    "v_mfma_f32_32x32x16_bf16 v[112:127], v[220:223], a[36:39], v[112:127]\n"
    "s_waitcnt lgkmcnt(4)\n"
    "v_mfma_f32_32x32x16_bf16 v[96:111], v[192:195], a[40:43], v[96:111]\n"
    "ds_read_b64_tr_b16 v[208:209], v26 offset:57344\n"
    "ds_read_b64_tr_b16 v[210:211], v26 offset:57856\n"
    "ds_read_b64_tr_b16 v[212:213], v27 offset:57344\n"
    "v_mfma_f32_32x32x16_bf16 v[112:127], v[196:199], a[40:43], v[112:127]\n"
    "ds_read_b64_tr_b16 v[214:215], v27 offset:57856\n"
    "ds_read_b64_tr_b16 v[216:217], v26 offset:61440\n"
    "ds_read_b64_tr_b16 v[218:219], v26 offset:61952\n"
    "s_waitcnt lgkmcnt(8)\n"
    "v_mfma_f32_32x32x16_bf16 v[96:111], v[200:203], a[44:47], v[96:111]\n"
    "ds_read_b64_tr_b16 v[220:221], v27 offset:61440\n"
    "ds_read_b64_tr_b16 v[222:223], v27 offset:61952\n"
    "s_waitcnt lgkmcnt(8)\n"
    "v_mfma_f32_32x32x16_bf16 v[112:127], v[204:207], a[44:47], v[112:127]\n"
    "s_waitcnt lgkmcnt(4)\n"
    "v_mfma_f32_32x32x16_bf16 v[96:111], v[208:211], a[48:51], v[96:111]\n"
    "ds_read_b64_tr_b16 v[192:193], v26 offset:58368\n"
    "ds_read_b64_tr_b16 v[194:195], v26 offset:58880\n"
    "ds_read_b64_tr_b16 v[196:197], v27 offset:58368\n"
    "v_mfma_f32_32x32x16_bf16 v[112:127], v[212:215], a[48:51], v[112:127]\n"
    "ds_read_b64_tr_b16 v[198:199], v27 offset:58880\n"
    "ds_read_b64_tr_b16 v[200:201], v26 offset:62464\n"
    "ds_read_b64_tr_b16 v[202:203], v26 offset:62976\n"
    "s_waitcnt lgkmcnt(8)\n"
    "v_mfma_f32_32x32x16_bf16 v[96:111], v[216:219], a[52:55], v[96:111]\n"
    "ds_read_b64_tr_b16 v[204:205], v27 offset:62464\n"
    "ds_read_b64_tr_b16 v[206:207], v27 offset:62976\n"
    "s_waitcnt lgkmcnt(8)\n"
    "v_mfma_f32_32x32x16_bf16 v[112:127], v[220:223], a[52:55], v[112:127]\n"
    "s_waitcnt vmcnt(0) lgkmcnt(4)\n"
    "s_barrier\n"
    "s_sub_u32 s63, s63, 0x40400\n"
    "s_cselect_b32 s63, 0x242400, s63\n"
    "buffer_load_dwordx4 a[208:211], v34, s[36:39], s63 offen offset:0 sc1\n"
    "v_mfma_f32_32x32x16_bf16 v[96:111], v[192:195], a[56:59], v[96:111]\n"
    "buffer_store_dwordx2 v[4:5], v9, s[36:39], s88 offen offset:0 sc1\n"
    "ds_read_b64_tr_b16 v[208:209], v26 offset:16384\n"
    "ds_read_b64_tr_b16 v[210:211], v26 offset:16896\n"
    "v_mfma_f32_32x32x16_bf16 v[112:127], v[196:199], a[56:59], v[112:127]\n"
    "ds_read_b64_tr_b16 v[212:213], v27 offset:16384\n"
    "ds_read_b64_tr_b16 v[214:215], v27 offset:16896\n"
    "ds_read_b64_tr_b16 v[216:217], v26 offset:20480\n"
    "buffer_load_dwordx4 a[212:215], v34, s[36:39], s63 offen offset:256 sc1\n"
    "s_waitcnt lgkmcnt(7)\n"
    "v_mfma_f32_32x32x16_bf16 v[96:111], v[200:203], a[60:63], v[96:111]\n"
    "ds_read_b64_tr_b16 v[218:219], v26 offset:20992\n"
    "ds_read_b64_tr_b16 v[220:221], v27 offset:20480\n"
    "ds_read_b64_tr_b16 v[222:223], v27 offset:20992\n"
    "s_waitcnt lgkmcnt(8)\n"
    "v_mfma_f32_32x32x16_bf16 v[112:127], v[204:207], a[60:63], v[112:127]\n"
    "s_waitcnt lgkmcnt(4)\n"
    "buffer_load_dwordx4 a[216:219], v36, s[36:39], s63 offen offset:512 sc1\n"
    "v_mfma_f32_32x32x16_bf16 v[96:111], v[208:211], a[64:67], v[96:111]\n"
    "ds_read_b64_tr_b16 v[192:193], v26 offset:17408\n"
    "ds_read_b64_tr_b16 v[194:195], v26 offset:17920\n"
    "ds_read_b64_tr_b16 v[196:197], v27 offset:17408\n"
    "buffer_load_dwordx4 a[220:223], v35, s[36:39], s63 offen offset:0 sc1\n"
    "v_mfma_f32_32x32x16_bf16 v[112:127], v[212:215], a[64:67], v[112:127]\n"
    "ds_read_b64_tr_b16 v[198:199], v27 offset:17920\n"
    "ds_read_b64_tr_b16 v[200:201], v26 offset:21504\n"
    "ds_read_b64_tr_b16 v[202:203], v26 offset:22016\n"
    "s_waitcnt lgkmcnt(8)\n"
    "v_mfma_f32_32x32x16_bf16 v[96:111], v[216:219], a[68:71], v[96:111]\n"
    "ds_read_b64_tr_b16 v[204:205], v27 offset:21504\n"
    "ds_read_b64_tr_b16 v[206:207], v27 offset:22016\n"
    "buffer_load_dwordx4 a[224:227], v35, s[36:39], s63 offen offset:256 sc1\n"
    "s_waitcnt lgkmcnt(8)\n"
    "v_mfma_f32_32x32x16_bf16 v[112:127], v[220:223], a[68:71], v[112:127]\n"
    "s_waitcnt lgkmcnt(4)\n"
    "v_mfma_f32_32x32x16_bf16 v[96:111], v[192:195], a[72:75], v[96:111]\n"
    "ds_read_b64_tr_b16 v[208:209], v26 offset:24576\n"
    "ds_read_b64_tr_b16 v[210:211], v26 offset:25088\n"
    "ds_read_b64_tr_b16 v[212:213], v27 offset:24576\n"
    "buffer_load_dwordx4 a[228:231], v37, s[36:39], s63 offen offset:512 sc1\n"
    "s_sub_u32 s63, s63, 0x40400\n"
    "s_cselect_b32 s63, 0x242400, s63\n"
    "v_mfma_f32_32x32x16_bf16 v[112:127], v[196:199], a[72:75], v[112:127]\n"
    "ds_read_b64_tr_b16 v[214:215], v27 offset:25088\n"
    "ds_read_b64_tr_b16 v[216:217], v26 offset:28672\n"
    "ds_read_b64_tr_b16 v[218:219], v26 offset:29184\n"
    "s_waitcnt lgkmcnt(8)\n"
    "v_mfma_f32_32x32x16_bf16 v[96:111], v[200:203], a[76:79], v[96:111]\n"
    "ds_read_b64_tr_b16 v[220:221], v27 offset:28672\n"
    "ds_read_b64_tr_b16 v[222:223], v27 offset:29184\n"
    "buffer_load_dwordx4 a[232:235], v34, s[36:39], s63 offen offset:0 sc1\n"
    "s_waitcnt lgkmcnt(8)\n"
    "v_mfma_f32_32x32x16_bf16 v[112:127], v[204:207], a[76:79], v[112:127]\n"
    "s_waitcnt lgkmcnt(4)\n"
    "v_mfma_f32_32x32x16_bf16 v[96:111], v[208:211], a[80:83], v[96:111]\n"
    "ds_read_b64_tr_b16 v[192:193], v26 offset:25600\n"
    "ds_read_b64_tr_b16 v[194:195], v26 offset:26112\n"
    "ds_read_b64_tr_b16 v[196:197], v27 offset:25600\n"
    "buffer_load_dwordx4 a[236:239], v34, s[36:39], s63 offen offset:256 sc1\n"
    "v_mfma_f32_32x32x16_bf16 v[112:127], v[212:215], a[80:83], v[112:127]\n"
    "ds_read_b64_tr_b16 v[198:199], v27 offset:26112\n"
    "ds_read_b64_tr_b16 v[200:201], v26 offset:29696\n"
    "ds_read_b64_tr_b16 v[202:203], v26 offset:30208\n"
    "buffer_load_dwordx4 a[240:243], v36, s[36:39], s63 offen offset:512 sc1\n"
    "s_waitcnt lgkmcnt(8)\n"
    "v_mfma_f32_32x32x16_bf16 v[96:111], v[216:219], a[84:87], v[96:111]\n"
    "ds_read_b64_tr_b16 v[204:205], v27 offset:29696\n"
    "ds_read_b64_tr_b16 v[206:207], v27 offset:30208\n"
    "s_waitcnt lgkmcnt(8)\n"
    "v_mfma_f32_32x32x16_bf16 v[112:127], v[220:223], a[84:87], v[112:127]\n"
    "s_waitcnt lgkmcnt(4)\n"
    "s_barrier\n"
    "buffer_load_dwordx4 a[244:247], v35, s[36:39], s63 offen offset:0 sc1\n"
    "v_mfma_f32_32x32x16_bf16 v[96:111], v[192:195], a[88:91], v[96:111]\n"
    "ds_read_b64_tr_b16 v[208:209], v30 offset:8192\n"
    "ds_read_b64_tr_b16 v[210:211], v30 offset:8704\n"
    "ds_read_b64_tr_b16 v[212:213], v31 offset:8192\n"
    "v_mfma_f32_32x32x16_bf16 v[112:127], v[196:199], a[88:91], v[112:127]\n"
    "ds_read_b64_tr_b16 v[214:215], v31 offset:8704\n"
    "ds_read_b64_tr_b16 v[216:217], v30 offset:12288\n"
    "ds_read_b64_tr_b16 v[218:219], v30 offset:12800\n"
    "buffer_load_dwordx4 a[248:251], v35, s[36:39], s63 offen offset:256 sc1\n"
    "s_waitcnt lgkmcnt(8)\n"
    "v_mfma_f32_32x32x16_bf16 v[96:111], v[200:203], a[92:95], v[96:111]\n"
    "ds_read_b64_tr_b16 v[220:221], v31 offset:12288\n"
    "ds_read_b64_tr_b16 v[222:223], v31 offset:12800\n"
    "s_waitcnt lgkmcnt(8)\n"
    "v_mfma_f32_32x32x16_bf16 v[112:127], v[204:207], a[92:95], v[112:127]\n"
    "s_waitcnt lgkmcnt(4)\n"
    "buffer_load_dwordx4 a[252:255], v37, s[36:39], s63 offen offset:512 sc1\n"
    "s_sub_u32 s63, s63, 0x40400\n"
    "s_cselect_b32 s63, 0x242400, s63\n"
    "v_mfma_f32_32x32x16_bf16 v[96:111], v[208:211], a[96:99], v[96:111]\n"
    "ds_read_b64_tr_b16 v[192:193], v30 offset:9216\n"
    "ds_read_b64_tr_b16 v[194:195], v30 offset:9728\n"
    "ds_read_b64_tr_b16 v[196:197], v31 offset:9216\n"
    "v_mfma_f32_32x32x16_bf16 v[112:127], v[212:215], a[96:99], v[112:127]\n"
    "ds_read_b64_tr_b16 v[198:199], v31 offset:9728\n"
    "ds_read_b64_tr_b16 v[200:201], v30 offset:13312\n"
    "ds_read_b64_tr_b16 v[202:203], v30 offset:13824\n"
    "buffer_load_dwordx4 a[0:3], v34, s[36:39], s63 offen offset:0 sc1\n"
    "s_waitcnt lgkmcnt(8)\n"
    "v_mfma_f32_32x32x16_bf16 v[96:111], v[216:219], a[100:103], v[96:111]\n"
    "ds_read_b64_tr_b16 v[204:205], v31 offset:13312\n"
    "ds_read_b64_tr_b16 v[206:207], v31 offset:13824\n"
    "buffer_load_dwordx4 a[4:7], v34, s[36:39], s63 offen offset:256 sc1\n"
    "s_waitcnt lgkmcnt(8)\n"
    "v_mfma_f32_32x32x16_bf16 v[112:127], v[220:223], a[100:103], v[112:127]\n"
    "s_waitcnt lgkmcnt(4)\n"
    "v_mfma_f32_32x32x16_bf16 v[96:111], v[192:195], a[104:107], v[96:111]\n"
    "ds_read_b64_tr_b16 v[208:209], v30 offset:16384\n"
    "ds_read_b64_tr_b16 v[210:211], v30 offset:16896\n"
    "ds_read_b64_tr_b16 v[212:213], v31 offset:16384\n"
    "buffer_load_dwordx4 a[8:11], v36, s[36:39], s63 offen offset:512 sc1\n"
    "v_mfma_f32_32x32x16_bf16 v[112:127], v[196:199], a[104:107], v[112:127]\n"
    "ds_read_b64_tr_b16 v[214:215], v31 offset:16896\n"
    "ds_read_b64_tr_b16 v[216:217], v30 offset:20480\n"
    "ds_read_b64_tr_b16 v[218:219], v30 offset:20992\n"
    "s_waitcnt lgkmcnt(8)\n"
    "v_mfma_f32_32x32x16_bf16 v[96:111], v[200:203], a[108:111], v[96:111]\n"
    "ds_read_b64_tr_b16 v[220:221], v31 offset:20480\n"
    "ds_read_b64_tr_b16 v[222:223], v31 offset:20992\n"
    "buffer_load_dwordx4 a[12:15], v35, s[36:39], s63 offen offset:0 sc1\n"
    "s_waitcnt lgkmcnt(8)\n"
    "v_mfma_f32_32x32x16_bf16 v[112:127], v[204:207], a[108:111], v[112:127]\n"
    "s_waitcnt lgkmcnt(4)\n"
    "v_mfma_f32_32x32x16_bf16 v[96:111], v[208:211], a[112:115], v[96:111]\n"
    "ds_read_b64_tr_b16 v[192:193], v30 offset:17408\n"
    "ds_read_b64_tr_b16 v[194:195], v30 offset:17920\n"
    "ds_read_b64_tr_b16 v[196:197], v31 offset:17408\n"
    "buffer_load_dwordx4 a[16:19], v35, s[36:39], s63 offen offset:256 sc1\n"
    "v_mfma_f32_32x32x16_bf16 v[112:127], v[212:215], a[112:115], v[112:127]\n"
    "ds_read_b64_tr_b16 v[198:199], v31 offset:17920\n"
    "ds_read_b64_tr_b16 v[200:201], v30 offset:21504\n"
    "ds_read_b64_tr_b16 v[202:203], v30 offset:22016\n"
    "s_waitcnt lgkmcnt(8)\n"
    "v_mfma_f32_32x32x16_bf16 v[96:111], v[216:219], a[116:119], v[96:111]\n"
    "ds_read_b64_tr_b16 v[204:205], v31 offset:21504\n"
    "ds_read_b64_tr_b16 v[206:207], v31 offset:22016\n"
    "buffer_load_dwordx4 a[20:23], v37, s[36:39], s63 offen offset:512 sc1\n"
    "s_add_u32 m0, s56, 0xc000\n"
    "s_mul_i32 s61, s75, 0x28000\n"
    "s_waitcnt lgkmcnt(8)\n"
    "v_mfma_f32_32x32x16_bf16 v[112:127], v[220:223], a[116:119], v[112:127]\n"
    "s_waitcnt lgkmcnt(4)\n"
    "s_barrier\n"
    "buffer_load_dwordx4 v20, s[28:31], s61 offen offset:0 lds\n"
    "v_mfma_f32_32x32x16_bf16 v[96:111], v[192:195], a[120:123], v[96:111]\n"
    "ds_read_b64_tr_b16 v[208:209], v28 offset:16384\n"
    "ds_read_b64_tr_b16 v[210:211], v28 offset:16896\n"
    "ds_read_b64_tr_b16 v[212:213], v29 offset:16384\n"
    "v_mfma_f32_32x32x16_bf16 v[112:127], v[196:199], a[120:123], v[112:127]\n"
    "ds_read_b64_tr_b16 v[214:215], v29 offset:16896\n"
    "ds_read_b64_tr_b16 v[216:217], v28 offset:20480\n"
    "ds_read_b64_tr_b16 v[218:219], v28 offset:20992\n"
    "buffer_load_dwordx4 v21, s[28:31], s61 offen offset:1024 lds\n"
    "s_add_u32 m0, s56, 0xe000\n"
    "s_add_u32 s62, s61, 0x118000\n"
    "s_waitcnt lgkmcnt(8)\n"
    "v_mfma_f32_32x32x16_bf16 v[96:111], v[200:203], a[124:127], v[96:111]\n"
    "ds_read_b64_tr_b16 v[220:221], v29 offset:20480\n"
    "ds_read_b64_tr_b16 v[222:223], v29 offset:20992\n"
    "s_cmp_ge_u32 s62, 0x230000\n"
    "s_cselect_b32 s0, 0x230000, 0\n"
    "s_sub_u32 s62, s62, s0\n"
    "s_waitcnt lgkmcnt(8)\n"
    "v_mfma_f32_32x32x16_bf16 v[112:127], v[204:207], a[124:127], v[112:127]\n"
    "s_waitcnt lgkmcnt(4)\n"
    "buffer_load_dwordx4 v20, s[28:31], s62 offen offset:0 lds\n"
    "v_mfma_f32_32x32x16_bf16 v[96:111], v[208:211], a[128:131], v[96:111]\n"
    "ds_read_b64_tr_b16 v[192:193], v28 offset:17408\n"
    "ds_read_b64_tr_b16 v[194:195], v28 offset:17920\n"
    "ds_read_b64_tr_b16 v[196:197], v29 offset:17408\n"
    "v_mfma_f32_32x32x16_bf16 v[112:127], v[212:215], a[128:131], v[112:127]\n"
    "ds_read_b64_tr_b16 v[198:199], v29 offset:17920\n"
    "ds_read_b64_tr_b16 v[200:201], v28 offset:21504\n"
    "ds_read_b64_tr_b16 v[202:203], v28 offset:22016\n"
    "buffer_load_dwordx4 v21, s[28:31], s62 offen offset:1024 lds\n"
    "s_add_u32 m0, s56, 0x4000\n"
    "s_waitcnt lgkmcnt(8)\n"
    "v_mfma_f32_32x32x16_bf16 v[96:111], v[216:219], a[132:135], v[96:111]\n"
    "ds_read_b64_tr_b16 v[204:205], v29 offset:21504\n"
    "ds_read_b64_tr_b16 v[206:207], v29 offset:22016\n"
    "s_sub_u32 s61, s61, 0x28000\n"
    "s_cselect_b32 s61, 0x208000, s61\n"
    "buffer_load_dwordx4 v20, s[28:31], s61 offen offset:0 lds\n"
    "s_waitcnt lgkmcnt(8)\n"
    "v_mfma_f32_32x32x16_bf16 v[112:127], v[220:223], a[132:135], v[112:127]\n"
    "s_waitcnt lgkmcnt(4)\n"
    "v_mfma_f32_32x32x16_bf16 v[96:111], v[192:195], a[136:139], v[96:111]\n"
    "ds_read_b64_tr_b16 v[208:209], v28 offset:24576\n"
    "ds_read_b64_tr_b16 v[210:211], v28 offset:25088\n"
    "ds_read_b64_tr_b16 v[212:213], v29 offset:24576\n"
    "buffer_load_dwordx4 v21, s[28:31], s61 offen offset:1024 lds\n"
    "s_add_u32 m0, s56, 0x6000\n"
    "v_mfma_f32_32x32x16_bf16 v[112:127], v[196:199], a[136:139], v[112:127]\n"
    "ds_read_b64_tr_b16 v[214:215], v29 offset:25088\n"
    "ds_read_b64_tr_b16 v[216:217], v28 offset:28672\n"
    "ds_read_b64_tr_b16 v[218:219], v28 offset:29184\n"
    "s_sub_u32 s62, s62, 0x28000\n"
    "s_cselect_b32 s62, 0x208000, s62\n"
    "s_waitcnt lgkmcnt(8)\n"
    "v_mfma_f32_32x32x16_bf16 v[96:111], v[200:203], a[140:143], v[96:111]\n"
    "ds_read_b64_tr_b16 v[220:221], v29 offset:28672\n"
    "ds_read_b64_tr_b16 v[222:223], v29 offset:29184\n"
    "buffer_load_dwordx4 v20, s[28:31], s62 offen offset:0 lds\n"
    "s_waitcnt lgkmcnt(8)\n"
    "v_mfma_f32_32x32x16_bf16 v[112:127], v[204:207], a[140:143], v[112:127]\n"
    "s_waitcnt lgkmcnt(4)\n"
    "v_mfma_f32_32x32x16_bf16 v[96:111], v[208:211], a[144:147], v[96:111]\n"
    "ds_read_b64_tr_b16 v[192:193], v28 offset:25600\n"
    "ds_read_b64_tr_b16 v[194:195], v28 offset:26112\n"
    "ds_read_b64_tr_b16 v[196:197], v29 offset:25600\n"
    "buffer_load_dwordx4 v21, s[28:31], s62 offen offset:1024 lds\n"
    "v_mfma_f32_32x32x16_bf16 v[112:127], v[212:215], a[144:147], v[112:127]\n"
    "ds_read_b64_tr_b16 v[198:199], v29 offset:26112\n"
    "ds_read_b64_tr_b16 v[200:201], v28 offset:29696\n"
    "ds_read_b64_tr_b16 v[202:203], v28 offset:30208\n"
    "s_waitcnt lgkmcnt(8)\n"
    "v_mfma_f32_32x32x16_bf16 v[96:111], v[216:219], a[148:151], v[96:111]\n"
    "ds_read_b64_tr_b16 v[204:205], v29 offset:29696\n"
    "ds_read_b64_tr_b16 v[206:207], v29 offset:30208\n"
    "s_waitcnt lgkmcnt(8)\n"
    "v_mfma_f32_32x32x16_bf16 v[112:127], v[220:223], a[148:151], v[112:127]\n"
    "s_mul_i32 s92, s93, 0xe00\n"
    "s_add_u32 s92, s92, 0x29000\n"
    "buffer_load_dwordx2 v[6:7], v8, s[36:39], s92 offen offset:0 sc1\n"
    "s_waitcnt lgkmcnt(0)\n"
    "s_barrier\n"
    "s_add_u32 m0, s56, 0x14000\n"
    "s_sub_u32 s61, s61, 0x28000\n"
    "s_cselect_b32 s61, 0x208000, s61\n"
    "v_mfma_f32_32x32x16_bf16 v[96:111], v[192:195], a[152:155], v[96:111]\n"
    "buffer_load_dwordx4 v20, s[28:31], s61 offen offset:0 lds\n"
    "ds_read_b64_tr_b16 v[208:209], v30 offset:1024\n"
    "ds_read_b64_tr_b16 v[210:211], v30 offset:1536\n"
    "ds_read_b64_tr_b16 v[212:213], v31 offset:1024\n"
    "v_mfma_f32_32x32x16_bf16 v[112:127], v[196:199], a[152:155], v[112:127]\n"
    "ds_read_b64_tr_b16 v[214:215], v31 offset:1536\n"
    "buffer_load_dwordx4 v21, s[28:31], s61 offen offset:1024 lds\n"
    "s_add_u32 m0, s56, 0x16000\n"
    "s_sub_u32 s62, s62, 0x28000\n"
    "s_cselect_b32 s62, 0x208000, s62\n"
    "v_mfma_f32_32x32x16_bf16 v[96:111], v[200:203], a[156:159], v[96:111]\n"
    "v_mfma_f32_32x32x16_bf16 v[112:127], v[204:207], a[156:159], v[112:127]\n"
    "s_waitcnt lgkmcnt(0)\n"
    "buffer_load_dwordx4 v20, s[28:31], s62 offen offset:0 lds\n"
    "v_mfma_f32_32x32x16_bf16 v[96:111], v[208:211], v[184:187], v[96:111]\n"
    "v_mfma_f32_32x32x16_bf16 v[112:127], v[212:215], v[184:187], v[112:127]\n"
    "s_nop 10\n"
    "v_pk_mul_f32 v[224:225], v[96:97], v[96:97] op_sel:[0,0] op_sel_hi:[1,1]\n"
    "v_pk_mul_f32 v[226:227], v[98:99], v[98:99] op_sel:[0,0] op_sel_hi:[1,1]\n"
    "v_pk_mul_f32 v[228:229], v[100:101], v[100:101] op_sel:[0,0] op_sel_hi:[1,1]\n"
    "v_pk_mul_f32 v[230:231], v[102:103], v[102:103] op_sel:[0,0] op_sel_hi:[1,1]\n"
    "buffer_load_dwordx4 v21, s[28:31], s62 offen offset:1024 lds\n"
    "v_pk_mul_f32 v[232:233], v[104:105], v[104:105] op_sel:[0,0] op_sel_hi:[1,1]\n"
    "v_pk_mul_f32 v[234:235], v[106:107], v[106:107] op_sel:[0,0] op_sel_hi:[1,1]\n"
    "v_pk_mul_f32 v[236:237], v[108:109], v[108:109] op_sel:[0,0] op_sel_hi:[1,1]\n"
    "v_pk_mul_f32 v[238:239], v[110:111], v[110:111] op_sel:[0,0] op_sel_hi:[1,1]\n"
    "v_pk_fma_f32 v[224:225], v[224:225], v[180:181], v[180:181] op_sel:[0,1,0] op_sel_hi:[1,1,0]\n"
    "v_pk_fma_f32 v[226:227], v[226:227], v[180:181], v[180:181] op_sel:[0,1,0] op_sel_hi:[1,1,0]\n"
    "v_pk_fma_f32 v[228:229], v[228:229], v[180:181], v[180:181] op_sel:[0,1,0] op_sel_hi:[1,1,0]\n"
    "v_pk_fma_f32 v[230:231], v[230:231], v[180:181], v[180:181] op_sel:[0,1,0] op_sel_hi:[1,1,0]\n"
    "v_pk_fma_f32 v[232:233], v[232:233], v[180:181], v[180:181] op_sel:[0,1,0] op_sel_hi:[1,1,0]\n"
    "v_pk_fma_f32 v[234:235], v[234:235], v[180:181], v[180:181] op_sel:[0,1,0] op_sel_hi:[1,1,0]\n"
    "v_pk_fma_f32 v[236:237], v[236:237], v[180:181], v[180:181] op_sel:[0,1,0] op_sel_hi:[1,1,0]\n"
    "v_pk_fma_f32 v[238:239], v[238:239], v[180:181], v[180:181] op_sel:[0,1,0] op_sel_hi:[1,1,0]\n"
    "v_pk_mul_f32 v[224:225], v[224:225], v[96:97] op_sel:[0,0] op_sel_hi:[1,1]\n"
    "v_pk_mul_f32 v[226:227], v[226:227], v[98:99] op_sel:[0,0] op_sel_hi:[1,1]\n"
    "v_pk_mul_f32 v[228:229], v[228:229], v[100:101] op_sel:[0,0] op_sel_hi:[1,1]\n"
    "v_pk_mul_f32 v[230:231], v[230:231], v[102:103] op_sel:[0,0] op_sel_hi:[1,1]\n"
    "v_pk_mul_f32 v[232:233], v[232:233], v[104:105] op_sel:[0,0] op_sel_hi:[1,1]\n"
    "v_pk_mul_f32 v[234:235], v[234:235], v[106:107] op_sel:[0,0] op_sel_hi:[1,1]\n"
    "v_pk_mul_f32 v[236:237], v[236:237], v[108:109] op_sel:[0,0] op_sel_hi:[1,1]\n"
    "v_pk_mul_f32 v[238:239], v[238:239], v[110:111] op_sel:[0,0] op_sel_hi:[1,1]\n"
    "v_exp_f32 v224, v224\n"
    "v_exp_f32 v225, v225\n"
    "v_exp_f32 v226, v226\n"
    "v_exp_f32 v227, v227\n"
    "v_exp_f32 v228, v228\n"
    "v_exp_f32 v229, v229\n"
    "s_waitcnt vmcnt(4)\n"
    "v_cmp_eq_u64 vcc, v[6:7], v[4:5]\n"
    "s_or_b32 vcc_lo, vcc_lo, s81\n"
    "s_cmp_eq_u32 vcc_lo, -1\n"
    "s_cbranch_scc0 .Lspin_GA\n"
    ".Ldone_GA:\n"
    "s_barrier\n"
    "s_mul_i32 s63, s54, 7\n"
    "s_add_u32 s63, s63, s75\n"
    "s_cmp_ge_u32 s63, 14\n"
    "s_cselect_b32 s1, 14, 0\n"
    "s_sub_u32 s63, s63, s1\n"
    "s_mul_i32 s63, s63, 0x80800\n"
    "v_add_u32 v34, 0x282c00, v34\n"
    "v_add_u32 v35, 0x282c00, v35\n"
    "v_add_u32 v36, 0x282c00, v36\n"
    "v_add_u32 v37, 0x282c00, v37\n"
    "buffer_load_dwordx4 a[24:27], v34, s[36:39], s63 offen offset:0 sc1\n"
    "buffer_load_dwordx4 a[28:31], v34, s[36:39], s63 offen offset:256 sc1\n"
    "v_exp_f32 v230, v230\n"
    "v_exp_f32 v231, v231\n"
    "v_exp_f32 v232, v232\n"
    "v_exp_f32 v233, v233\n"
    "v_exp_f32 v234, v234\n"
    "v_exp_f32 v235, v235\n"
    "v_exp_f32 v236, v236\n"
    "v_exp_f32 v237, v237\n"
    "v_exp_f32 v238, v238\n"
    "v_exp_f32 v239, v239\n"
    "v_pk_fma_f32 v[224:225], v[224:225], v[182:183], v[182:183] op_sel:[0,0,0] op_sel_hi:[1,0,0]\n"
    "v_pk_fma_f32 v[226:227], v[226:227], v[182:183], v[182:183] op_sel:[0,0,0] op_sel_hi:[1,0,0]\n"
    "v_pk_fma_f32 v[228:229], v[228:229], v[182:183], v[182:183] op_sel:[0,0,0] op_sel_hi:[1,0,0]\n"
    "v_pk_fma_f32 v[230:231], v[230:231], v[182:183], v[182:183] op_sel:[0,0,0] op_sel_hi:[1,0,0]\n"
    "v_pk_fma_f32 v[232:233], v[232:233], v[182:183], v[182:183] op_sel:[0,0,0] op_sel_hi:[1,0,0]\n"
    "v_pk_fma_f32 v[234:235], v[234:235], v[182:183], v[182:183] op_sel:[0,0,0] op_sel_hi:[1,0,0]\n"
    "buffer_load_dwordx4 a[32:35], v36, s[36:39], s63 offen offset:512 sc1\n"
    "buffer_load_dwordx4 a[36:39], v35, s[36:39], s63 offen offset:0 sc1\n"
    "v_pk_fma_f32 v[236:237], v[236:237], v[182:183], v[182:183] op_sel:[0,0,0] op_sel_hi:[1,0,0]\n"
    "v_pk_fma_f32 v[238:239], v[238:239], v[182:183], v[182:183] op_sel:[0,0,0] op_sel_hi:[1,0,0]\n"
    "v_rcp_f32 v224, v224\n"
    "v_rcp_f32 v225, v225\n"
    "v_rcp_f32 v226, v226\n"
    "v_rcp_f32 v227, v227\n"
    "v_rcp_f32 v228, v228\n"
    "v_rcp_f32 v229, v229\n"
    "v_rcp_f32 v230, v230\n"
    "v_rcp_f32 v231, v231\n"
    "v_rcp_f32 v232, v232\n"
    "v_rcp_f32 v233, v233\n"
    "v_rcp_f32 v234, v234\n"
    "v_rcp_f32 v235, v235\n"
    "buffer_load_dwordx4 a[40:43], v35, s[36:39], s63 offen offset:256 sc1\n"
    "buffer_load_dwordx4 a[44:47], v37, s[36:39], s63 offen offset:512 sc1\n"
    "v_rcp_f32 v236, v236\n"
    "v_rcp_f32 v237, v237\n"
    "v_rcp_f32 v238, v238\n"
    "v_rcp_f32 v239, v239\n"
    "v_pk_mul_f32 v[96:97], v[96:97], v[224:225] op_sel:[0,0] op_sel_hi:[1,1]\n"
    "v_pk_mul_f32 v[98:99], v[98:99], v[226:227] op_sel:[0,0] op_sel_hi:[1,1]\n"
    "v_pk_mul_f32 v[100:101], v[100:101], v[228:229] op_sel:[0,0] op_sel_hi:[1,1]\n"
    "v_pk_mul_f32 v[102:103], v[102:103], v[230:231] op_sel:[0,0] op_sel_hi:[1,1]\n"
    "v_pk_mul_f32 v[104:105], v[104:105], v[232:233] op_sel:[0,0] op_sel_hi:[1,1]\n"
    "v_pk_mul_f32 v[106:107], v[106:107], v[234:235] op_sel:[0,0] op_sel_hi:[1,1]\n"
    "v_pk_mul_f32 v[108:109], v[108:109], v[236:237] op_sel:[0,0] op_sel_hi:[1,1]\n"
    "v_pk_mul_f32 v[110:111], v[110:111], v[238:239] op_sel:[0,0] op_sel_hi:[1,1]\n"
    "v_cvt_pk_bf16_f32 v240, v96, v97\n"
    "v_cvt_pk_bf16_f32 v241, v98, v99\n"
    "v_cvt_pk_bf16_f32 v242, v100, v101\n"
    "v_cvt_pk_bf16_f32 v243, v102, v103\n"
    "v_cvt_pk_bf16_f32 v244, v104, v105\n"
    "v_cvt_pk_bf16_f32 v245, v106, v107\n"
    "v_cvt_pk_bf16_f32 v246, v108, v109\n"
    "v_cvt_pk_bf16_f32 v247, v110, v111\n"
    "v_permlane32_swap_b32 v240, v242\n"
    "s_sub_u32 s63, s63, 0x80800\n"
    "s_cselect_b32 s63, 0x686800, s63\n"
    "buffer_load_dwordx4 a[48:51], v34, s[36:39], s63 offen offset:0 sc1\n"
    "buffer_load_dwordx4 a[52:55], v34, s[36:39], s63 offen offset:256 sc1\n"
    "v_permlane32_swap_b32 v241, v243\n"
    "buffer_store_dwordx4 v[240:243], v43, s[36:39], 0 offen offset:0 sc1\n"
    "v_permlane32_swap_b32 v244, v246\n"
    "v_permlane32_swap_b32 v245, v247\n"
    "buffer_store_dwordx4 v[244:247], v43, s[36:39], s69 offen offset:0 sc1\n"
    "v_pk_mul_f32 v[224:225], v[112:113], v[112:113] op_sel:[0,0] op_sel_hi:[1,1]\n"
    "v_pk_mul_f32 v[226:227], v[114:115], v[114:115] op_sel:[0,0] op_sel_hi:[1,1]\n"
    "v_pk_mul_f32 v[228:229], v[116:117], v[116:117] op_sel:[0,0] op_sel_hi:[1,1]\n"
    "v_pk_mul_f32 v[230:231], v[118:119], v[118:119] op_sel:[0,0] op_sel_hi:[1,1]\n"
    "v_pk_mul_f32 v[232:233], v[120:121], v[120:121] op_sel:[0,0] op_sel_hi:[1,1]\n"
    "v_pk_mul_f32 v[234:235], v[122:123], v[122:123] op_sel:[0,0] op_sel_hi:[1,1]\n"
    "v_pk_mul_f32 v[236:237], v[124:125], v[124:125] op_sel:[0,0] op_sel_hi:[1,1]\n"
    "v_pk_mul_f32 v[238:239], v[126:127], v[126:127] op_sel:[0,0] op_sel_hi:[1,1]\n"
    "v_pk_fma_f32 v[224:225], v[224:225], v[180:181], v[180:181] op_sel:[0,1,0] op_sel_hi:[1,1,0]\n"
    "v_pk_fma_f32 v[226:227], v[226:227], v[180:181], v[180:181] op_sel:[0,1,0] op_sel_hi:[1,1,0]\n"
    "v_pk_fma_f32 v[228:229], v[228:229], v[180:181], v[180:181] op_sel:[0,1,0] op_sel_hi:[1,1,0]\n"
    "v_pk_fma_f32 v[230:231], v[230:231], v[180:181], v[180:181] op_sel:[0,1,0] op_sel_hi:[1,1,0]\n"
    "v_pk_fma_f32 v[232:233], v[232:233], v[180:181], v[180:181] op_sel:[0,1,0] op_sel_hi:[1,1,0]\n"
    "v_pk_fma_f32 v[234:235], v[234:235], v[180:181], v[180:181] op_sel:[0,1,0] op_sel_hi:[1,1,0]\n"
    "v_pk_fma_f32 v[236:237], v[236:237], v[180:181], v[180:181] op_sel:[0,1,0] op_sel_hi:[1,1,0]\n"
    "v_pk_fma_f32 v[238:239], v[238:239], v[180:181], v[180:181] op_sel:[0,1,0] op_sel_hi:[1,1,0]\n"
    "v_pk_mul_f32 v[224:225], v[224:225], v[112:113] op_sel:[0,0] op_sel_hi:[1,1]\n"
    "v_pk_mul_f32 v[226:227], v[226:227], v[114:115] op_sel:[0,0] op_sel_hi:[1,1]\n"
    "v_pk_mul_f32 v[228:229], v[228:229], v[116:117] op_sel:[0,0] op_sel_hi:[1,1]\n"
    "v_pk_mul_f32 v[230:231], v[230:231], v[118:119] op_sel:[0,0] op_sel_hi:[1,1]\n"
    "v_pk_mul_f32 v[232:233], v[232:233], v[120:121] op_sel:[0,0] op_sel_hi:[1,1]\n"
    "v_pk_mul_f32 v[234:235], v[234:235], v[122:123] op_sel:[0,0] op_sel_hi:[1,1]\n"
    "v_pk_mul_f32 v[236:237], v[236:237], v[124:125] op_sel:[0,0] op_sel_hi:[1,1]\n"
    "v_pk_mul_f32 v[238:239], v[238:239], v[126:127] op_sel:[0,0] op_sel_hi:[1,1]\n"
    "v_exp_f32 v224, v224\n"
    "v_exp_f32 v225, v225\n"
    "v_exp_f32 v226, v226\n"
    "v_exp_f32 v227, v227\n"
    "v_exp_f32 v228, v228\n"
    "v_exp_f32 v229, v229\n"
    "v_exp_f32 v230, v230\n"
    "v_exp_f32 v231, v231\n"
    "v_exp_f32 v232, v232\n"
    "v_exp_f32 v233, v233\n"
    "v_exp_f32 v234, v234\n"
    "buffer_load_dwordx4 a[56:59], v36, s[36:39], s63 offen offset:512 sc1\n"
    "buffer_load_dwordx4 a[60:63], v35, s[36:39], s63 offen offset:0 sc1\n"
    "v_exp_f32 v235, v235\n"
    "v_exp_f32 v236, v236\n"
    "v_exp_f32 v237, v237\n"
    "v_exp_f32 v238, v238\n"
    "v_exp_f32 v239, v239\n"
    "v_pk_fma_f32 v[224:225], v[224:225], v[182:183], v[182:183] op_sel:[0,0,0] op_sel_hi:[1,0,0]\n"
    "v_pk_fma_f32 v[226:227], v[226:227], v[182:183], v[182:183] op_sel:[0,0,0] op_sel_hi:[1,0,0]\n"
    "v_pk_fma_f32 v[228:229], v[228:229], v[182:183], v[182:183] op_sel:[0,0,0] op_sel_hi:[1,0,0]\n"
    "v_pk_fma_f32 v[230:231], v[230:231], v[182:183], v[182:183] op_sel:[0,0,0] op_sel_hi:[1,0,0]\n"
    "v_pk_fma_f32 v[232:233], v[232:233], v[182:183], v[182:183] op_sel:[0,0,0] op_sel_hi:[1,0,0]\n"
    "v_pk_fma_f32 v[234:235], v[234:235], v[182:183], v[182:183] op_sel:[0,0,0] op_sel_hi:[1,0,0]\n"
    "v_pk_fma_f32 v[236:237], v[236:237], v[182:183], v[182:183] op_sel:[0,0,0] op_sel_hi:[1,0,0]\n"
    "v_pk_fma_f32 v[238:239], v[238:239], v[182:183], v[182:183] op_sel:[0,0,0] op_sel_hi:[1,0,0]\n"
    "v_rcp_f32 v224, v224\n"
    "v_rcp_f32 v225, v225\n"
    "v_rcp_f32 v226, v226\n"
    "v_rcp_f32 v227, v227\n"
    "buffer_load_dwordx4 a[64:67], v35, s[36:39], s63 offen offset:256 sc1\n"
    "buffer_load_dwordx4 a[68:71], v37, s[36:39], s63 offen offset:512 sc1\n"
    "v_rcp_f32 v228, v228\n"
    "v_rcp_f32 v229, v229\n"
    "v_rcp_f32 v230, v230\n"
    "v_rcp_f32 v231, v231\n"
    "v_rcp_f32 v232, v232\n"
    "v_rcp_f32 v233, v233\n"
    "v_rcp_f32 v234, v234\n"
    "v_rcp_f32 v235, v235\n"
    "v_rcp_f32 v236, v236\n"
    "v_rcp_f32 v237, v237\n"
    "v_rcp_f32 v238, v238\n"
    "v_rcp_f32 v239, v239\n"
    "v_pk_mul_f32 v[112:113], v[112:113], v[224:225] op_sel:[0,0] op_sel_hi:[1,1]\n"
    "v_pk_mul_f32 v[114:115], v[114:115], v[226:227] op_sel:[0,0] op_sel_hi:[1,1]\n"
    "s_sub_u32 s63, s63, 0x80800\n"
    "s_cselect_b32 s63, 0x686800, s63\n"
    "buffer_load_dwordx4 a[72:75], v34, s[36:39], s63 offen offset:0 sc1\n"
    "buffer_load_dwordx4 a[76:79], v34, s[36:39], s63 offen offset:256 sc1\n"
    "v_pk_mul_f32 v[116:117], v[116:117], v[228:229] op_sel:[0,0] op_sel_hi:[1,1]\n"
    "v_pk_mul_f32 v[118:119], v[118:119], v[230:231] op_sel:[0,0] op_sel_hi:[1,1]\n"
    "v_pk_mul_f32 v[120:121], v[120:121], v[232:233] op_sel:[0,0] op_sel_hi:[1,1]\n"
    "v_pk_mul_f32 v[122:123], v[122:123], v[234:235] op_sel:[0,0] op_sel_hi:[1,1]\n"
    "v_pk_mul_f32 v[124:125], v[124:125], v[236:237] op_sel:[0,0] op_sel_hi:[1,1]\n"
    "v_pk_mul_f32 v[126:127], v[126:127], v[238:239] op_sel:[0,0] op_sel_hi:[1,1]\n"
    "v_cvt_pk_bf16_f32 v248, v112, v113\n"
    "v_cvt_pk_bf16_f32 v249, v114, v115\n"
    "v_cvt_pk_bf16_f32 v250, v116, v117\n"
    "v_cvt_pk_bf16_f32 v251, v118, v119\n"
    "v_cvt_pk_bf16_f32 v252, v120, v121\n"
    "v_cvt_pk_bf16_f32 v253, v122, v123\n"
    "v_cvt_pk_bf16_f32 v254, v124, v125\n"
    "v_cvt_pk_bf16_f32 v255, v126, v127\n"
    "v_permlane32_swap_b32 v248, v250\n"
    "v_permlane32_swap_b32 v249, v251\n"
    "buffer_store_dwordx4 v[248:251], v43, s[36:39], s70 offen offset:0 sc1\n"
    "v_permlane32_swap_b32 v252, v254\n"
    "v_permlane32_swap_b32 v253, v255\n"
    "buffer_store_dwordx4 v[252:255], v43, s[36:39], s71 offen offset:0 sc1\n"
    "s_lshl_b32 s64, s54, 1\n"
    "s_lshl_b32 s65, s54, 13\n"
    "v_bfe_u32 v58, v1, 3, 1\n"
    "v_lshlrev_b32 v58, 1, v58\n"
    "v_and_b32 v59, 1, v2\n"
    "v_or_b32 v59, v58, v59\n"
    "v_xor_b32 v59, s64, v59\n"
    "v_lshlrev_b32 v59, 5, v59\n"
    "v_bfe_u32 v58, v1, 2, 2\n"
    "v_lshlrev_b32 v58, 7, v58\n"
    "v_lshl_add_u32 v58, v2, 11, v58\n"
    "v_or_b32 v59, v58, v59\n"
    "v_and_b32 v58, 3, v1\n"
    "v_lshl_add_u32 v26, v58, 3, v59\n"
    "v_add_u32 v26, s65, v26\n"
    "v_xor_b32 v27, 32, v26\n"
    "v_xor_b32 v28, 64, v26\n"
    "v_xor_b32 v29, 0x60, v26\n"
    "v_add_u32 v30, 0x10000, v26\n"
    "v_add_u32 v31, 0x10000, v27\n"
    "v_add_u32 v32, 0x10000, v28\n"
    "v_add_u32 v33, 0x10000, v29\n"
    "s_barrier\n"
    "ds_read_b64_tr_b16 v[192:193], v30 offset:32768\n"
    "ds_read_b64_tr_b16 v[194:195], v30 offset:33280\n"
    "buffer_load_dwordx4 a[80:83], v36, s[36:39], s63 offen offset:512 sc1\n"
    "buffer_load_dwordx4 a[84:87], v35, s[36:39], s63 offen offset:0 sc1\n"
    "ds_read_b64_tr_b16 v[196:197], v31 offset:32768\n"
    "ds_read_b64_tr_b16 v[198:199], v31 offset:33280\n"
    "ds_read_b64_tr_b16 v[200:201], v32 offset:32768\n"
    "ds_read_b64_tr_b16 v[202:203], v32 offset:33280\n"
    "ds_read_b64_tr_b16 v[204:205], v33 offset:32768\n"
    "ds_read_b64_tr_b16 v[206:207], v33 offset:33280\n"
    "s_waitcnt lgkmcnt(4)\n"
    "v_mfma_f32_16x16x32_bf16 v[164:167], v[192:195], a[160:163], 0\n"
    "ds_read_b64_tr_b16 v[208:209], v30 offset:33792\n"
    "v_mfma_f32_16x16x32_bf16 v[168:171], v[192:195], a[164:167], 0\n"
    "ds_read_b64_tr_b16 v[210:211], v30 offset:34304\n"
    "v_mfma_f32_16x16x32_bf16 v[172:175], v[192:195], a[168:171], 0\n"
    "ds_read_b64_tr_b16 v[212:213], v31 offset:33792\n"
    "buffer_load_dwordx4 a[88:91], v35, s[36:39], s63 offen offset:256 sc1\n"
    "v_mfma_f32_16x16x32_bf16 v[176:179], v[196:199], a[160:163], 0\n"
    "ds_read_b64_tr_b16 v[214:215], v31 offset:34304\n"
    "v_mfma_f32_16x16x32_bf16 v[180:183], v[196:199], a[164:167], 0\n"
    "v_mfma_f32_16x16x32_bf16 v[184:187], v[196:199], a[168:171], 0\n"
    "s_waitcnt lgkmcnt(4)\n"
    "v_mfma_f32_16x16x32_bf16 v[64:67], v[200:203], a[160:163], 0\n"
    "ds_read_b64_tr_b16 v[216:217], v32 offset:33792\n"
    "buffer_load_dwordx4 a[92:95], v37, s[36:39], s63 offen offset:512 sc1\n"
    "v_mfma_f32_16x16x32_bf16 v[68:71], v[200:203], a[164:167], 0\n"
    "ds_read_b64_tr_b16 v[218:219], v32 offset:34304\n"
    "v_mfma_f32_16x16x32_bf16 v[72:75], v[200:203], a[168:171], 0\n"
    "ds_read_b64_tr_b16 v[220:221], v33 offset:33792\n"
    "v_mfma_f32_16x16x32_bf16 v[76:79], v[204:207], a[160:163], 0\n"
    "ds_read_b64_tr_b16 v[222:223], v33 offset:34304\n"
    "v_mfma_f32_16x16x32_bf16 v[80:83], v[204:207], a[164:167], 0\n"
    "s_sub_u32 s63, s63, 0x80800\n"
    "s_cselect_b32 s63, 0x686800, s63\n"
    "v_mfma_f32_16x16x32_bf16 v[84:87], v[204:207], a[168:171], 0\n"
    "s_waitcnt lgkmcnt(0)\n"
    "s_barrier\n"
    "s_add_u32 m0, s56, 0x18000\n"
    "s_sub_u32 s61, s61, 0x28000\n"
    "s_cselect_b32 s61, 0x208000, s61\n"
    "buffer_load_dwordx4 v20, s[28:31], s61 offen offset:0 lds\n"
    "v_mfma_f32_16x16x32_bf16 v[164:167], v[208:211], a[172:175], v[164:167]\n"
    "ds_read_b64_tr_b16 v[192:193], v30 offset:0\n"
    "buffer_load_dwordx4 v21, s[28:31], s61 offen offset:1024 lds\n"
    "v_mfma_f32_16x16x32_bf16 v[168:171], v[208:211], a[176:179], v[168:171]\n"
    "ds_read_b64_tr_b16 v[194:195], v30 offset:512\n"
    "s_add_u32 m0, s56, 0x1a000\n"
    "v_mfma_f32_16x16x32_bf16 v[172:175], v[208:211], a[180:183], v[172:175]\n"
    "ds_read_b64_tr_b16 v[196:197], v31 offset:0\n"
    "v_mfma_f32_16x16x32_bf16 v[176:179], v[212:215], a[172:175], v[176:179]\n"
    "ds_read_b64_tr_b16 v[198:199], v31 offset:512\n"
    "v_mfma_f32_16x16x32_bf16 v[180:183], v[212:215], a[176:179], v[180:183]\n"
    "s_sub_u32 s62, s62, 0x28000\n"
    "s_cselect_b32 s62, 0x208000, s62\n"
    "v_mfma_f32_16x16x32_bf16 v[184:187], v[212:215], a[180:183], v[184:187]\n"
    "buffer_load_dwordx4 v20, s[28:31], s62 offen offset:0 lds\n"
    "v_mfma_f32_16x16x32_bf16 v[64:67], v[216:219], a[172:175], v[64:67]\n"
    "ds_read_b64_tr_b16 v[200:201], v32 offset:0\n"
    "v_mfma_f32_16x16x32_bf16 v[68:71], v[216:219], a[176:179], v[68:71]\n"
    "ds_read_b64_tr_b16 v[202:203], v32 offset:512\n"
    "buffer_load_dwordx4 v21, s[28:31], s62 offen offset:1024 lds\n"
    "v_mfma_f32_16x16x32_bf16 v[72:75], v[216:219], a[180:183], v[72:75]\n"
    "ds_read_b64_tr_b16 v[204:205], v33 offset:0\n"
    "v_mfma_f32_16x16x32_bf16 v[76:79], v[220:223], a[172:175], v[76:79]\n"
    "ds_read_b64_tr_b16 v[206:207], v33 offset:512\n"
    "v_mfma_f32_16x16x32_bf16 v[80:83], v[220:223], a[176:179], v[80:83]\n"
    "v_mfma_f32_16x16x32_bf16 v[84:87], v[220:223], a[180:183], v[84:87]\n"
    "s_waitcnt lgkmcnt(4)\n"
    "buffer_load_dwordx4 a[96:99], v34, s[36:39], s63 offen offset:0 sc1\n"
    "v_mfma_f32_16x16x32_bf16 v[164:167], v[192:195], a[184:187], v[164:167]\n"
    "ds_read_b64_tr_b16 v[208:209], v30 offset:1024\n"
    "v_mfma_f32_16x16x32_bf16 v[168:171], v[192:195], a[188:191], v[168:171]\n"
    "ds_read_b64_tr_b16 v[210:211], v30 offset:1536\n"
    "v_mfma_f32_16x16x32_bf16 v[172:175], v[192:195], a[192:195], v[172:175]\n"
    "ds_read_b64_tr_b16 v[212:213], v31 offset:1024\n"
    "buffer_load_dwordx4 a[100:103], v34, s[36:39], s63 offen offset:256 sc1\n"
    "v_mfma_f32_16x16x32_bf16 v[176:179], v[196:199], a[184:187], v[176:179]\n"
    "ds_read_b64_tr_b16 v[214:215], v31 offset:1536\n"
    "v_mfma_f32_16x16x32_bf16 v[180:183], v[196:199], a[188:191], v[180:183]\n"
    "v_mfma_f32_16x16x32_bf16 v[184:187], v[196:199], a[192:195], v[184:187]\n"
    "s_waitcnt lgkmcnt(4)\n"
    "v_mfma_f32_16x16x32_bf16 v[64:67], v[200:203], a[184:187], v[64:67]\n"
    "ds_read_b64_tr_b16 v[216:217], v32 offset:1024\n"
    "buffer_load_dwordx4 a[104:107], v36, s[36:39], s63 offen offset:512 sc1\n"
    "v_mfma_f32_16x16x32_bf16 v[68:71], v[200:203], a[188:191], v[68:71]\n"
    "ds_read_b64_tr_b16 v[218:219], v32 offset:1536\n"
    "v_mfma_f32_16x16x32_bf16 v[72:75], v[200:203], a[192:195], v[72:75]\n"
    "ds_read_b64_tr_b16 v[220:221], v33 offset:1024\n"
    "v_mfma_f32_16x16x32_bf16 v[76:79], v[204:207], a[184:187], v[76:79]\n"
    "ds_read_b64_tr_b16 v[222:223], v33 offset:1536\n"
    "buffer_load_dwordx4 a[108:111], v35, s[36:39], s63 offen offset:0 sc1\n"
    "v_mfma_f32_16x16x32_bf16 v[80:83], v[204:207], a[188:191], v[80:83]\n"
    "v_mfma_f32_16x16x32_bf16 v[84:87], v[204:207], a[192:195], v[84:87]\n"
    "s_waitcnt vmcnt(12) lgkmcnt(0)\n"
    "s_barrier\n"
    "v_mfma_f32_16x16x32_bf16 v[164:167], v[208:211], a[196:199], v[164:167]\n"
    "buffer_store_dwordx2 v[4:5], v9, s[36:39], s89 offen offset:0 sc1\n"
    "buffer_load_dwordx4 a[112:115], v35, s[36:39], s63 offen offset:256 sc1\n"
    "v_mfma_f32_16x16x32_bf16 v[168:171], v[208:211], a[200:203], v[168:171]\n"
    "ds_read_b64_tr_b16 v[192:193], v26 offset:0\n"
    "v_mfma_f32_16x16x32_bf16 v[172:175], v[208:211], a[204:207], v[172:175]\n"
    "ds_read_b64_tr_b16 v[194:195], v26 offset:512\n"
    "v_mfma_f32_16x16x32_bf16 v[176:179], v[212:215], a[196:199], v[176:179]\n"
    "ds_read_b64_tr_b16 v[196:197], v27 offset:0\n"
    "v_mfma_f32_16x16x32_bf16 v[180:183], v[212:215], a[200:203], v[180:183]\n"
    "ds_read_b64_tr_b16 v[198:199], v27 offset:512\n"
    "buffer_load_dwordx4 a[116:119], v37, s[36:39], s63 offen offset:512 sc1\n"
    "v_mfma_f32_16x16x32_bf16 v[184:187], v[212:215], a[204:207], v[184:187]\n"
    "s_add_u32 m0, s56, 0x10000\n"
    "v_mfma_f32_16x16x32_bf16 v[64:67], v[216:219], a[196:199], v[64:67]\n"
    "ds_read_b64_tr_b16 v[200:201], v28 offset:0\n"
    "v_mfma_f32_16x16x32_bf16 v[68:71], v[216:219], a[200:203], v[68:71]\n"
    "ds_read_b64_tr_b16 v[202:203], v28 offset:512\n"
    "v_mfma_f32_16x16x32_bf16 v[72:75], v[216:219], a[204:207], v[72:75]\n"
    "ds_read_b64_tr_b16 v[204:205], v29 offset:0\n"
    "v_mfma_f32_16x16x32_bf16 v[76:79], v[220:223], a[196:199], v[76:79]\n"
    "s_sub_u32 s61, s61, 0x28000\n"
    "s_cselect_b32 s61, 0x208000, s61\n"
    "ds_read_b64_tr_b16 v[206:207], v29 offset:512\n"
    "v_mfma_f32_16x16x32_bf16 v[80:83], v[220:223], a[200:203], v[80:83]\n"
    "buffer_load_dwordx4 v20, s[28:31], s61 offen offset:0 lds\n"
    "v_mfma_f32_16x16x32_bf16 v[84:87], v[220:223], a[204:207], v[84:87]\n"
    "s_waitcnt lgkmcnt(4)\n"
    "buffer_load_dwordx4 v21, s[28:31], s61 offen offset:1024 lds\n"
    "s_add_u32 m0, s56, 0x12000\n"
    "v_mfma_f32_16x16x32_bf16 v[164:167], v[192:195], a[208:211], v[164:167]\n"
    "ds_read_b64_tr_b16 v[208:209], v26 offset:1024\n"
    "v_mfma_f32_16x16x32_bf16 v[168:171], v[192:195], a[212:215], v[168:171]\n"
    "ds_read_b64_tr_b16 v[210:211], v26 offset:1536\n"
    "v_mfma_f32_16x16x32_bf16 v[172:175], v[192:195], a[216:219], v[172:175]\n"
    "ds_read_b64_tr_b16 v[212:213], v27 offset:1024\n"
    "v_mfma_f32_16x16x32_bf16 v[176:179], v[196:199], a[208:211], v[176:179]\n"
    "ds_read_b64_tr_b16 v[214:215], v27 offset:1536\n"
    "v_mfma_f32_16x16x32_bf16 v[180:183], v[196:199], a[212:215], v[180:183]\n"
    "s_sub_u32 s62, s62, 0x28000\n"
    "s_cselect_b32 s62, 0x208000, s62\n"
    "v_mfma_f32_16x16x32_bf16 v[184:187], v[196:199], a[216:219], v[184:187]\n"
    "s_waitcnt lgkmcnt(4)\n"
    "buffer_load_dwordx4 v20, s[28:31], s62 offen offset:0 lds\n"
    "v_mfma_f32_16x16x32_bf16 v[64:67], v[200:203], a[208:211], v[64:67]\n"
    "ds_read_b64_tr_b16 v[216:217], v28 offset:1024\n"
    "buffer_load_dwordx4 v21, s[28:31], s62 offen offset:1024 lds\n"
    "v_mfma_f32_16x16x32_bf16 v[68:71], v[200:203], a[212:215], v[68:71]\n"
    "ds_read_b64_tr_b16 v[218:219], v28 offset:1536\n"
    "v_mfma_f32_16x16x32_bf16 v[72:75], v[200:203], a[216:219], v[72:75]\n"
    "ds_read_b64_tr_b16 v[220:221], v29 offset:1024\n"
    "v_mfma_f32_16x16x32_bf16 v[76:79], v[204:207], a[208:211], v[76:79]\n"
    "ds_read_b64_tr_b16 v[222:223], v29 offset:1536\n"
    "v_mfma_f32_16x16x32_bf16 v[80:83], v[204:207], a[212:215], v[80:83]\n"
    "s_sub_u32 s63, s63, 0x80800\n"
    "s_cselect_b32 s63, 0x686800, s63\n"
    "v_mfma_f32_16x16x32_bf16 v[84:87], v[204:207], a[216:219], v[84:87]\n"
    "s_waitcnt lgkmcnt(0)\n"
    "s_barrier\n"
    "v_mfma_f32_16x16x32_bf16 v[164:167], v[208:211], a[220:223], v[164:167]\n"
    "buffer_load_dwordx4 a[120:123], v34, s[36:39], s63 offen offset:0 sc1\n"
    "ds_read_b64_tr_b16 v[192:193], v32 offset:49152\n"
    "v_mfma_f32_16x16x32_bf16 v[168:171], v[208:211], a[224:227], v[168:171]\n"
    "ds_read_b64_tr_b16 v[194:195], v32 offset:49664\n"
    "buffer_load_dwordx4 a[124:127], v34, s[36:39], s63 offen offset:256 sc1\n"
    "v_mfma_f32_16x16x32_bf16 v[172:175], v[208:211], a[228:231], v[172:175]\n"
    "ds_read_b64_tr_b16 v[196:197], v33 offset:49152\n"
    "v_mfma_f32_16x16x32_bf16 v[176:179], v[212:215], a[220:223], v[176:179]\n"
    "ds_read_b64_tr_b16 v[198:199], v33 offset:49664\n"
    "v_mfma_f32_16x16x32_bf16 v[180:183], v[212:215], a[224:227], v[180:183]\n"
    "v_mfma_f32_16x16x32_bf16 v[184:187], v[212:215], a[228:231], v[184:187]\n"
    "buffer_load_dwordx4 a[128:131], v36, s[36:39], s63 offen offset:512 sc1\n"
    "v_mfma_f32_16x16x32_bf16 v[64:67], v[216:219], a[220:223], v[64:67]\n"
    "ds_read_b64_tr_b16 v[200:201], v32 offset:50176\n"
    "v_mfma_f32_16x16x32_bf16 v[68:71], v[216:219], a[224:227], v[68:71]\n"
    "ds_read_b64_tr_b16 v[202:203], v32 offset:50688\n"
    "v_mfma_f32_16x16x32_bf16 v[72:75], v[216:219], a[228:231], v[72:75]\n"
    "ds_read_b64_tr_b16 v[204:205], v33 offset:50176\n"
    "buffer_load_dwordx4 a[132:135], v35, s[36:39], s63 offen offset:0 sc1\n"
    "v_mfma_f32_16x16x32_bf16 v[76:79], v[220:223], a[220:223], v[76:79]\n"
    "ds_read_b64_tr_b16 v[206:207], v33 offset:50688\n"
    "v_mfma_f32_16x16x32_bf16 v[80:83], v[220:223], a[224:227], v[80:83]\n"
    "v_mfma_f32_16x16x32_bf16 v[84:87], v[220:223], a[228:231], v[84:87]\n"
    "s_waitcnt lgkmcnt(4)\n"
    "v_mfma_f32_16x16x32_bf16 v[64:67], v[192:195], a[232:235], v[64:67]\n"
    "ds_read_b64_tr_b16 v[208:209], v28 offset:32768\n"
    "buffer_load_dwordx4 a[136:139], v35, s[36:39], s63 offen offset:256 sc1\n"
    "v_mfma_f32_16x16x32_bf16 v[68:71], v[192:195], a[236:239], v[68:71]\n"
    "ds_read_b64_tr_b16 v[210:211], v28 offset:33280\n"
    "v_mfma_f32_16x16x32_bf16 v[72:75], v[192:195], a[240:243], v[72:75]\n"
    "ds_read_b64_tr_b16 v[212:213], v29 offset:32768\n"
    "v_mfma_f32_16x16x32_bf16 v[76:79], v[196:199], a[232:235], v[76:79]\n"
    "ds_read_b64_tr_b16 v[214:215], v29 offset:33280\n"
    "buffer_load_dwordx4 a[140:143], v37, s[36:39], s63 offen offset:512 sc1\n"
    "v_mfma_f32_16x16x32_bf16 v[80:83], v[196:199], a[236:239], v[80:83]\n"
    "s_add_u32 m0, s56, 0\n"
    "v_mfma_f32_16x16x32_bf16 v[84:87], v[196:199], a[240:243], v[84:87]\n"
    "s_waitcnt lgkmcnt(4)\n"
    "s_sub_u32 s61, s61, 0x28000\n"
    "s_cselect_b32 s61, 0x208000, s61\n"
    "v_mfma_f32_16x16x32_bf16 v[64:67], v[200:203], a[244:247], v[64:67]\n"
    "ds_read_b64_tr_b16 v[216:217], v28 offset:33792\n"
    "buffer_load_dwordx4 v20, s[28:31], s61 offen offset:0 lds\n"
    "v_mfma_f32_16x16x32_bf16 v[68:71], v[200:203], a[248:251], v[68:71]\n"
    "ds_read_b64_tr_b16 v[218:219], v28 offset:34304\n"
    "v_mfma_f32_16x16x32_bf16 v[72:75], v[200:203], a[252:255], v[72:75]\n"
    "ds_read_b64_tr_b16 v[220:221], v29 offset:33792\n"
    "v_mfma_f32_16x16x32_bf16 v[76:79], v[204:207], a[244:247], v[76:79]\n"
    "ds_read_b64_tr_b16 v[222:223], v29 offset:34304\n"
    "v_mfma_f32_16x16x32_bf16 v[80:83], v[204:207], a[248:251], v[80:83]\n"
    "buffer_load_dwordx4 v21, s[28:31], s61 offen offset:1024 lds\n"
    "s_add_u32 m0, s56, 0x2000\n"
    "v_mfma_f32_16x16x32_bf16 v[84:87], v[204:207], a[252:255], v[84:87]\n"
    "s_waitcnt lgkmcnt(4)\n"
    "s_sub_u32 s62, s62, 0x28000\n"
    "s_cselect_b32 s62, 0x208000, s62\n"
    "v_mfma_f32_16x16x32_bf16 v[64:67], v[208:211], a[0:3], v[64:67]\n"
    "ds_read_b64_tr_b16 v[192:193], v30 offset:49152\n"
    "v_mfma_f32_16x16x32_bf16 v[68:71], v[208:211], a[4:7], v[68:71]\n"
    "ds_read_b64_tr_b16 v[194:195], v30 offset:49664\n"
    "buffer_load_dwordx4 v20, s[28:31], s62 offen offset:0 lds\n"
    "v_mfma_f32_16x16x32_bf16 v[72:75], v[208:211], a[8:11], v[72:75]\n"
    "ds_read_b64_tr_b16 v[196:197], v31 offset:49152\n"
    "v_mfma_f32_16x16x32_bf16 v[76:79], v[212:215], a[0:3], v[76:79]\n"
    "ds_read_b64_tr_b16 v[198:199], v31 offset:49664\n"
    "v_mfma_f32_16x16x32_bf16 v[80:83], v[212:215], a[4:7], v[80:83]\n"
    "v_mfma_f32_16x16x32_bf16 v[84:87], v[212:215], a[8:11], v[84:87]\n"
    "s_waitcnt lgkmcnt(4)\n"
    "buffer_load_dwordx4 v21, s[28:31], s62 offen offset:1024 lds\n"
    "v_mfma_f32_16x16x32_bf16 v[64:67], v[216:219], a[12:15], v[64:67]\n"
    "ds_read_b64_tr_b16 v[200:201], v30 offset:50176\n"
    "v_mfma_f32_16x16x32_bf16 v[68:71], v[216:219], a[16:19], v[68:71]\n"
    "ds_read_b64_tr_b16 v[202:203], v30 offset:50688\n"
    "v_mfma_f32_16x16x32_bf16 v[72:75], v[216:219], a[20:23], v[72:75]\n"
    "ds_read_b64_tr_b16 v[204:205], v31 offset:50176\n"
    "v_mfma_f32_16x16x32_bf16 v[76:79], v[220:223], a[12:15], v[76:79]\n"
    "ds_read_b64_tr_b16 v[206:207], v31 offset:50688\n"
    "s_sub_u32 s63, s63, 0x80800\n"
    "s_cselect_b32 s63, 0x686800, s63\n"
    "v_mfma_f32_16x16x32_bf16 v[80:83], v[220:223], a[16:19], v[80:83]\n"
    "buffer_load_dwordx4 a[144:147], v34, s[36:39], s63 offen offset:0 sc1\n"
    "v_mfma_f32_16x16x32_bf16 v[84:87], v[220:223], a[20:23], v[84:87]\n"
    "s_waitcnt lgkmcnt(4)\n"
    "buffer_load_dwordx4 a[148:151], v34, s[36:39], s63 offen offset:256 sc1\n"
    "v_mfma_f32_16x16x32_bf16 v[164:167], v[192:195], a[232:235], v[164:167]\n"
    "ds_read_b64_tr_b16 v[208:209], v26 offset:32768\n"
    "v_mfma_f32_16x16x32_bf16 v[168:171], v[192:195], a[236:239], v[168:171]\n"
    "ds_read_b64_tr_b16 v[210:211], v26 offset:33280\n"
    "v_mfma_f32_16x16x32_bf16 v[172:175], v[192:195], a[240:243], v[172:175]\n"
    "ds_read_b64_tr_b16 v[212:213], v27 offset:32768\n"
    "v_mfma_f32_16x16x32_bf16 v[176:179], v[196:199], a[232:235], v[176:179]\n"
    "ds_read_b64_tr_b16 v[214:215], v27 offset:33280\n"
    "buffer_load_dwordx4 a[152:155], v36, s[36:39], s63 offen offset:512 sc1\n"
    "v_mfma_f32_16x16x32_bf16 v[180:183], v[196:199], a[236:239], v[180:183]\n"
    "ds_write_b128 v56, v[64:67] offset:0\n"
    "v_mfma_f32_16x16x32_bf16 v[184:187], v[196:199], a[240:243], v[184:187]\n"
    "s_waitcnt lgkmcnt(5)\n"
    "v_mfma_f32_16x16x32_bf16 v[164:167], v[200:203], a[244:247], v[164:167]\n"
    "ds_read_b64_tr_b16 v[216:217], v26 offset:33792\n"
    "buffer_load_dwordx4 a[156:159], v35, s[36:39], s63 offen offset:0 sc1\n"
    "v_mfma_f32_16x16x32_bf16 v[168:171], v[200:203], a[248:251], v[168:171]\n"
    "ds_read_b64_tr_b16 v[218:219], v26 offset:34304\n"
    "v_mfma_f32_16x16x32_bf16 v[172:175], v[200:203], a[252:255], v[172:175]\n"
    "ds_read_b64_tr_b16 v[220:221], v27 offset:33792\n"
    "v_mfma_f32_16x16x32_bf16 v[176:179], v[204:207], a[244:247], v[176:179]\n"
    "ds_read_b64_tr_b16 v[222:223], v27 offset:34304\n"
    "buffer_load_dwordx4 a[160:163], v35, s[36:39], s63 offen offset:256 sc1\n"
    "v_mfma_f32_16x16x32_bf16 v[180:183], v[204:207], a[248:251], v[180:183]\n"
    "ds_write_b128 v56, v[68:71] offset:1024\n"
    "v_mfma_f32_16x16x32_bf16 v[184:187], v[204:207], a[252:255], v[184:187]\n"
    "ds_write_b128 v56, v[72:75] offset:2048\n"
    "s_waitcnt lgkmcnt(7)\n"
    "v_mfma_f32_16x16x32_bf16 v[164:167], v[208:211], a[0:3], v[164:167]\n"
    "buffer_load_dwordx4 a[164:167], v37, s[36:39], s63 offen offset:512 sc1\n"
    "v_mfma_f32_16x16x32_bf16 v[168:171], v[208:211], a[4:7], v[168:171]\n"
    "s_sub_u32 s63, s63, 0x80800\n"
    "s_cselect_b32 s63, 0x686800, s63\n"
    "v_mfma_f32_16x16x32_bf16 v[172:175], v[208:211], a[8:11], v[172:175]\n"
    "ds_write_b128 v56, v[76:79] offset:3072\n"
    "v_mfma_f32_16x16x32_bf16 v[176:179], v[212:215], a[0:3], v[176:179]\n"
    "buffer_load_dwordx4 a[168:171], v34, s[36:39], s63 offen offset:0 sc1\n"
    "v_mfma_f32_16x16x32_bf16 v[180:183], v[212:215], a[4:7], v[180:183]\n"
    "v_mfma_f32_16x16x32_bf16 v[184:187], v[212:215], a[8:11], v[184:187]\n"
    "ds_write_b128 v56, v[80:83] offset:4096\n"
    "s_waitcnt lgkmcnt(4)\n"
    "v_mfma_f32_16x16x32_bf16 v[164:167], v[216:219], a[12:15], v[164:167]\n"
    "buffer_load_dwordx4 a[172:175], v34, s[36:39], s63 offen offset:256 sc1\n"
    "v_mfma_f32_16x16x32_bf16 v[168:171], v[216:219], a[16:19], v[168:171]\n"
    "v_mfma_f32_16x16x32_bf16 v[172:175], v[216:219], a[20:23], v[172:175]\n"
    "ds_write_b128 v56, v[84:87] offset:5120\n"
    "v_mfma_f32_16x16x32_bf16 v[176:179], v[220:223], a[12:15], v[176:179]\n"
    "buffer_load_dwordx4 a[176:179], v36, s[36:39], s63 offen offset:512 sc1\n"
    "v_mfma_f32_16x16x32_bf16 v[180:183], v[220:223], a[16:19], v[180:183]\n"
    "v_mfma_f32_16x16x32_bf16 v[184:187], v[220:223], a[20:23], v[184:187]\n"
    "s_waitcnt lgkmcnt(0)\n"
    "s_barrier\n"
    "ds_read_b128 v[224:227], v57 offset:0\n"
    "ds_read_b128 v[236:239], v57 offset:3072\n"
    "s_add_u32 m0, s56, 0x1c000\n"
    "s_sub_u32 s61, s61, 0x28000\n"
    "s_cselect_b32 s61, 0x208000, s61\n"
    "ds_read_b128 v[228:231], v57 offset:1024\n"
    "buffer_load_dwordx4 v20, s[28:31], s61 offen offset:0 lds\n"
    "buffer_load_dwordx4 v21, s[28:31], s61 offen offset:1024 lds\n"
    "ds_read_b128 v[240:243], v57 offset:4096\n"
    "ds_read_b128 v[232:235], v57 offset:2048\n"
    "ds_read_b128 v[244:247], v57 offset:5120\n"
    "s_barrier\n"
    "ds_read_b64_tr_b16 v[192:193], v26 offset:49152\n"
    "ds_read_b64_tr_b16 v[194:195], v26 offset:49664\n"
    "ds_read_b64_tr_b16 v[196:197], v27 offset:49152\n"
    "ds_read_b64_tr_b16 v[198:199], v27 offset:49664\n"
    "s_waitcnt lgkmcnt(9)\n"
    "v_pk_add_f32 v[164:165], v[164:165], v[224:225] op_sel:[0,0] op_sel_hi:[1,1]\n"
    "v_pk_add_f32 v[166:167], v[166:167], v[226:227] op_sel:[0,0] op_sel_hi:[1,1]\n"
    "s_waitcnt lgkmcnt(8)\n"
    "v_pk_add_f32 v[176:177], v[176:177], v[236:237] op_sel:[0,0] op_sel_hi:[1,1]\n"
    "v_pk_add_f32 v[178:179], v[178:179], v[238:239] op_sel:[0,0] op_sel_hi:[1,1]\n"
    "s_waitcnt lgkmcnt(7)\n"
    "v_pk_add_f32 v[168:169], v[168:169], v[228:229] op_sel:[0,0] op_sel_hi:[1,1]\n"
    "v_pk_add_f32 v[170:171], v[170:171], v[230:231] op_sel:[0,0] op_sel_hi:[1,1]\n"
    "s_waitcnt lgkmcnt(6)\n"
    "v_pk_add_f32 v[180:181], v[180:181], v[240:241] op_sel:[0,0] op_sel_hi:[1,1]\n"
    "v_pk_add_f32 v[182:183], v[182:183], v[242:243] op_sel:[0,0] op_sel_hi:[1,1]\n"
    "s_waitcnt lgkmcnt(5)\n"
    "v_pk_add_f32 v[172:173], v[172:173], v[232:233] op_sel:[0,0] op_sel_hi:[1,1]\n"
    "v_pk_add_f32 v[174:175], v[174:175], v[234:235] op_sel:[0,0] op_sel_hi:[1,1]\n"
    "s_waitcnt lgkmcnt(4)\n"
    "v_pk_add_f32 v[184:185], v[184:185], v[244:245] op_sel:[0,0] op_sel_hi:[1,1]\n"
    "v_pk_add_f32 v[186:187], v[186:187], v[246:247] op_sel:[0,0] op_sel_hi:[1,1]\n"
    "s_add_u32 m0, s56, 0x1e000\n"
    "s_sub_u32 s62, s62, 0x28000\n"
    "s_cselect_b32 s62, 0x208000, s62\n"
    "ds_read_b64_tr_b16 v[200:201], v28 offset:49152\n"
    "buffer_load_dwordx4 v20, s[28:31], s62 offen offset:0 lds\n"
    "ds_read_b64_tr_b16 v[202:203], v28 offset:49664\n"
    "ds_read_b64_tr_b16 v[204:205], v29 offset:49152\n"
    "ds_read_b64_tr_b16 v[206:207], v29 offset:49664\n"
    "s_waitcnt lgkmcnt(4)\n"
    "v_mfma_f32_16x16x32_bf16 v[64:67], v[192:195], a[24:27], v[128:131]\n"
    "ds_read_b64_tr_b16 v[208:209], v26 offset:50176\n"
    "buffer_load_dwordx4 v21, s[28:31], s62 offen offset:1024 lds\n"
    "v_mfma_f32_16x16x32_bf16 v[68:71], v[192:195], a[28:31], v[132:135]\n"
    "ds_read_b64_tr_b16 v[210:211], v26 offset:50688\n"
    "v_mfma_f32_16x16x32_bf16 v[72:75], v[192:195], a[32:35], v[136:139]\n"
    "ds_read_b64_tr_b16 v[212:213], v27 offset:50176\n"
    "v_mfma_f32_16x16x32_bf16 v[76:79], v[196:199], a[24:27], v[140:143]\n"
    "ds_read_b64_tr_b16 v[214:215], v27 offset:50688\n"
    "buffer_load_dwordx4 a[180:183], v35, s[36:39], s63 offen offset:0 sc1\n"
    "v_mfma_f32_16x16x32_bf16 v[80:83], v[196:199], a[28:31], v[144:147]\n"
    "v_mfma_f32_16x16x32_bf16 v[84:87], v[196:199], a[32:35], v[148:151]\n"
    "s_mul_i32 s92, s93, 0xe00\n"
    "s_add_u32 s92, s92, 0x31000\n"
    "buffer_load_dwordx2 v[6:7], v8, s[36:39], s92 offen offset:0 sc1\n"
    "s_waitcnt lgkmcnt(4)\n"
    "v_mfma_f32_16x16x32_bf16 v[88:91], v[200:203], a[24:27], 0\n"
    "ds_read_b64_tr_b16 v[216:217], v28 offset:50176\n"
    "v_mfma_f32_16x16x32_bf16 v[92:95], v[200:203], a[28:31], 0\n"
    "ds_read_b64_tr_b16 v[218:219], v28 offset:50688\n"
    "v_mfma_f32_16x16x32_bf16 v[96:99], v[200:203], a[32:35], 0\n"
    "ds_read_b64_tr_b16 v[220:221], v29 offset:50176\n"
    "buffer_load_dwordx4 a[184:187], v35, s[36:39], s63 offen offset:256 sc1\n"
    "v_mfma_f32_16x16x32_bf16 v[100:103], v[204:207], a[24:27], 0\n"
    "ds_read_b64_tr_b16 v[222:223], v29 offset:50688\n"
    "v_mfma_f32_16x16x32_bf16 v[104:107], v[204:207], a[28:31], 0\n"
    "v_mfma_f32_16x16x32_bf16 v[108:111], v[204:207], a[32:35], 0\n"
    "s_waitcnt lgkmcnt(0)\n"
    "s_barrier\n"
    "v_mfma_f32_16x16x32_bf16 v[64:67], v[208:211], a[36:39], v[64:67]\n"
    "ds_read_b64_tr_b16 v[192:193], v26 offset:16384\n"
    "buffer_load_dwordx4 a[188:191], v37, s[36:39], s63 offen offset:512 sc1\n"
    "v_mfma_f32_16x16x32_bf16 v[68:71], v[208:211], a[40:43], v[68:71]\n"
    "ds_read_b64_tr_b16 v[194:195], v26 offset:16896\n"
    "s_add_u32 m0, s56, 0x8000\n"
    "v_mfma_f32_16x16x32_bf16 v[72:75], v[208:211], a[44:47], v[72:75]\n"
    "ds_read_b64_tr_b16 v[196:197], v27 offset:16384\n"
    "s_mul_i32 s61, s75, 0x28000\n"
    "v_mfma_f32_16x16x32_bf16 v[76:79], v[212:215], a[36:39], v[76:79]\n"
    "ds_read_b64_tr_b16 v[198:199], v27 offset:16896\n"
    "v_add_u32 v20, 0x14000, v20\n"
    "v_add_u32 v21, 0x14000, v21\n"
    "v_mfma_f32_16x16x32_bf16 v[80:83], v[212:215], a[40:43], v[80:83]\n"
    "buffer_load_dwordx4 v20, s[28:31], s61 offen offset:0 lds\n"
    "v_mfma_f32_16x16x32_bf16 v[84:87], v[212:215], a[44:47], v[84:87]\n"
    "buffer_load_dwordx4 v21, s[28:31], s61 offen offset:1024 lds\n"
    "s_add_u32 m0, s56, 0xa000\n"
    "v_mfma_f32_16x16x32_bf16 v[88:91], v[216:219], a[36:39], v[88:91]\n"
    "ds_read_b64_tr_b16 v[200:201], v28 offset:16384\n"
    "s_add_u32 s62, s61, 0xf0000\n"
    "v_mfma_f32_16x16x32_bf16 v[92:95], v[216:219], a[40:43], v[92:95]\n"
    "ds_read_b64_tr_b16 v[202:203], v28 offset:16896\n"
    "v_mfma_f32_16x16x32_bf16 v[96:99], v[216:219], a[44:47], v[96:99]\n"
    "ds_read_b64_tr_b16 v[204:205], v29 offset:16384\n"
    "v_mfma_f32_16x16x32_bf16 v[100:103], v[220:223], a[36:39], v[100:103]\n"
    "ds_read_b64_tr_b16 v[206:207], v29 offset:16896\n"
    "v_mfma_f32_16x16x32_bf16 v[104:107], v[220:223], a[40:43], v[104:107]\n"
    "s_cmp_ge_u32 s62, 0x208000\n"
    "s_cselect_b32 s0, 0x208000, 0\n"
    "s_sub_u32 s62, s62, s0\n"
    "v_mfma_f32_16x16x32_bf16 v[108:111], v[220:223], a[44:47], v[108:111]\n"
    "s_waitcnt lgkmcnt(4)\n"
    "buffer_load_dwordx4 v20, s[28:31], s62 offen offset:0 lds\n"
    "v_mfma_f32_16x16x32_bf16 v[64:67], v[192:195], a[48:51], v[64:67]\n"
    "ds_read_b64_tr_b16 v[208:209], v26 offset:17408\n"
    "buffer_load_dwordx4 v21, s[28:31], s62 offen offset:1024 lds\n"
    "v_mfma_f32_16x16x32_bf16 v[68:71], v[192:195], a[52:55], v[68:71]\n"
    "ds_read_b64_tr_b16 v[210:211], v26 offset:17920\n"
    "s_add_u32 m0, s56, 0xc000\n"
    "v_mfma_f32_16x16x32_bf16 v[72:75], v[192:195], a[56:59], v[72:75]\n"
    "ds_read_b64_tr_b16 v[212:213], v27 offset:17408\n"
    "v_mfma_f32_16x16x32_bf16 v[76:79], v[196:199], a[48:51], v[76:79]\n"
    "ds_read_b64_tr_b16 v[214:215], v27 offset:17920\n"
    "v_mfma_f32_16x16x32_bf16 v[80:83], v[196:199], a[52:55], v[80:83]\n"
    "s_sub_u32 s61, s61, 0x28000\n"
    "s_cselect_b32 s61, 0x1e0000, s61\n"
    "v_mfma_f32_16x16x32_bf16 v[84:87], v[196:199], a[56:59], v[84:87]\n"
    "s_waitcnt lgkmcnt(4)\n"
    "buffer_load_dwordx4 v20, s[28:31], s61 offen offset:0 lds\n"
    "v_mfma_f32_16x16x32_bf16 v[88:91], v[200:203], a[48:51], v[88:91]\n"
    "ds_read_b64_tr_b16 v[216:217], v28 offset:17408\n"
    "v_mfma_f32_16x16x32_bf16 v[92:95], v[200:203], a[52:55], v[92:95]\n"
    "ds_read_b64_tr_b16 v[218:219], v28 offset:17920\n"
    "buffer_load_dwordx4 v21, s[28:31], s61 offen offset:1024 lds\n"
    "v_mfma_f32_16x16x32_bf16 v[96:99], v[200:203], a[56:59], v[96:99]\n"
    "ds_read_b64_tr_b16 v[220:221], v29 offset:17408\n"
    "s_add_u32 m0, s56, 0xe000\n"
    "v_mfma_f32_16x16x32_bf16 v[100:103], v[204:207], a[48:51], v[100:103]\n"
    "ds_read_b64_tr_b16 v[222:223], v29 offset:17920\n"
    "v_mfma_f32_16x16x32_bf16 v[104:107], v[204:207], a[52:55], v[104:107]\n"
    "s_sub_u32 s62, s62, 0x28000\n"
    "s_cselect_b32 s62, 0x1e0000, s62\n"
    "v_mfma_f32_16x16x32_bf16 v[108:111], v[204:207], a[56:59], v[108:111]\n"
    "s_waitcnt vmcnt(8)\n"
    "v_cmp_eq_u64 vcc, v[6:7], v[4:5]\n"
    "s_or_b32 vcc_lo, vcc_lo, s82\n"
    "s_cmp_eq_u32 vcc_lo, -1\n"
    "s_cbranch_scc0 .Lspin_GB\n"
    ".Ldone_GB:\n"
    "s_waitcnt lgkmcnt(4)\n"
    "s_barrier\n"
    "s_mul_i32 s63, s54, 6\n"
    "s_add_u32 s63, s63, s75\n"
    "s_cmp_ge_u32 s63, 13\n"
    "s_cselect_b32 s1, 13, 0\n"
    "s_sub_u32 s63, s63, s1\n"
    "s_mul_i32 s63, s63, 0x80800\n"
    "v_add_u32 v34, 0x40400, v34\n"
    "v_add_u32 v35, 0x40400, v35\n"
    "v_add_u32 v36, 0x40400, v36\n"
    "v_add_u32 v37, 0x40400, v37\n"
    "buffer_load_dwordx4 a[192:195], v34, s[36:39], s63 offen offset:0 sc1\n"
    "v_mfma_f32_16x16x32_bf16 v[64:67], v[208:211], a[60:63], v[64:67]\n"
    "ds_read_b64_tr_b16 v[192:193], v30 offset:16384\n"
    "buffer_load_dwordx4 a[196:199], v34, s[36:39], s63 offen offset:256 sc1\n"
    "v_mfma_f32_16x16x32_bf16 v[68:71], v[208:211], a[64:67], v[68:71]\n"
    "ds_read_b64_tr_b16 v[194:195], v30 offset:16896\n"
    "v_mfma_f32_16x16x32_bf16 v[72:75], v[208:211], a[68:71], v[72:75]\n"
    "ds_read_b64_tr_b16 v[196:197], v31 offset:16384\n"
    "v_mfma_f32_16x16x32_bf16 v[76:79], v[212:215], a[60:63], v[76:79]\n"
    "ds_read_b64_tr_b16 v[198:199], v31 offset:16896\n"
    "v_mfma_f32_16x16x32_bf16 v[80:83], v[212:215], a[64:67], v[80:83]\n"
    "buffer_load_dwordx4 a[200:203], v36, s[36:39], s63 offen offset:512 sc1\n"
    "v_mfma_f32_16x16x32_bf16 v[84:87], v[212:215], a[68:71], v[84:87]\n"
    "s_waitcnt lgkmcnt(4)\n"
    "v_mfma_f32_16x16x32_bf16 v[88:91], v[216:219], a[60:63], v[88:91]\n"
    "ds_read_b64_tr_b16 v[200:201], v32 offset:16384\n"
    "v_mfma_f32_16x16x32_bf16 v[92:95], v[216:219], a[64:67], v[92:95]\n"
    "ds_read_b64_tr_b16 v[202:203], v32 offset:16896\n"
    "buffer_load_dwordx4 a[204:207], v35, s[36:39], s63 offen offset:0 sc1\n"
    "v_mfma_f32_16x16x32_bf16 v[96:99], v[216:219], a[68:71], v[96:99]\n"
    "ds_read_b64_tr_b16 v[204:205], v33 offset:16384\n"
    "v_mfma_f32_16x16x32_bf16 v[100:103], v[220:223], a[60:63], v[100:103]\n"
    "ds_read_b64_tr_b16 v[206:207], v33 offset:16896\n"
    "v_mfma_f32_16x16x32_bf16 v[104:107], v[220:223], a[64:67], v[104:107]\n"
    "v_mfma_f32_16x16x32_bf16 v[108:111], v[220:223], a[68:71], v[108:111]\n"
    "s_waitcnt lgkmcnt(4)\n"
    "buffer_load_dwordx4 a[208:211], v35, s[36:39], s63 offen offset:256 sc1\n"
    "v_mfma_f32_16x16x32_bf16 v[64:67], v[192:195], a[72:75], v[64:67]\n"
    "ds_read_b64_tr_b16 v[208:209], v30 offset:17408\n"
    "v_mfma_f32_16x16x32_bf16 v[68:71], v[192:195], a[76:79], v[68:71]\n"
    "ds_read_b64_tr_b16 v[210:211], v30 offset:17920\n"
    "v_mfma_f32_16x16x32_bf16 v[72:75], v[192:195], a[80:83], v[72:75]\n"
    "ds_read_b64_tr_b16 v[212:213], v31 offset:17408\n"
    "buffer_load_dwordx4 a[212:215], v37, s[36:39], s63 offen offset:512 sc1\n"
    "v_mfma_f32_16x16x32_bf16 v[76:79], v[196:199], a[72:75], v[76:79]\n"
    "ds_read_b64_tr_b16 v[214:215], v31 offset:17920\n"
    "v_mfma_f32_16x16x32_bf16 v[80:83], v[196:199], a[76:79], v[80:83]\n"
    "v_mfma_f32_16x16x32_bf16 v[84:87], v[196:199], a[80:83], v[84:87]\n"
    "s_waitcnt lgkmcnt(4)\n"
    "v_mfma_f32_16x16x32_bf16 v[88:91], v[200:203], a[72:75], v[88:91]\n"
    "ds_read_b64_tr_b16 v[216:217], v32 offset:17408\n"
    "buffer_load_dwordx4 v20, s[28:31], s62 offen offset:0 lds\n"
    "v_mfma_f32_16x16x32_bf16 v[92:95], v[200:203], a[76:79], v[92:95]\n"
    "ds_read_b64_tr_b16 v[218:219], v32 offset:17920\n"
    "v_mfma_f32_16x16x32_bf16 v[96:99], v[200:203], a[80:83], v[96:99]\n"
    "ds_read_b64_tr_b16 v[220:221], v33 offset:17408\n"
    "v_mfma_f32_16x16x32_bf16 v[100:103], v[204:207], a[72:75], v[100:103]\n"
    "ds_read_b64_tr_b16 v[222:223], v33 offset:17920\n"
    "buffer_load_dwordx4 v21, s[28:31], s62 offen offset:1024 lds\n"
    "v_mfma_f32_16x16x32_bf16 v[104:107], v[204:207], a[76:79], v[104:107]\n"
    "s_sub_u32 s63, s63, 0x80800\n"
    "s_cselect_b32 s63, 0x606000, s63\n"
    "v_mfma_f32_16x16x32_bf16 v[108:111], v[204:207], a[80:83], v[108:111]\n"
    "s_waitcnt lgkmcnt(4)\n"
    "s_barrier\n"
    "v_mfma_f32_16x16x32_bf16 v[64:67], v[208:211], a[84:87], v[64:67]\n"
    "ds_read_b64_tr_b16 v[192:193], v30 offset:32768\n"
    "buffer_load_dwordx4 a[216:219], v34, s[36:39], s63 offen offset:0 sc1\n"
    "v_mfma_f32_16x16x32_bf16 v[68:71], v[208:211], a[88:91], v[68:71]\n"
    "ds_read_b64_tr_b16 v[194:195], v30 offset:33280\n"
    "v_mfma_f32_16x16x32_bf16 v[72:75], v[208:211], a[92:95], v[72:75]\n"
    "ds_read_b64_tr_b16 v[196:197], v31 offset:32768\n"
    "v_mfma_f32_16x16x32_bf16 v[76:79], v[212:215], a[84:87], v[76:79]\n"
    "ds_read_b64_tr_b16 v[198:199], v31 offset:33280\n"
    "buffer_load_dwordx4 a[220:223], v34, s[36:39], s63 offen offset:256 sc1\n"
    "v_mfma_f32_16x16x32_bf16 v[80:83], v[212:215], a[88:91], v[80:83]\n"
    "v_mfma_f32_16x16x32_bf16 v[84:87], v[212:215], a[92:95], v[84:87]\n"
    "s_waitcnt lgkmcnt(4)\n"
    "v_mfma_f32_16x16x32_bf16 v[88:91], v[216:219], a[84:87], v[88:91]\n"
    "ds_read_b64_tr_b16 v[200:201], v32 offset:32768\n"
    "buffer_load_dwordx4 a[224:227], v36, s[36:39], s63 offen offset:512 sc1\n"
    "v_mfma_f32_16x16x32_bf16 v[92:95], v[216:219], a[88:91], v[92:95]\n"
    "ds_read_b64_tr_b16 v[202:203], v32 offset:33280\n"
    "v_mfma_f32_16x16x32_bf16 v[96:99], v[216:219], a[92:95], v[96:99]\n"
    "ds_read_b64_tr_b16 v[204:205], v33 offset:32768\n"
    "v_mfma_f32_16x16x32_bf16 v[100:103], v[220:223], a[84:87], v[100:103]\n"
    "ds_read_b64_tr_b16 v[206:207], v33 offset:33280\n"
    "v_mfma_f32_16x16x32_bf16 v[104:107], v[220:223], a[88:91], v[104:107]\n"
    "buffer_load_dwordx4 a[228:231], v35, s[36:39], s63 offen offset:0 sc1\n"
    "v_mfma_f32_16x16x32_bf16 v[108:111], v[220:223], a[92:95], v[108:111]\n"
    "s_waitcnt lgkmcnt(4)\n"
    "v_mfma_f32_16x16x32_bf16 v[64:67], v[192:195], a[96:99], v[64:67]\n"
    "ds_read_b64_tr_b16 v[208:209], v30 offset:33792\n"
    "v_mfma_f32_16x16x32_bf16 v[68:71], v[192:195], a[100:103], v[68:71]\n"
    "ds_read_b64_tr_b16 v[210:211], v30 offset:34304\n"
    "buffer_load_dwordx4 a[232:235], v35, s[36:39], s63 offen offset:256 sc1\n"
    "v_mfma_f32_16x16x32_bf16 v[72:75], v[192:195], a[104:107], v[72:75]\n"
    "ds_read_b64_tr_b16 v[212:213], v31 offset:33792\n"
    "v_mfma_f32_16x16x32_bf16 v[76:79], v[196:199], a[96:99], v[76:79]\n"
    "ds_read_b64_tr_b16 v[214:215], v31 offset:34304\n"
    "v_mfma_f32_16x16x32_bf16 v[80:83], v[196:199], a[100:103], v[80:83]\n"
    "v_mfma_f32_16x16x32_bf16 v[84:87], v[196:199], a[104:107], v[84:87]\n"
    "s_waitcnt lgkmcnt(4)\n"
    "buffer_load_dwordx4 a[236:239], v37, s[36:39], s63 offen offset:512 sc1\n"
    "s_add_u32 m0, s56, 0x4000\n"
    "v_mfma_f32_16x16x32_bf16 v[88:91], v[200:203], a[96:99], v[88:91]\n"
    "ds_read_b64_tr_b16 v[216:217], v32 offset:33792\n"
    "v_mfma_f32_16x16x32_bf16 v[92:95], v[200:203], a[100:103], v[92:95]\n"
    "ds_read_b64_tr_b16 v[218:219], v32 offset:34304\n"
    "v_mfma_f32_16x16x32_bf16 v[96:99], v[200:203], a[104:107], v[96:99]\n"
    "ds_read_b64_tr_b16 v[220:221], v33 offset:33792\n"
    "v_mfma_f32_16x16x32_bf16 v[100:103], v[204:207], a[96:99], v[100:103]\n"
    "ds_read_b64_tr_b16 v[222:223], v33 offset:34304\n"
    "s_sub_u32 s61, s61, 0x28000\n"
    "s_cselect_b32 s61, 0x1e0000, s61\n"
    "v_mfma_f32_16x16x32_bf16 v[104:107], v[204:207], a[100:103], v[104:107]\n"
    "buffer_load_dwordx4 v20, s[28:31], s61 offen offset:0 lds\n"
    "v_mfma_f32_16x16x32_bf16 v[108:111], v[204:207], a[104:107], v[108:111]\n"
    "s_waitcnt lgkmcnt(4)\n"
    "s_barrier\n"
    "v_mfma_f32_16x16x32_bf16 v[64:67], v[208:211], a[108:111], v[64:67]\n"
    "ds_read_b64_tr_b16 v[192:193], v30 offset:0\n"
    "buffer_load_dwordx4 v21, s[28:31], s61 offen offset:1024 lds\n"
    "v_mfma_f32_16x16x32_bf16 v[68:71], v[208:211], a[112:115], v[68:71]\n"
    "ds_read_b64_tr_b16 v[194:195], v30 offset:512\n"
    "s_add_u32 m0, s56, 0x6000\n"
    "v_mfma_f32_16x16x32_bf16 v[72:75], v[208:211], a[116:119], v[72:75]\n"
    "ds_read_b64_tr_b16 v[196:197], v31 offset:0\n"
    "v_mfma_f32_16x16x32_bf16 v[76:79], v[212:215], a[108:111], v[76:79]\n"
    "ds_read_b64_tr_b16 v[198:199], v31 offset:512\n"
    "s_sub_u32 s62, s62, 0x28000\n"
    "s_cselect_b32 s62, 0x1e0000, s62\n"
    "v_mfma_f32_16x16x32_bf16 v[80:83], v[212:215], a[112:115], v[80:83]\n"
    "buffer_load_dwordx4 v20, s[28:31], s62 offen offset:0 lds\n"
    "v_mfma_f32_16x16x32_bf16 v[84:87], v[212:215], a[116:119], v[84:87]\n"
    "s_waitcnt lgkmcnt(4)\n"
    "buffer_load_dwordx4 v21, s[28:31], s62 offen offset:1024 lds\n"
    "v_mfma_f32_16x16x32_bf16 v[88:91], v[216:219], a[108:111], v[88:91]\n"
    "ds_read_b64_tr_b16 v[200:201], v32 offset:0\n"
    "v_mfma_f32_16x16x32_bf16 v[92:95], v[216:219], a[112:115], v[92:95]\n"
    "ds_read_b64_tr_b16 v[202:203], v32 offset:512\n"
    "v_mfma_f32_16x16x32_bf16 v[96:99], v[216:219], a[116:119], v[96:99]\n"
    "ds_read_b64_tr_b16 v[204:205], v33 offset:0\n"
    "v_mfma_f32_16x16x32_bf16 v[100:103], v[220:223], a[108:111], v[100:103]\n"
    "ds_read_b64_tr_b16 v[206:207], v33 offset:512\n"
    "v_mfma_f32_16x16x32_bf16 v[104:107], v[220:223], a[112:115], v[104:107]\n"
    "s_sub_u32 s63, s63, 0x80800\n"
    "s_cselect_b32 s63, 0x606000, s63\n"
    "v_mfma_f32_16x16x32_bf16 v[108:111], v[220:223], a[116:119], v[108:111]\n"
    "s_waitcnt lgkmcnt(4)\n"
    "buffer_load_dwordx4 a[240:243], v34, s[36:39], s63 offen offset:0 sc1\n"
    "v_mfma_f32_16x16x32_bf16 v[64:67], v[192:195], a[120:123], v[64:67]\n"
    "ds_read_b64_tr_b16 v[208:209], v30 offset:1024\n"
    "buffer_load_dwordx4 a[244:247], v34, s[36:39], s63 offen offset:256 sc1\n"
    "v_mfma_f32_16x16x32_bf16 v[68:71], v[192:195], a[124:127], v[68:71]\n"
    "ds_read_b64_tr_b16 v[210:211], v30 offset:1536\n"
    "v_mfma_f32_16x16x32_bf16 v[72:75], v[192:195], a[128:131], v[72:75]\n"
    "ds_read_b64_tr_b16 v[212:213], v31 offset:1024\n"
    "v_mfma_f32_16x16x32_bf16 v[76:79], v[196:199], a[120:123], v[76:79]\n"
    "ds_read_b64_tr_b16 v[214:215], v31 offset:1536\n"
    "v_mfma_f32_16x16x32_bf16 v[80:83], v[196:199], a[124:127], v[80:83]\n"
    "buffer_load_dwordx4 a[248:251], v36, s[36:39], s63 offen offset:512 sc1\n"
    "v_mfma_f32_16x16x32_bf16 v[84:87], v[196:199], a[128:131], v[84:87]\n"
    "s_waitcnt lgkmcnt(4)\n"
    "v_mfma_f32_16x16x32_bf16 v[88:91], v[200:203], a[120:123], v[88:91]\n"
    "ds_read_b64_tr_b16 v[216:217], v32 offset:1024\n"
    "v_mfma_f32_16x16x32_bf16 v[92:95], v[200:203], a[124:127], v[92:95]\n"
    "ds_read_b64_tr_b16 v[218:219], v32 offset:1536\n"
    "buffer_load_dwordx4 a[252:255], v35, s[36:39], s63 offen offset:0 sc1\n"
    "v_mfma_f32_16x16x32_bf16 v[96:99], v[200:203], a[128:131], v[96:99]\n"
    "ds_read_b64_tr_b16 v[220:221], v33 offset:1024\n"
    "v_mfma_f32_16x16x32_bf16 v[100:103], v[204:207], a[120:123], v[100:103]\n"
    "ds_read_b64_tr_b16 v[222:223], v33 offset:1536\n"
    "v_mfma_f32_16x16x32_bf16 v[104:107], v[204:207], a[124:127], v[104:107]\n"
    "v_mfma_f32_16x16x32_bf16 v[108:111], v[204:207], a[128:131], v[108:111]\n"
    "s_waitcnt lgkmcnt(4)\n"
    "s_barrier\n"
    "v_mfma_f32_16x16x32_bf16 v[64:67], v[208:211], a[132:135], v[64:67]\n"
    "ds_read_b64_tr_b16 v[192:193], v26 offset:0\n"
    "buffer_load_dwordx4 a[0:3], v35, s[36:39], s63 offen offset:256 sc1\n"
    "v_mfma_f32_16x16x32_bf16 v[68:71], v[208:211], a[136:139], v[68:71]\n"
    "ds_read_b64_tr_b16 v[194:195], v26 offset:512\n"
    "buffer_load_dwordx4 a[4:7], v37, s[36:39], s63 offen offset:512 sc1\n"
    "v_mfma_f32_16x16x32_bf16 v[72:75], v[208:211], a[140:143], v[72:75]\n"
    "ds_read_b64_tr_b16 v[196:197], v27 offset:0\n"
    "s_add_u32 m0, s56, 0x14000\n"
    "v_mfma_f32_16x16x32_bf16 v[76:79], v[212:215], a[132:135], v[76:79]\n"
    "ds_read_b64_tr_b16 v[198:199], v27 offset:512\n"
    "v_mfma_f32_16x16x32_bf16 v[80:83], v[212:215], a[136:139], v[80:83]\n"
    "s_sub_u32 s61, s61, 0x28000\n"
    "s_cselect_b32 s61, 0x1e0000, s61\n"
    "v_mfma_f32_16x16x32_bf16 v[84:87], v[212:215], a[140:143], v[84:87]\n"
    "s_waitcnt lgkmcnt(4)\n"
    "buffer_load_dwordx4 v20, s[28:31], s61 offen offset:0 lds\n"
    "v_mfma_f32_16x16x32_bf16 v[88:91], v[216:219], a[132:135], v[88:91]\n"
    "ds_read_b64_tr_b16 v[200:201], v28 offset:0\n"
    "v_mfma_f32_16x16x32_bf16 v[92:95], v[216:219], a[136:139], v[92:95]\n"
    "ds_read_b64_tr_b16 v[202:203], v28 offset:512\n"
    "v_mfma_f32_16x16x32_bf16 v[96:99], v[216:219], a[140:143], v[96:99]\n"
    "ds_read_b64_tr_b16 v[204:205], v29 offset:0\n"
    "buffer_load_dwordx4 v21, s[28:31], s61 offen offset:1024 lds\n"
    "v_mfma_f32_16x16x32_bf16 v[100:103], v[220:223], a[132:135], v[100:103]\n"
    "ds_read_b64_tr_b16 v[206:207], v29 offset:512\n"
    "s_add_u32 m0, s56, 0x16000\n"
    "v_mfma_f32_16x16x32_bf16 v[104:107], v[220:223], a[136:139], v[104:107]\n"
    "s_sub_u32 s62, s62, 0x28000\n"
    "s_cselect_b32 s62, 0x1e0000, s62\n"
    "v_mfma_f32_16x16x32_bf16 v[108:111], v[220:223], a[140:143], v[108:111]\n"
    "s_waitcnt lgkmcnt(4)\n"
    "v_mfma_f32_16x16x32_bf16 v[64:67], v[192:195], a[144:147], v[64:67]\n"
    "ds_read_b64_tr_b16 v[208:209], v26 offset:1024\n"
    "buffer_load_dwordx4 v20, s[28:31], s62 offen offset:0 lds\n"
    "v_mfma_f32_16x16x32_bf16 v[68:71], v[192:195], a[148:151], v[68:71]\n"
    "ds_read_b64_tr_b16 v[210:211], v26 offset:1536\n"
    "v_mfma_f32_16x16x32_bf16 v[72:75], v[192:195], a[152:155], v[72:75]\n"
    "ds_read_b64_tr_b16 v[212:213], v27 offset:1024\n"
    "v_mfma_f32_16x16x32_bf16 v[76:79], v[196:199], a[144:147], v[76:79]\n"
    "ds_read_b64_tr_b16 v[214:215], v27 offset:1536\n"
    "buffer_load_dwordx4 v21, s[28:31], s62 offen offset:1024 lds\n"
    "v_mfma_f32_16x16x32_bf16 v[80:83], v[196:199], a[148:151], v[80:83]\n"
    "s_sub_u32 s63, s63, 0x80800\n"
    "s_cselect_b32 s63, 0x606000, s63\n"
    "v_mfma_f32_16x16x32_bf16 v[84:87], v[196:199], a[152:155], v[84:87]\n"
    "s_waitcnt lgkmcnt(4)\n"
    "v_mfma_f32_16x16x32_bf16 v[88:91], v[200:203], a[144:147], v[88:91]\n"
    "ds_read_b64_tr_b16 v[216:217], v28 offset:1024\n"
    "buffer_load_dwordx4 a[8:11], v34, s[36:39], s63 offen offset:0 sc1\n"
    "v_mfma_f32_16x16x32_bf16 v[92:95], v[200:203], a[148:151], v[92:95]\n"
    "ds_read_b64_tr_b16 v[218:219], v28 offset:1536\n"
    "v_mfma_f32_16x16x32_bf16 v[96:99], v[200:203], a[152:155], v[96:99]\n"
    "ds_read_b64_tr_b16 v[220:221], v29 offset:1024\n"
    "v_mfma_f32_16x16x32_bf16 v[100:103], v[204:207], a[144:147], v[100:103]\n"
    "ds_read_b64_tr_b16 v[222:223], v29 offset:1536\n"
    "v_mfma_f32_16x16x32_bf16 v[104:107], v[204:207], a[148:151], v[104:107]\n"
    "buffer_load_dwordx4 a[12:15], v34, s[36:39], s63 offen offset:256 sc1\n"
    "v_mfma_f32_16x16x32_bf16 v[108:111], v[204:207], a[152:155], v[108:111]\n"
    "s_waitcnt lgkmcnt(4)\n"
    "s_barrier\n"
    "v_mfma_f32_16x16x32_bf16 v[64:67], v[208:211], a[156:159], v[64:67]\n"
    "ds_read_b64_tr_b16 v[192:193], v30 offset:49152\n"
    "buffer_load_dwordx4 a[16:19], v36, s[36:39], s63 offen offset:512 sc1\n"
    "v_mfma_f32_16x16x32_bf16 v[68:71], v[208:211], a[160:163], v[68:71]\n"
    "ds_read_b64_tr_b16 v[194:195], v30 offset:49664\n"
    "v_mfma_f32_16x16x32_bf16 v[72:75], v[208:211], a[164:167], v[72:75]\n"
    "ds_read_b64_tr_b16 v[196:197], v31 offset:49152\n"
    "v_mfma_f32_16x16x32_bf16 v[76:79], v[212:215], a[156:159], v[76:79]\n"
    "ds_read_b64_tr_b16 v[198:199], v31 offset:49664\n"
    "v_mfma_f32_16x16x32_bf16 v[80:83], v[212:215], a[160:163], v[80:83]\n"
    "buffer_load_dwordx4 a[20:23], v35, s[36:39], s63 offen offset:0 sc1\n"
    "v_mfma_f32_16x16x32_bf16 v[84:87], v[212:215], a[164:167], v[84:87]\n"
    "s_waitcnt lgkmcnt(4)\n"
    "v_mfma_f32_16x16x32_bf16 v[88:91], v[216:219], a[156:159], v[88:91]\n"
    "ds_read_b64_tr_b16 v[200:201], v32 offset:49152\n"
    "v_mfma_f32_16x16x32_bf16 v[92:95], v[216:219], a[160:163], v[92:95]\n"
    "ds_read_b64_tr_b16 v[202:203], v32 offset:49664\n"
    "buffer_load_dwordx4 a[24:27], v35, s[36:39], s63 offen offset:256 sc1\n"
    "v_mfma_f32_16x16x32_bf16 v[96:99], v[216:219], a[164:167], v[96:99]\n"
    "ds_read_b64_tr_b16 v[204:205], v33 offset:49152\n"
    "v_mfma_f32_16x16x32_bf16 v[100:103], v[220:223], a[156:159], v[100:103]\n"
    "ds_read_b64_tr_b16 v[206:207], v33 offset:49664\n"
    "v_mfma_f32_16x16x32_bf16 v[104:107], v[220:223], a[160:163], v[104:107]\n"
    "v_mfma_f32_16x16x32_bf16 v[108:111], v[220:223], a[164:167], v[108:111]\n"
    "s_waitcnt lgkmcnt(4)\n"
    "buffer_load_dwordx4 a[28:31], v37, s[36:39], s63 offen offset:512 sc1\n"
    "s_add_u32 m0, s56, 0x18000\n"
    "v_mfma_f32_16x16x32_bf16 v[64:67], v[192:195], a[168:171], v[64:67]\n"
    "ds_read_b64_tr_b16 v[208:209], v30 offset:50176\n"
    "s_mul_i32 s67, s55, 48\n"
    "v_mfma_f32_16x16x32_bf16 v[68:71], v[192:195], a[172:175], v[68:71]\n"
    "ds_read_b64_tr_b16 v[210:211], v30 offset:50688\n"
    "s_add_u32 s67, s67, s51\n"
    "v_mfma_f32_16x16x32_bf16 v[72:75], v[192:195], a[176:179], v[72:75]\n"
    "ds_read_b64_tr_b16 v[212:213], v31 offset:50176\n"
    "s_mul_i32 s67, s67, 0x500\n"
    "v_mfma_f32_16x16x32_bf16 v[76:79], v[196:199], a[168:171], v[76:79]\n"
    "ds_read_b64_tr_b16 v[214:215], v31 offset:50688\n"
    "s_lshl_b32 s68, s50, 7\n"
    "s_sub_u32 s61, s61, 0x28000\n"
    "s_cselect_b32 s61, 0x1e0000, s61\n"
    "v_mfma_f32_16x16x32_bf16 v[80:83], v[196:199], a[172:175], v[80:83]\n"
    "buffer_load_dwordx4 v20, s[28:31], s61 offen offset:0 lds\n"
    "s_add_u32 s67, s67, s68\n"
    "v_mfma_f32_16x16x32_bf16 v[84:87], v[196:199], a[176:179], v[84:87]\n"
    "s_waitcnt lgkmcnt(4)\n"
    "buffer_load_dwordx4 v21, s[28:31], s61 offen offset:1024 lds\n"
    "s_add_u32 m0, s56, 0x1a000\n"
    "v_mfma_f32_16x16x32_bf16 v[88:91], v[200:203], a[168:171], v[88:91]\n"
    "ds_read_b64_tr_b16 v[216:217], v32 offset:50176\n"
    "s_lshl_b32 s68, s54, 6\n"
    "v_mfma_f32_16x16x32_bf16 v[92:95], v[200:203], a[172:175], v[92:95]\n"
    "ds_read_b64_tr_b16 v[218:219], v32 offset:50688\n"
    "s_add_u32 s67, s67, s68\n"
    "v_mfma_f32_16x16x32_bf16 v[96:99], v[200:203], a[176:179], v[96:99]\n"
    "ds_read_b64_tr_b16 v[220:221], v33 offset:50176\n"
    "v_and_b32 v58, 1, v2\n"
    "v_mfma_f32_16x16x32_bf16 v[100:103], v[204:207], a[168:171], v[100:103]\n"
    "ds_read_b64_tr_b16 v[222:223], v33 offset:50688\n"
    "v_lshrrev_b32 v59, 1, v2\n"
    "v_mfma_f32_16x16x32_bf16 v[104:107], v[204:207], a[172:175], v[104:107]\n"
    "s_sub_u32 s62, s62, 0x28000\n"
    "s_cselect_b32 s62, 0x1e0000, s62\n"
    "v_mfma_f32_16x16x32_bf16 v[108:111], v[204:207], a[176:179], v[108:111]\n"
    "s_waitcnt vmcnt(38) lgkmcnt(4)\n"
    "s_barrier\n"
    "buffer_load_dwordx4 v20, s[28:31], s62 offen offset:0 lds\n"
    "v_mfma_f32_16x16x32_bf16 v[64:67], v[208:211], a[180:183], v[64:67]\n"
    "ds_read_b64_tr_b16 v[192:193], v26 offset:32768\n"
    "buffer_load_dwordx4 v21, s[28:31], s62 offen offset:1024 lds\n"
    "v_mfma_f32_16x16x32_bf16 v[68:71], v[208:211], a[184:187], v[68:71]\n"
    "ds_read_b64_tr_b16 v[194:195], v26 offset:33280\n"
    "v_lshl_add_u32 v58, v58, 1, v59\n"
    "v_mfma_f32_16x16x32_bf16 v[72:75], v[208:211], a[188:191], v[72:75]\n"
    "ds_read_b64_tr_b16 v[196:197], v27 offset:32768\n"
    "v_lshlrev_b32 v58, 4, v58\n"
    "v_mfma_f32_16x16x32_bf16 v[76:79], v[212:215], a[180:183], v[76:79]\n"
    "ds_read_b64_tr_b16 v[198:199], v27 offset:33280\n"
    "v_mul_u32_u24 v59, 0x500, v3\n"
    "v_mfma_f32_16x16x32_bf16 v[80:83], v[212:215], a[184:187], v[80:83]\n"
    "s_sub_u32 s63, s63, 0x80800\n"
    "s_cselect_b32 s63, 0x606000, s63\n"
    "v_mfma_f32_16x16x32_bf16 v[84:87], v[212:215], a[188:191], v[84:87]\n"
    "s_waitcnt lgkmcnt(4)\n"
    "buffer_load_dwordx4 a[32:35], v34, s[36:39], s63 offen offset:0 sc1\n"
    "v_add_u32 v58, v58, v59\n"
    "v_mfma_f32_16x16x32_bf16 v[88:91], v[216:219], a[180:183], v[88:91]\n"
    "ds_read_b64_tr_b16 v[200:201], v28 offset:32768\n"
    "v_add_u32 v44, s67, v58\n"
    "v_mfma_f32_16x16x32_bf16 v[92:95], v[216:219], a[184:187], v[92:95]\n"
    "ds_read_b64_tr_b16 v[202:203], v28 offset:33280\n"
    "buffer_load_dwordx4 a[36:39], v34, s[36:39], s63 offen offset:256 sc1\n"
    "v_mfma_f32_16x16x32_bf16 v[96:99], v[216:219], a[188:191], v[96:99]\n"
    "ds_read_b64_tr_b16 v[204:205], v29 offset:32768\n"
    "s_add_u32 s68, s94, 0xa000\n"
    "v_mfma_f32_16x16x32_bf16 v[100:103], v[220:223], a[180:183], v[100:103]\n"
    "ds_read_b64_tr_b16 v[206:207], v29 offset:33280\n"
    "s_mov_b32 s67, 0x5000\n"
    "v_mfma_f32_16x16x32_bf16 v[104:107], v[220:223], a[184:187], v[104:107]\n"
    "v_mov_b32 v60, 0xbfb8aa3b\n"
    "s_mul_i32 s72, s54, 0xffffe400\n"
    "v_mfma_f32_16x16x32_bf16 v[108:111], v[220:223], a[188:191], v[108:111]\n"
    "s_waitcnt vmcnt(37) lgkmcnt(4)\n"
    "buffer_load_dwordx4 a[40:43], v36, s[36:39], s63 offen offset:512 sc1\n"
    "s_add_u32 m0, s56, 0x10000\n"
    "v_mfma_f32_16x16x32_bf16 v[64:67], v[192:195], a[192:195], v[64:67]\n"
    "ds_read_b64_tr_b16 v[208:209], v26 offset:33792\n"
    "v_lshlrev_b32 v224, 16, v152\n"
    "v_mfma_f32_16x16x32_bf16 v[68:71], v[192:195], a[196:199], v[68:71]\n"
    "ds_read_b64_tr_b16 v[210:211], v26 offset:34304\n"
    "v_and_b32 v225, 0xffff0000, v152\n"
    "v_mfma_f32_16x16x32_bf16 v[72:75], v[192:195], a[200:203], v[72:75]\n"
    "ds_read_b64_tr_b16 v[212:213], v27 offset:33792\n"
    "v_lshlrev_b32 v226, 16, v153\n"
    "v_mfma_f32_16x16x32_bf16 v[76:79], v[196:199], a[192:195], v[76:79]\n"
    "ds_read_b64_tr_b16 v[214:215], v27 offset:34304\n"
    "v_and_b32 v227, 0xffff0000, v153\n"
    "s_sub_u32 s61, s61, 0x28000\n"
    "s_cselect_b32 s61, 0x1e0000, s61\n"
    "v_mfma_f32_16x16x32_bf16 v[80:83], v[196:199], a[196:199], v[80:83]\n"
    "buffer_load_dwordx4 v20, s[28:31], s61 offen offset:0 lds\n"
    "v_lshlrev_b32 v228, 16, v154\n"
    "v_mfma_f32_16x16x32_bf16 v[84:87], v[196:199], a[200:203], v[84:87]\n"
    "s_waitcnt lgkmcnt(4)\n"
    "buffer_load_dwordx4 v21, s[28:31], s61 offen offset:1024 lds\n"
    "s_add_u32 m0, s56, 0x12000\n"
    "v_mfma_f32_16x16x32_bf16 v[88:91], v[200:203], a[192:195], v[88:91]\n"
    "ds_read_b64_tr_b16 v[216:217], v28 offset:33792\n"
    "v_and_b32 v229, 0xffff0000, v154\n"
    "v_mfma_f32_16x16x32_bf16 v[92:95], v[200:203], a[196:199], v[92:95]\n"
    "ds_read_b64_tr_b16 v[218:219], v28 offset:34304\n"
    "v_lshlrev_b32 v230, 16, v155\n"
    "v_mfma_f32_16x16x32_bf16 v[96:99], v[200:203], a[200:203], v[96:99]\n"
    "ds_read_b64_tr_b16 v[220:221], v29 offset:33792\n"
    "v_and_b32 v231, 0xffff0000, v155\n"
    "v_mfma_f32_16x16x32_bf16 v[100:103], v[204:207], a[192:195], v[100:103]\n"
    "ds_read_b64_tr_b16 v[222:223], v29 offset:34304\n"
    "v_lshlrev_b32 v232, 16, v156\n"
    "v_mfma_f32_16x16x32_bf16 v[104:107], v[204:207], a[196:199], v[104:107]\n"
    "s_sub_u32 s62, s62, 0x28000\n"
    "s_cselect_b32 s62, 0x1e0000, s62\n"
    "v_mfma_f32_16x16x32_bf16 v[108:111], v[204:207], a[200:203], v[108:111]\n"
    "s_waitcnt vmcnt(35) lgkmcnt(4)\n"
    "s_barrier\n"
    "buffer_load_dwordx4 v20, s[28:31], s62 offen offset:0 lds\n"
    "v_mfma_f32_16x16x32_bf16 v[64:67], v[208:211], a[204:207], v[64:67]\n"
    "ds_read_b64_tr_b16 v[192:193], v26 offset:49152\n"
    "buffer_load_dwordx4 v21, s[28:31], s62 offen offset:1024 lds\n"
    "v_mfma_f32_16x16x32_bf16 v[68:71], v[208:211], a[208:211], v[68:71]\n"
    "ds_read_b64_tr_b16 v[194:195], v26 offset:49664\n"
    "v_and_b32 v233, 0xffff0000, v156\n"
    "v_mfma_f32_16x16x32_bf16 v[72:75], v[208:211], a[212:215], v[72:75]\n"
    "ds_read_b64_tr_b16 v[196:197], v27 offset:49152\n"
    "v_lshlrev_b32 v234, 16, v157\n"
    "v_mfma_f32_16x16x32_bf16 v[76:79], v[212:215], a[204:207], v[76:79]\n"
    "ds_read_b64_tr_b16 v[198:199], v27 offset:49664\n"
    "v_and_b32 v235, 0xffff0000, v157\n"
    "v_mfma_f32_16x16x32_bf16 v[80:83], v[212:215], a[208:211], v[80:83]\n"
    "buffer_load_dwordx4 a[44:47], v35, s[36:39], s63 offen offset:0 sc1\n"
    "v_lshlrev_b32 v236, 16, v158\n"
    "v_mfma_f32_16x16x32_bf16 v[84:87], v[212:215], a[212:215], v[84:87]\n"
    "s_waitcnt lgkmcnt(4)\n"
    "v_and_b32 v237, 0xffff0000, v158\n"
    "v_lshlrev_b32 v238, 16, v159\n"
    "v_mfma_f32_16x16x32_bf16 v[88:91], v[216:219], a[204:207], v[88:91]\n"
    "ds_read_b64_tr_b16 v[200:201], v28 offset:49152\n"
    "v_and_b32 v239, 0xffff0000, v159\n"
    "v_mfma_f32_16x16x32_bf16 v[92:95], v[216:219], a[208:211], v[92:95]\n"
    "ds_read_b64_tr_b16 v[202:203], v28 offset:49664\n"
    "buffer_load_dwordx4 a[48:51], v35, s[36:39], s63 offen offset:256 sc1\n"
    "v_mfma_f32_16x16x32_bf16 v[96:99], v[216:219], a[212:215], v[96:99]\n"
    "ds_read_b64_tr_b16 v[204:205], v29 offset:49152\n"
    "v_lshlrev_b32 v240, 16, v160\n"
    "v_mfma_f32_16x16x32_bf16 v[100:103], v[220:223], a[204:207], v[100:103]\n"
    "ds_read_b64_tr_b16 v[206:207], v29 offset:49664\n"
    "v_and_b32 v241, 0xffff0000, v160\n"
    "v_mfma_f32_16x16x32_bf16 v[104:107], v[220:223], a[208:211], v[104:107]\n"
    "v_lshlrev_b32 v242, 16, v161\n"
    "v_and_b32 v243, 0xffff0000, v161\n"
    "v_mfma_f32_16x16x32_bf16 v[108:111], v[220:223], a[212:215], v[108:111]\n"
    "s_waitcnt vmcnt(36) lgkmcnt(4)\n"
    "buffer_load_dwordx4 a[52:55], v37, s[36:39], s63 offen offset:512 sc1\n"
    "v_lshlrev_b32 v244, 16, v162\n"
    "v_mfma_f32_16x16x32_bf16 v[64:67], v[192:195], a[216:219], v[64:67]\n"
    "ds_read_b64_tr_b16 v[208:209], v26 offset:50176\n"
    "v_and_b32 v245, 0xffff0000, v162\n"
    "v_mfma_f32_16x16x32_bf16 v[68:71], v[192:195], a[220:223], v[68:71]\n"
    "ds_read_b64_tr_b16 v[210:211], v26 offset:50688\n"
    "v_lshlrev_b32 v246, 16, v163\n"
    "v_mfma_f32_16x16x32_bf16 v[72:75], v[192:195], a[224:227], v[72:75]\n"
    "ds_read_b64_tr_b16 v[212:213], v27 offset:50176\n"
    "v_and_b32 v247, 0xffff0000, v163\n"
    "v_mfma_f32_16x16x32_bf16 v[76:79], v[196:199], a[216:219], v[76:79]\n"
    "ds_read_b64_tr_b16 v[214:215], v27 offset:50688\n"
    "v_mfma_f32_16x16x32_bf16 v[80:83], v[196:199], a[220:223], v[80:83]\n"
    "s_sub_u32 s63, s63, 0x80800\n"
    "s_cselect_b32 s63, 0x606000, s63\n"
    "v_mfma_f32_16x16x32_bf16 v[84:87], v[196:199], a[224:227], v[84:87]\n"
    "buffer_load_dwordx4 a[56:59], v34, s[36:39], s63 offen offset:0 sc1\n"
    "s_waitcnt lgkmcnt(4)\n"
    "v_mfma_f32_16x16x32_bf16 v[88:91], v[200:203], a[216:219], v[88:91]\n"
    "ds_read_b64_tr_b16 v[216:217], v28 offset:50176\n"
    "buffer_load_dwordx4 a[60:63], v34, s[36:39], s63 offen offset:256 sc1\n"
    "v_mfma_f32_16x16x32_bf16 v[92:95], v[200:203], a[220:223], v[92:95]\n"
    "ds_read_b64_tr_b16 v[218:219], v28 offset:50688\n"
    "v_mfma_f32_16x16x32_bf16 v[96:99], v[200:203], a[224:227], v[96:99]\n"
    "ds_read_b64_tr_b16 v[220:221], v29 offset:50176\n"
    "v_mfma_f32_16x16x32_bf16 v[100:103], v[204:207], a[216:219], v[100:103]\n"
    "ds_read_b64_tr_b16 v[222:223], v29 offset:50688\n"
    "v_mfma_f32_16x16x32_bf16 v[104:107], v[204:207], a[220:223], v[104:107]\n"
    "buffer_load_dwordx4 a[64:67], v36, s[36:39], s63 offen offset:512 sc1\n"
    "s_add_u32 m0, s56, 0\n"
    "v_mfma_f32_16x16x32_bf16 v[108:111], v[204:207], a[224:227], v[108:111]\n"
    "s_waitcnt vmcnt(33) lgkmcnt(4)\n"
    "s_barrier\n"
    "v_mfma_f32_16x16x32_bf16 v[64:67], v[208:211], a[228:231], v[64:67]\n"
    "ds_read_b64_tr_b16 v[192:193], v26 offset:16384\n"
    "v_mfma_f32_16x16x32_bf16 v[68:71], v[208:211], a[232:235], v[68:71]\n"
    "ds_read_b64_tr_b16 v[194:195], v26 offset:16896\n"
    "v_mfma_f32_16x16x32_bf16 v[72:75], v[208:211], a[236:239], v[72:75]\n"
    "s_sub_u32 s61, s61, 0x28000\n"
    "s_cselect_b32 s61, 0x1e0000, s61\n"
    "ds_read_b64_tr_b16 v[196:197], v27 offset:16384\n"
    "v_mfma_f32_16x16x32_bf16 v[76:79], v[212:215], a[228:231], v[76:79]\n"
    "buffer_load_dwordx4 v20, s[28:31], s61 offen offset:0 lds\n"
    "ds_read_b64_tr_b16 v[198:199], v27 offset:16896\n"
    "v_mfma_f32_16x16x32_bf16 v[80:83], v[212:215], a[232:235], v[80:83]\n"
    "buffer_load_dwordx4 v21, s[28:31], s61 offen offset:1024 lds\n"
    "v_mfma_f32_16x16x32_bf16 v[84:87], v[212:215], a[236:239], v[84:87]\n"
    "s_waitcnt lgkmcnt(4)\n"
    "v_mfma_f32_16x16x32_bf16 v[88:91], v[216:219], a[228:231], v[88:91]\n"
    "ds_read_b64_tr_b16 v[200:201], v28 offset:16384\n"
    "v_mfma_f32_16x16x32_bf16 v[92:95], v[216:219], a[232:235], v[92:95]\n"
    "ds_read_b64_tr_b16 v[202:203], v28 offset:16896\n"
    "v_mfma_f32_16x16x32_bf16 v[96:99], v[216:219], a[236:239], v[96:99]\n"
    "ds_read_b64_tr_b16 v[204:205], v29 offset:16384\n"
    "buffer_load_dwordx4 a[68:71], v35, s[36:39], s63 offen offset:0 sc1\n"
    "v_mfma_f32_16x16x32_bf16 v[100:103], v[220:223], a[228:231], v[100:103]\n"
    "ds_read_b64_tr_b16 v[206:207], v29 offset:16896\n"
    "v_mfma_f32_16x16x32_bf16 v[104:107], v[220:223], a[232:235], v[104:107]\n"
    "v_mfma_f32_16x16x32_bf16 v[108:111], v[220:223], a[236:239], v[108:111]\n"
    "s_waitcnt vmcnt(33) lgkmcnt(4)\n"
    "buffer_load_dwordx4 a[72:75], v35, s[36:39], s63 offen offset:256 sc1\n"
    "v_mfma_f32_16x16x32_bf16 v[64:67], v[192:195], a[240:243], v[64:67]\n"
    "ds_read_b64_tr_b16 v[208:209], v26 offset:17408\n"
    "v_mfma_f32_16x16x32_bf16 v[68:71], v[192:195], a[244:247], v[68:71]\n"
    "ds_read_b64_tr_b16 v[210:211], v26 offset:17920\n"
    "v_mfma_f32_16x16x32_bf16 v[72:75], v[192:195], a[248:251], v[72:75]\n"
    "ds_read_b64_tr_b16 v[212:213], v27 offset:17408\n"
    "buffer_load_dwordx4 a[76:79], v37, s[36:39], s63 offen offset:512 sc1\n"
    "v_mfma_f32_16x16x32_bf16 v[76:79], v[196:199], a[240:243], v[76:79]\n"
    "ds_read_b64_tr_b16 v[214:215], v27 offset:17920\n"
    "s_add_u32 s63, s75, 7\n"
    "v_mfma_f32_16x16x32_bf16 v[80:83], v[196:199], a[244:247], v[80:83]\n"
    "s_cmp_ge_u32 s63, 13\n"
    "s_cselect_b32 s1, 13, 0\n"
    "v_mfma_f32_16x16x32_bf16 v[84:87], v[196:199], a[248:251], v[84:87]\n"
    "s_waitcnt lgkmcnt(4)\n"
    "s_sub_u32 s63, s63, s1\n"
    "s_mul_i32 s63, s63, 0x80800\n"
    "v_mfma_f32_16x16x32_bf16 v[88:91], v[200:203], a[240:243], v[88:91]\n"
    "ds_read_b64_tr_b16 v[216:217], v28 offset:17408\n"
    "s_mul_i32 s1, s54, 0x20200\n"
    "v_mfma_f32_16x16x32_bf16 v[92:95], v[200:203], a[244:247], v[92:95]\n"
    "ds_read_b64_tr_b16 v[218:219], v28 offset:17920\n"
    "s_add_u32 s63, s63, s1\n"
    "v_mfma_f32_16x16x32_bf16 v[96:99], v[200:203], a[248:251], v[96:99]\n"
    "ds_read_b64_tr_b16 v[220:221], v29 offset:17408\n"
    "buffer_load_dwordx4 a[80:83], v34, s[36:39], s63 offen offset:0 sc1\n"
    "v_mfma_f32_16x16x32_bf16 v[100:103], v[204:207], a[240:243], v[100:103]\n"
    "ds_read_b64_tr_b16 v[222:223], v29 offset:17920\n"
    "buffer_load_dwordx4 a[84:87], v34, s[36:39], s63 offen offset:256 sc1\n"
    "v_mfma_f32_16x16x32_bf16 v[104:107], v[204:207], a[244:247], v[104:107]\n"
    "v_mfma_f32_16x16x32_bf16 v[108:111], v[204:207], a[248:251], v[108:111]\n"
    "s_waitcnt vmcnt(30) lgkmcnt(4)\n"
    "s_barrier\n"
    "v_mfma_f32_16x16x32_bf16 v[64:67], v[208:211], a[252:255], v[64:67]\n"
    "ds_read_b64_tr_b16 v[192:193], v30 offset:16384\n"
    "buffer_load_dwordx4 a[88:91], v36, s[36:39], s63 offen offset:512 sc1\n"
    "v_mfma_f32_16x16x32_bf16 v[68:71], v[208:211], a[0:3], v[68:71]\n"
    "ds_read_b64_tr_b16 v[194:195], v30 offset:16896\n"
    "v_mfma_f32_16x16x32_bf16 v[72:75], v[208:211], a[4:7], v[72:75]\n"
    "ds_read_b64_tr_b16 v[196:197], v31 offset:16384\n"
    "v_mfma_f32_16x16x32_bf16 v[76:79], v[212:215], a[252:255], v[76:79]\n"
    "ds_read_b64_tr_b16 v[198:199], v31 offset:16896\n"
    "v_mfma_f32_16x16x32_bf16 v[80:83], v[212:215], a[0:3], v[80:83]\n"
    "v_mfma_f32_16x16x32_bf16 v[84:87], v[212:215], a[4:7], v[84:87]\n"
    "s_waitcnt lgkmcnt(4)\n"
    "v_mfma_f32_16x16x32_bf16 v[88:91], v[216:219], a[252:255], v[88:91]\n"
    "ds_read_b64_tr_b16 v[200:201], v32 offset:16384\n"
    "v_mfma_f32_16x16x32_bf16 v[92:95], v[216:219], a[0:3], v[92:95]\n"
    "ds_read_b64_tr_b16 v[202:203], v32 offset:16896\n"
    "v_mfma_f32_16x16x32_bf16 v[96:99], v[216:219], a[4:7], v[96:99]\n"
    "ds_read_b64_tr_b16 v[204:205], v33 offset:16384\n"
    "v_mfma_f32_16x16x32_bf16 v[100:103], v[220:223], a[252:255], v[100:103]\n"
    "ds_read_b64_tr_b16 v[206:207], v33 offset:16896\n"
    "v_mfma_f32_16x16x32_bf16 v[104:107], v[220:223], a[0:3], v[104:107]\n"
    "v_mfma_f32_16x16x32_bf16 v[108:111], v[220:223], a[4:7], v[108:111]\n"
    "s_waitcnt vmcnt(28) lgkmcnt(4)\n"
    "v_mfma_f32_16x16x32_bf16 v[64:67], v[192:195], a[8:11], v[64:67]\n"
    "ds_read_b64_tr_b16 v[208:209], v30 offset:17408\n"
    "v_mfma_f32_16x16x32_bf16 v[68:71], v[192:195], a[12:15], v[68:71]\n"
    "ds_read_b64_tr_b16 v[210:211], v30 offset:17920\n"
    "v_mfma_f32_16x16x32_bf16 v[72:75], v[192:195], a[16:19], v[72:75]\n"
    "ds_read_b64_tr_b16 v[212:213], v31 offset:17408\n"
    "v_mfma_f32_16x16x32_bf16 v[76:79], v[196:199], a[8:11], v[76:79]\n"
    "ds_read_b64_tr_b16 v[214:215], v31 offset:17920\n"
    "v_mfma_f32_16x16x32_bf16 v[80:83], v[196:199], a[12:15], v[80:83]\n"
    "v_mfma_f32_16x16x32_bf16 v[84:87], v[196:199], a[16:19], v[84:87]\n"
    "s_waitcnt lgkmcnt(4)\n"
    "v_mfma_f32_16x16x32_bf16 v[88:91], v[200:203], a[8:11], v[88:91]\n"
    "ds_read_b64_tr_b16 v[216:217], v32 offset:17408\n"
    "v_mfma_f32_16x16x32_bf16 v[92:95], v[200:203], a[12:15], v[92:95]\n"
    "ds_read_b64_tr_b16 v[218:219], v32 offset:17920\n"
    "v_mfma_f32_16x16x32_bf16 v[96:99], v[200:203], a[16:19], v[96:99]\n"
    "ds_read_b64_tr_b16 v[220:221], v33 offset:17408\n"
    "v_mfma_f32_16x16x32_bf16 v[100:103], v[204:207], a[8:11], v[100:103]\n"
    "ds_read_b64_tr_b16 v[222:223], v33 offset:17920\n"
    "v_mfma_f32_16x16x32_bf16 v[104:107], v[204:207], a[12:15], v[104:107]\n"
    "v_mfma_f32_16x16x32_bf16 v[108:111], v[204:207], a[16:19], v[108:111]\n"
    "s_waitcnt vmcnt(21) lgkmcnt(4)\n"
    "s_barrier\n"
    "v_mfma_f32_16x16x32_bf16 v[64:67], v[208:211], a[20:23], v[64:67]\n"
    "ds_read_b64_tr_b16 v[192:193], v30 offset:32768\n"
    "v_mfma_f32_16x16x32_bf16 v[68:71], v[208:211], a[24:27], v[68:71]\n"
    "ds_read_b64_tr_b16 v[194:195], v30 offset:33280\n"
    "v_mfma_f32_16x16x32_bf16 v[72:75], v[208:211], a[28:31], v[72:75]\n"
    "ds_read_b64_tr_b16 v[196:197], v31 offset:32768\n"
    "v_mfma_f32_16x16x32_bf16 v[76:79], v[212:215], a[20:23], v[76:79]\n"
    "ds_read_b64_tr_b16 v[198:199], v31 offset:33280\n"
    "v_mfma_f32_16x16x32_bf16 v[80:83], v[212:215], a[24:27], v[80:83]\n"
    "v_mfma_f32_16x16x32_bf16 v[84:87], v[212:215], a[28:31], v[84:87]\n"
    "s_waitcnt lgkmcnt(4)\n"
    "v_mfma_f32_16x16x32_bf16 v[88:91], v[216:219], a[20:23], v[88:91]\n"
    "ds_read_b64_tr_b16 v[200:201], v32 offset:32768\n"
    "v_mfma_f32_16x16x32_bf16 v[92:95], v[216:219], a[24:27], v[92:95]\n"
    "ds_read_b64_tr_b16 v[202:203], v32 offset:33280\n"
    "v_mfma_f32_16x16x32_bf16 v[96:99], v[216:219], a[28:31], v[96:99]\n"
    "ds_read_b64_tr_b16 v[204:205], v33 offset:32768\n"
    "v_mfma_f32_16x16x32_bf16 v[100:103], v[220:223], a[20:23], v[100:103]\n"
    "ds_read_b64_tr_b16 v[206:207], v33 offset:33280\n"
    "v_mfma_f32_16x16x32_bf16 v[104:107], v[220:223], a[24:27], v[104:107]\n"
    "v_mfma_f32_16x16x32_bf16 v[108:111], v[220:223], a[28:31], v[108:111]\n"
    "s_waitcnt vmcnt(18) lgkmcnt(4)\n"
    "v_mfma_f32_16x16x32_bf16 v[64:67], v[192:195], a[32:35], v[64:67]\n"
    "ds_read_b64_tr_b16 v[208:209], v32 offset:33792\n"
    "v_mfma_f32_16x16x32_bf16 v[68:71], v[192:195], a[36:39], v[68:71]\n"
    "ds_read_b64_tr_b16 v[210:211], v32 offset:34304\n"
    "v_mfma_f32_16x16x32_bf16 v[72:75], v[192:195], a[40:43], v[72:75]\n"
    "ds_read_b64_tr_b16 v[212:213], v33 offset:33792\n"
    "v_mfma_f32_16x16x32_bf16 v[76:79], v[196:199], a[32:35], v[76:79]\n"
    "ds_read_b64_tr_b16 v[214:215], v33 offset:34304\n"
    "v_mfma_f32_16x16x32_bf16 v[80:83], v[196:199], a[36:39], v[80:83]\n"
    "v_mfma_f32_16x16x32_bf16 v[84:87], v[196:199], a[40:43], v[84:87]\n"
    "s_waitcnt vmcnt(14) lgkmcnt(4)\n"
    "s_barrier\n"
    "v_mfma_f32_16x16x32_bf16 v[88:91], v[200:203], a[32:35], v[88:91]\n"
    "ds_read_b64_tr_b16 v[216:217], v32 offset:0\n"
    "v_mfma_f32_16x16x32_bf16 v[92:95], v[200:203], a[36:39], v[92:95]\n"
    "ds_read_b64_tr_b16 v[218:219], v32 offset:512\n"
    "v_mfma_f32_16x16x32_bf16 v[96:99], v[200:203], a[40:43], v[96:99]\n"
    "ds_read_b64_tr_b16 v[220:221], v33 offset:0\n"
    "v_mfma_f32_16x16x32_bf16 v[100:103], v[204:207], a[32:35], v[100:103]\n"
    "ds_read_b64_tr_b16 v[222:223], v33 offset:512\n"
    "v_mfma_f32_16x16x32_bf16 v[104:107], v[204:207], a[36:39], v[104:107]\n"
    "v_mfma_f32_16x16x32_bf16 v[108:111], v[204:207], a[40:43], v[108:111]\n"
    "s_waitcnt vmcnt(11) lgkmcnt(4)\n"
    "v_mfma_f32_16x16x32_bf16 v[88:91], v[208:211], a[44:47], v[88:91]\n"
    "ds_read_b64_tr_b16 v[192:193], v32 offset:1024\n"
    "v_mfma_f32_16x16x32_bf16 v[92:95], v[208:211], a[48:51], v[92:95]\n"
    "ds_read_b64_tr_b16 v[194:195], v32 offset:1536\n"
    "v_mfma_f32_16x16x32_bf16 v[96:99], v[208:211], a[52:55], v[96:99]\n"
    "ds_read_b64_tr_b16 v[196:197], v33 offset:1024\n"
    "v_mfma_f32_16x16x32_bf16 v[100:103], v[212:215], a[44:47], v[100:103]\n"
    "ds_read_b64_tr_b16 v[198:199], v33 offset:1536\n"
    "v_mfma_f32_16x16x32_bf16 v[104:107], v[212:215], a[48:51], v[104:107]\n"
    "v_mfma_f32_16x16x32_bf16 v[108:111], v[212:215], a[52:55], v[108:111]\n"
    "s_waitcnt vmcnt(6) lgkmcnt(4)\n"
    "s_barrier\n"
    "v_mfma_f32_16x16x32_bf16 v[88:91], v[216:219], a[56:59], v[88:91]\n"
    "v_add_u32 v62, s72, v28\n"
    "v_mfma_f32_16x16x32_bf16 v[92:95], v[216:219], a[60:63], v[92:95]\n"
    "ds_read_b64_tr_b16 v[200:201], v62 offset:0\n"
    "v_mfma_f32_16x16x32_bf16 v[96:99], v[216:219], a[64:67], v[96:99]\n"
    "ds_read_b64_tr_b16 v[202:203], v62 offset:512\n"
    "v_mfma_f32_16x16x32_bf16 v[100:103], v[220:223], a[56:59], v[100:103]\n"
    "v_add_u32 v62, s72, v29\n"
    "v_mfma_f32_16x16x32_bf16 v[104:107], v[220:223], a[60:63], v[104:107]\n"
    "ds_read_b64_tr_b16 v[204:205], v62 offset:0\n"
    "v_mfma_f32_16x16x32_bf16 v[108:111], v[220:223], a[64:67], v[108:111]\n"
    "ds_read_b64_tr_b16 v[206:207], v62 offset:512\n"
    "s_waitcnt vmcnt(3) lgkmcnt(4)\n"
    "v_mfma_f32_16x16x32_bf16 v[88:91], v[192:195], a[68:71], v[88:91]\n"
    "ds_read_b64_tr_b16 v[208:209], v30 offset:33792\n"
    "v_mfma_f32_16x16x32_bf16 v[92:95], v[192:195], a[72:75], v[92:95]\n"
    "ds_read_b64_tr_b16 v[210:211], v30 offset:34304\n"
    "v_mfma_f32_16x16x32_bf16 v[96:99], v[192:195], a[76:79], v[96:99]\n"
    "ds_read_b64_tr_b16 v[212:213], v31 offset:33792\n"
    "v_mfma_f32_16x16x32_bf16 v[100:103], v[196:199], a[68:71], v[100:103]\n"
    "ds_read_b64_tr_b16 v[214:215], v31 offset:34304\n"
    "v_mfma_f32_16x16x32_bf16 v[104:107], v[196:199], a[72:75], v[104:107]\n"
    "v_mfma_f32_16x16x32_bf16 v[108:111], v[196:199], a[76:79], v[108:111]\n"
    "s_waitcnt vmcnt(0) lgkmcnt(4)\n"
    "v_mfma_f32_16x16x32_bf16 v[88:91], v[200:203], a[80:83], v[88:91]\n"
    "ds_read_b64_tr_b16 v[216:217], v30 offset:0\n"
    "v_mfma_f32_16x16x32_bf16 v[92:95], v[200:203], a[84:87], v[92:95]\n"
    "ds_read_b64_tr_b16 v[218:219], v30 offset:512\n"
    "v_mfma_f32_16x16x32_bf16 v[96:99], v[200:203], a[88:91], v[96:99]\n"
    "ds_read_b64_tr_b16 v[220:221], v31 offset:0\n"
    "v_mfma_f32_16x16x32_bf16 v[100:103], v[204:207], a[80:83], v[100:103]\n"
    "ds_read_b64_tr_b16 v[222:223], v31 offset:512\n"
    "v_mfma_f32_16x16x32_bf16 v[104:107], v[204:207], a[84:87], v[104:107]\n"
    "v_mfma_f32_16x16x32_bf16 v[108:111], v[204:207], a[88:91], v[108:111]\n"
    "s_waitcnt lgkmcnt(4)\n"
    "v_mfma_f32_16x16x32_bf16 v[64:67], v[208:211], a[44:47], v[64:67]\n"
    "ds_read_b64_tr_b16 v[192:193], v30 offset:1024\n"
    "v_mfma_f32_16x16x32_bf16 v[68:71], v[208:211], a[48:51], v[68:71]\n"
    "ds_read_b64_tr_b16 v[194:195], v30 offset:1536\n"
    "v_mfma_f32_16x16x32_bf16 v[72:75], v[208:211], a[52:55], v[72:75]\n"
    "ds_read_b64_tr_b16 v[196:197], v31 offset:1024\n"
    "v_mfma_f32_16x16x32_bf16 v[76:79], v[212:215], a[44:47], v[76:79]\n"
    "ds_read_b64_tr_b16 v[198:199], v31 offset:1536\n"
    "v_mfma_f32_16x16x32_bf16 v[80:83], v[212:215], a[48:51], v[80:83]\n"
    "ds_write_b128 v56, v[88:91] offset:0\n"
    "v_mfma_f32_16x16x32_bf16 v[84:87], v[212:215], a[52:55], v[84:87]\n"
    "s_waitcnt lgkmcnt(5)\n"
    "v_mfma_f32_16x16x32_bf16 v[64:67], v[216:219], a[56:59], v[64:67]\n"
    "v_add_u32 v62, s72, v26\n"
    "v_mfma_f32_16x16x32_bf16 v[68:71], v[216:219], a[60:63], v[68:71]\n"
    "ds_read_b64_tr_b16 v[200:201], v62 offset:0\n"
    "v_mfma_f32_16x16x32_bf16 v[72:75], v[216:219], a[64:67], v[72:75]\n"
    "ds_read_b64_tr_b16 v[202:203], v62 offset:512\n"
    "v_mfma_f32_16x16x32_bf16 v[76:79], v[220:223], a[56:59], v[76:79]\n"
    "v_add_u32 v62, s72, v27\n"
    "v_mfma_f32_16x16x32_bf16 v[80:83], v[220:223], a[60:63], v[80:83]\n"
    "ds_read_b64_tr_b16 v[204:205], v62 offset:0\n"
    "v_mfma_f32_16x16x32_bf16 v[84:87], v[220:223], a[64:67], v[84:87]\n"
    "ds_read_b64_tr_b16 v[206:207], v62 offset:512\n"
    "ds_write_b128 v56, v[92:95] offset:1024\n"
    "ds_write_b128 v56, v[96:99] offset:2048\n"
    "s_waitcnt lgkmcnt(7)\n"
    "v_mfma_f32_16x16x32_bf16 v[64:67], v[192:195], a[68:71], v[64:67]\n"
    "v_mfma_f32_16x16x32_bf16 v[68:71], v[192:195], a[72:75], v[68:71]\n"
    "v_mfma_f32_16x16x32_bf16 v[72:75], v[192:195], a[76:79], v[72:75]\n"
    "ds_write_b128 v56, v[100:103] offset:3072\n"
    "v_mfma_f32_16x16x32_bf16 v[76:79], v[196:199], a[68:71], v[76:79]\n"
    "v_mfma_f32_16x16x32_bf16 v[80:83], v[196:199], a[72:75], v[80:83]\n"
    "v_mfma_f32_16x16x32_bf16 v[84:87], v[196:199], a[76:79], v[84:87]\n"
    "ds_write_b128 v56, v[104:107] offset:4096\n"
    "s_waitcnt lgkmcnt(4)\n"
    "v_mfma_f32_16x16x32_bf16 v[64:67], v[200:203], a[80:83], v[64:67]\n"
    "v_mfma_f32_16x16x32_bf16 v[68:71], v[200:203], a[84:87], v[68:71]\n"
    "v_mfma_f32_16x16x32_bf16 v[72:75], v[200:203], a[88:91], v[72:75]\n"
    "ds_write_b128 v56, v[108:111] offset:5120\n"
    "v_mfma_f32_16x16x32_bf16 v[76:79], v[204:207], a[80:83], v[76:79]\n"
    "v_mfma_f32_16x16x32_bf16 v[80:83], v[204:207], a[84:87], v[80:83]\n"
    "v_mfma_f32_16x16x32_bf16 v[84:87], v[204:207], a[88:91], v[84:87]\n"
    "s_waitcnt lgkmcnt(0)\n"
    "s_barrier\n"
    "ds_read_b128 v[192:195], v57 offset:0\n"
    "ds_read_b128 v[204:207], v57 offset:3072\n"
    "ds_read_b128 v[196:199], v57 offset:1024\n"
    "ds_read_b128 v[208:211], v57 offset:4096\n"
    "ds_read_b128 v[200:203], v57 offset:2048\n"
    "ds_read_b128 v[212:215], v57 offset:5120\n"
    "s_waitcnt lgkmcnt(5)\n"
    "v_pk_add_f32 v[64:65], v[64:65], v[192:193] op_sel:[0,0] op_sel_hi:[1,1]\n"
    "v_pk_add_f32 v[66:67], v[66:67], v[194:195] op_sel:[0,0] op_sel_hi:[1,1]\n"
    "s_waitcnt lgkmcnt(4)\n"
    "v_pk_add_f32 v[76:77], v[76:77], v[204:205] op_sel:[0,0] op_sel_hi:[1,1]\n"
    "v_pk_add_f32 v[78:79], v[78:79], v[206:207] op_sel:[0,0] op_sel_hi:[1,1]\n"
    "s_waitcnt lgkmcnt(3)\n"
    "v_pk_add_f32 v[68:69], v[68:69], v[196:197] op_sel:[0,0] op_sel_hi:[1,1]\n"
    "v_pk_add_f32 v[70:71], v[70:71], v[198:199] op_sel:[0,0] op_sel_hi:[1,1]\n"
    "s_waitcnt lgkmcnt(2)\n"
    "v_pk_add_f32 v[80:81], v[80:81], v[208:209] op_sel:[0,0] op_sel_hi:[1,1]\n"
    "v_pk_add_f32 v[82:83], v[82:83], v[210:211] op_sel:[0,0] op_sel_hi:[1,1]\n"
    "s_waitcnt lgkmcnt(1)\n"
    "v_pk_add_f32 v[72:73], v[72:73], v[200:201] op_sel:[0,0] op_sel_hi:[1,1]\n"
    "v_pk_add_f32 v[74:75], v[74:75], v[202:203] op_sel:[0,0] op_sel_hi:[1,1]\n"
    "s_waitcnt lgkmcnt(0)\n"
    "v_pk_add_f32 v[84:85], v[84:85], v[212:213] op_sel:[0,0] op_sel_hi:[1,1]\n"
    "v_pk_add_f32 v[86:87], v[86:87], v[214:215] op_sel:[0,0] op_sel_hi:[1,1]\n"
    "v_pk_mul_f32 v[88:89], v[64:65], v[60:61] op_sel:[0,0] op_sel_hi:[1,0]\n"
    "v_pk_mul_f32 v[90:91], v[66:67], v[60:61] op_sel:[0,0] op_sel_hi:[1,0]\n"
    "v_pk_mul_f32 v[92:93], v[76:77], v[60:61] op_sel:[0,0] op_sel_hi:[1,0]\n"
    "v_pk_mul_f32 v[94:95], v[78:79], v[60:61] op_sel:[0,0] op_sel_hi:[1,0]\n"
    "v_pk_mul_f32 v[96:97], v[68:69], v[60:61] op_sel:[0,0] op_sel_hi:[1,0]\n"
    "v_pk_mul_f32 v[98:99], v[70:71], v[60:61] op_sel:[0,0] op_sel_hi:[1,0]\n"
    "v_pk_mul_f32 v[100:101], v[80:81], v[60:61] op_sel:[0,0] op_sel_hi:[1,0]\n"
    "v_pk_mul_f32 v[102:103], v[82:83], v[60:61] op_sel:[0,0] op_sel_hi:[1,0]\n"
    "v_pk_mul_f32 v[104:105], v[72:73], v[60:61] op_sel:[0,0] op_sel_hi:[1,0]\n"
    "v_pk_mul_f32 v[106:107], v[74:75], v[60:61] op_sel:[0,0] op_sel_hi:[1,0]\n"
    "v_pk_mul_f32 v[108:109], v[84:85], v[60:61] op_sel:[0,0] op_sel_hi:[1,0]\n"
    "v_pk_mul_f32 v[110:111], v[86:87], v[60:61] op_sel:[0,0] op_sel_hi:[1,0]\n"
    "v_exp_f32 v88, v88\n"
    "v_exp_f32 v89, v89\n"
    "v_exp_f32 v90, v90\n"
    "v_exp_f32 v91, v91\n"
    "v_exp_f32 v92, v92\n"
    "v_exp_f32 v93, v93\n"
    "v_exp_f32 v94, v94\n"
    "v_exp_f32 v95, v95\n"
    "v_exp_f32 v96, v96\n"
    "v_exp_f32 v97, v97\n"
    "v_exp_f32 v98, v98\n"
    "v_exp_f32 v99, v99\n"
    "v_exp_f32 v100, v100\n"
    "v_exp_f32 v101, v101\n"
    "v_exp_f32 v102, v102\n"
    "v_exp_f32 v103, v103\n"
    "v_exp_f32 v104, v104\n"
    "v_exp_f32 v105, v105\n"
    "v_exp_f32 v106, v106\n"
    "v_exp_f32 v107, v107\n"
    "v_exp_f32 v108, v108\n"
    "v_exp_f32 v109, v109\n"
    "v_exp_f32 v110, v110\n"
    "v_exp_f32 v111, v111\n"
    "v_pk_add_f32 v[88:89], v[88:89], 1.0 op_sel:[0,0] op_sel_hi:[1,0]\n"
    "v_pk_add_f32 v[90:91], v[90:91], 1.0 op_sel:[0,0] op_sel_hi:[1,0]\n"
    "v_pk_add_f32 v[92:93], v[92:93], 1.0 op_sel:[0,0] op_sel_hi:[1,0]\n"
    "v_pk_add_f32 v[94:95], v[94:95], 1.0 op_sel:[0,0] op_sel_hi:[1,0]\n"
    "v_pk_add_f32 v[96:97], v[96:97], 1.0 op_sel:[0,0] op_sel_hi:[1,0]\n"
    "v_pk_add_f32 v[98:99], v[98:99], 1.0 op_sel:[0,0] op_sel_hi:[1,0]\n"
    "v_pk_add_f32 v[100:101], v[100:101], 1.0 op_sel:[0,0] op_sel_hi:[1,0]\n"
    "v_pk_add_f32 v[102:103], v[102:103], 1.0 op_sel:[0,0] op_sel_hi:[1,0]\n"
    "v_pk_add_f32 v[104:105], v[104:105], 1.0 op_sel:[0,0] op_sel_hi:[1,0]\n"
    "v_pk_add_f32 v[106:107], v[106:107], 1.0 op_sel:[0,0] op_sel_hi:[1,0]\n"
    "v_pk_add_f32 v[108:109], v[108:109], 1.0 op_sel:[0,0] op_sel_hi:[1,0]\n"
    "v_pk_add_f32 v[110:111], v[110:111], 1.0 op_sel:[0,0] op_sel_hi:[1,0]\n"
    "v_pk_mul_f32 v[64:65], v[64:65], v[164:165] op_sel:[0,0] op_sel_hi:[1,1]\n"
    "v_pk_mul_f32 v[66:67], v[66:67], v[166:167] op_sel:[0,0] op_sel_hi:[1,1]\n"
    "v_pk_mul_f32 v[76:77], v[76:77], v[176:177] op_sel:[0,0] op_sel_hi:[1,1]\n"
    "v_pk_mul_f32 v[78:79], v[78:79], v[178:179] op_sel:[0,0] op_sel_hi:[1,1]\n"
    "v_pk_mul_f32 v[68:69], v[68:69], v[168:169] op_sel:[0,0] op_sel_hi:[1,1]\n"
    "v_pk_mul_f32 v[70:71], v[70:71], v[170:171] op_sel:[0,0] op_sel_hi:[1,1]\n"
    "v_pk_mul_f32 v[80:81], v[80:81], v[180:181] op_sel:[0,0] op_sel_hi:[1,1]\n"
    "v_pk_mul_f32 v[82:83], v[82:83], v[182:183] op_sel:[0,0] op_sel_hi:[1,1]\n"
    "v_pk_mul_f32 v[72:73], v[72:73], v[172:173] op_sel:[0,0] op_sel_hi:[1,1]\n"
    "v_pk_mul_f32 v[74:75], v[74:75], v[174:175] op_sel:[0,0] op_sel_hi:[1,1]\n"
    "v_pk_mul_f32 v[84:85], v[84:85], v[184:185] op_sel:[0,0] op_sel_hi:[1,1]\n"
    "v_pk_mul_f32 v[86:87], v[86:87], v[186:187] op_sel:[0,0] op_sel_hi:[1,1]\n"
    "v_rcp_f32 v88, v88\n"
    "v_rcp_f32 v89, v89\n"
    "v_rcp_f32 v90, v90\n"
    "v_rcp_f32 v91, v91\n"
    "v_rcp_f32 v92, v92\n"
    "v_rcp_f32 v93, v93\n"
    "v_rcp_f32 v94, v94\n"
    "v_rcp_f32 v95, v95\n"
    "v_pk_fma_f32 v[64:65], v[64:65], v[88:89], v[224:225] op_sel:[0,0,0] op_sel_hi:[1,1,1]\n"
    "v_pk_fma_f32 v[66:67], v[66:67], v[90:91], v[226:227] op_sel:[0,0,0] op_sel_hi:[1,1,1]\n"
    "v_pk_fma_f32 v[76:77], v[76:77], v[92:93], v[228:229] op_sel:[0,0,0] op_sel_hi:[1,1,1]\n"
    "v_pk_fma_f32 v[78:79], v[78:79], v[94:95], v[230:231] op_sel:[0,0,0] op_sel_hi:[1,1,1]\n"
    "v_cvt_pk_bf16_f32 v192, v64, v65\n"
    "v_cvt_pk_bf16_f32 v193, v66, v67\n"
    "v_cvt_pk_bf16_f32 v194, v76, v77\n"
    "v_cvt_pk_bf16_f32 v195, v78, v79\n"
    "s_nop 0\n"
    "v_permlane16_swap_b32 v192, v194\n"
    "v_permlane16_swap_b32 v193, v195\n"
    "buffer_store_dwordx4 v[192:195], v44, s[32:35], 0 offen sc0 sc1\n"
    "v_rcp_f32 v96, v96\n"
    "v_rcp_f32 v97, v97\n"
    "v_rcp_f32 v98, v98\n"
    "v_rcp_f32 v99, v99\n"
    "v_rcp_f32 v100, v100\n"
    "v_rcp_f32 v101, v101\n"
    "v_rcp_f32 v102, v102\n"
    "v_rcp_f32 v103, v103\n"
    "v_pk_fma_f32 v[68:69], v[68:69], v[96:97], v[232:233] op_sel:[0,0,0] op_sel_hi:[1,1,1]\n"
    "v_pk_fma_f32 v[70:71], v[70:71], v[98:99], v[234:235] op_sel:[0,0,0] op_sel_hi:[1,1,1]\n"
    "v_pk_fma_f32 v[80:81], v[80:81], v[100:101], v[236:237] op_sel:[0,0,0] op_sel_hi:[1,1,1]\n"
    "v_pk_fma_f32 v[82:83], v[82:83], v[102:103], v[238:239] op_sel:[0,0,0] op_sel_hi:[1,1,1]\n"
    "v_cvt_pk_bf16_f32 v196, v68, v69\n"
    "v_cvt_pk_bf16_f32 v197, v70, v71\n"
    "v_cvt_pk_bf16_f32 v198, v80, v81\n"
    "v_cvt_pk_bf16_f32 v199, v82, v83\n"
    "s_nop 0\n"
    "v_permlane16_swap_b32 v196, v198\n"
    "v_permlane16_swap_b32 v197, v199\n"
    "buffer_store_dwordx4 v[196:199], v44, s[32:35], s67 offen sc0 sc1\n"
    "v_rcp_f32 v104, v104\n"
    "v_rcp_f32 v105, v105\n"
    "v_rcp_f32 v106, v106\n"
    "v_rcp_f32 v107, v107\n"
    "v_rcp_f32 v108, v108\n"
    "v_rcp_f32 v109, v109\n"
    "v_rcp_f32 v110, v110\n"
    "v_rcp_f32 v111, v111\n"
    "v_pk_fma_f32 v[72:73], v[72:73], v[104:105], v[240:241] op_sel:[0,0,0] op_sel_hi:[1,1,1]\n"
    "v_pk_fma_f32 v[74:75], v[74:75], v[106:107], v[242:243] op_sel:[0,0,0] op_sel_hi:[1,1,1]\n"
    "v_pk_fma_f32 v[84:85], v[84:85], v[108:109], v[244:245] op_sel:[0,0,0] op_sel_hi:[1,1,1]\n"
    "v_pk_fma_f32 v[86:87], v[86:87], v[110:111], v[246:247] op_sel:[0,0,0] op_sel_hi:[1,1,1]\n"
    "v_cvt_pk_bf16_f32 v200, v72, v73\n"
    "v_cvt_pk_bf16_f32 v201, v74, v75\n"
    "v_cvt_pk_bf16_f32 v202, v84, v85\n"
    "v_cvt_pk_bf16_f32 v203, v86, v87\n"
    "s_nop 0\n"
    "v_permlane16_swap_b32 v200, v202\n"
    "v_permlane16_swap_b32 v201, v203\n"
    "buffer_store_dwordx4 v[200:203], v44, s[32:35], s68 offen sc0 sc1\n"
    "s_endpgm\n"
    ".Lspin_R:\n"
    "s_mov_b32 s96, 65536\n"
    ".Lspin2_R:\n"
    "buffer_load_dwordx2 v[6:7], v10, s[36:39], 0 offen sc1\n"
    "s_waitcnt vmcnt(0)\n"
    "v_cmp_eq_u64 vcc, v[6:7], v[4:5]\n"
    "s_or_b32 vcc_lo, vcc_lo, s76\n"
    "s_cmp_eq_u32 vcc_lo, -1\n"
    "s_cbranch_scc1 .Ldone_R\n"
    "s_sub_u32 s96, s96, 1\n"
    "s_cmp_eq_u32 s96, 0\n"
    "s_cbranch_scc0 .Lspin2_R\n"
    "s_branch .Ldone_R\n"
    ".Lspin_H:\n"
    "s_mov_b32 s96, 65536\n"
    ".Lspin2_H:\n"
    "buffer_load_dwordx2 v[6:7], v8, s[36:39], s92 offen sc1\n"
    "s_waitcnt vmcnt(0)\n"
    "v_cmp_eq_u64 vcc, v[6:7], v[4:5]\n"
    "s_or_b32 vcc_lo, vcc_lo, s77\n"
    "s_cmp_eq_u32 vcc_lo, -1\n"
    "s_cbranch_scc1 .Ldone_H\n"
    "s_sub_u32 s96, s96, 1\n"
    "s_cmp_eq_u32 s96, 0\n"
    "s_cbranch_scc0 .Lspin2_H\n"
    "s_branch .Ldone_H\n"
    ".Lspin_KV:\n"
    "s_mov_b32 s96, 65536\n"
    ".Lspin2_KV:\n"
    "buffer_load_dwordx2 v[6:7], v8, s[36:39], s92 offen sc1\n"
    "s_waitcnt vmcnt(0)\n"
    "v_cmp_eq_u64 vcc, v[6:7], v[4:5]\n"
    "s_or_b32 vcc_lo, vcc_lo, s78\n"
    "s_cmp_eq_u32 vcc_lo, -1\n"
    "s_cbranch_scc1 .Ldone_KV\n"
    "s_sub_u32 s96, s96, 1\n"
    "s_cmp_eq_u32 s96, 0\n"
    "s_cbranch_scc0 .Lspin2_KV\n"
    "s_branch .Ldone_KV\n"
    ".Lgen0:\n"
    "v_lshlrev_b32 v58, 7, v1\n"
    "s_mov_b32 s96, 65536\n"
    "s_mov_b32 s64, 4096\n"
    "s_mov_b32 s65, 12288\n"
    "s_mov_b32 s66, 20480\n"
    "s_mov_b32 s67, 28672\n"
    ".Lgen:\n"
    "buffer_load_dwordx2 v[64:65], v58, s[36:39], s64 offen sc1\n"
    "buffer_load_dwordx2 v[66:67], v58, s[36:39], s65 offen sc1\n"
    "buffer_load_dwordx2 v[68:69], v58, s[36:39], s66 offen sc1\n"
    "buffer_load_dwordx2 v[70:71], v58, s[36:39], s67 offen sc1\n"
    "s_waitcnt vmcnt(0)\n"
    "v_cmp_eq_u64 vcc, v[64:65], v[4:5]\n"
    "v_cmp_eq_u64 s[90:91], v[66:67], v[4:5]\n"
    "s_and_b64 vcc, vcc, s[90:91]\n"
    "v_cmp_eq_u64 s[90:91], v[68:69], v[4:5]\n"
    "s_and_b64 vcc, vcc, s[90:91]\n"
    "v_cmp_eq_u64 s[90:91], v[70:71], v[4:5]\n"
    "s_and_b64 vcc, vcc, s[90:91]\n"
    "s_cmp_eq_u64 vcc, -1\n"
    "s_cbranch_scc1 .Lgen2\n"
    "s_sub_u32 s96, s96, 1\n"
    "s_cmp_eq_u32 s96, 0\n"
    "s_cbranch_scc0 .Lgen\n"
    ".Lgen2:\n"
    "v_mov_b32 v58, 0\n"
    "s_nop 1\n"
    "buffer_store_dwordx2 v[4:5], v58, s[36:39], 0 offen sc1\n"
    "s_nop 1\n"
    "s_branch .Lgen9\n"
    ".Lspin_X1:\n"
    "s_mov_b32 s96, 65536\n"
    ".Lspin2_X1:\n"
    "buffer_load_dwordx2 v[6:7], v10, s[36:39], 0 offen sc1\n"
    "s_waitcnt vmcnt(0)\n"
    "v_cmp_eq_u64 vcc, v[6:7], v[4:5]\n"
    "s_or_b32 vcc_lo, vcc_lo, s79\n"
    "s_cmp_eq_u32 vcc_lo, -1\n"
    "s_cbranch_scc1 .Ldone_X1\n"
    "s_sub_u32 s96, s96, 1\n"
    "s_cmp_eq_u32 s96, 0\n"
    "s_cbranch_scc0 .Lspin2_X1\n"
    "s_branch .Ldone_X1\n"
    ".Lspin_O:\n"
    "s_mov_b32 s96, 65536\n"
    ".Lspin2_O:\n"
    "buffer_load_dwordx2 v[6:7], v8, s[36:39], s92 offen sc1\n"
    "s_waitcnt vmcnt(0)\n"
    "v_cmp_eq_u64 vcc, v[6:7], v[4:5]\n"
    "s_or_b32 vcc_lo, vcc_lo, s80\n"
    "s_cmp_eq_u32 vcc_lo, -1\n"
    "s_cbranch_scc1 .Ldone_O\n"
    "s_sub_u32 s96, s96, 1\n"
    "s_cmp_eq_u32 s96, 0\n"
    "s_cbranch_scc0 .Lspin2_O\n"
    "s_branch .Ldone_O\n"
    ".Lspin_GA:\n"
    "s_mov_b32 s96, 65536\n"
    ".Lspin2_GA:\n"
    "buffer_load_dwordx2 v[6:7], v8, s[36:39], s92 offen sc1\n"
    "s_waitcnt vmcnt(0)\n"
    "v_cmp_eq_u64 vcc, v[6:7], v[4:5]\n"
    "s_or_b32 vcc_lo, vcc_lo, s81\n"
    "s_cmp_eq_u32 vcc_lo, -1\n"
    "s_cbranch_scc1 .Ldone_GA\n"
    "s_sub_u32 s96, s96, 1\n"
    "s_cmp_eq_u32 s96, 0\n"
    "s_cbranch_scc0 .Lspin2_GA\n"
    "s_branch .Ldone_GA\n"
    ".Lspin_GB:\n"
    "s_mov_b32 s96, 65536\n"
    ".Lspin2_GB:\n"
    "buffer_load_dwordx2 v[6:7], v8, s[36:39], s92 offen sc1\n"
    "s_waitcnt vmcnt(0)\n"
    "v_cmp_eq_u64 vcc, v[6:7], v[4:5]\n"
    "s_or_b32 vcc_lo, vcc_lo, s82\n"
    "s_cmp_eq_u32 vcc_lo, -1\n"
    "s_cbranch_scc1 .Ldone_GB\n"
    "s_sub_u32 s96, s96, 1\n"
    "s_cmp_eq_u32 s96, 0\n"
    "s_cbranch_scc0 .Lspin2_GB\n"
    "s_branch .Ldone_GB\n"
    ".Lcodeend:\n"
    :
    : "s"(__builtin_amdgcn_kernarg_segment_ptr()), "s"(__builtin_amdgcn_workgroup_id_x()), "v"(__builtin_amdgcn_workitem_id_x()), "s"((unsigned)(size_t)lds)
    : "s4", "s5", "s6", "s7", "s8", "s9", "s10", "s11", "s12", "s13", "s14", "s15", "s16", "s17", "s18", "s19", "s20", "s21", "s22", "s23", "s24", "s25", "s26", "s27", "s28", "s29", "s30", "s31", "s32", "s33", "s34", "s35", "s36", "s37", "s38", "s39", "s40", "s41", "s42", "s43", "s44", "s45", "s46", "s47", "s48", "s49", "s50", "s51", "s52", "s53", "s54", "s55", "s56", "s57", "s58", "s59", "s60", "s61", "s62", "s63", "s64", "s65", "s66", "s67", "s68", "s69", "s70", "s71", "s72", "s73", "s74", "s75", "s76", "s77", "s78", "s79", "s80", "s81", "s82", "s83", "s84", "s85", "s86", "s87", "s88", "s89", "s90", "s91", "s92", "s93", "s94", "s95", "s96", "s97", "s98", "s99", "s100", "s101", "vcc", "scc", "v1", "v2", "v3", "v4", "v5", "v6", "v7", "v8", "v9", "v10", "v11", "v12", "v13", "v14", "v15", "v16", "v17", "v18", "v19", "v20", "v21", "v22", "v23", "v24", "v25", "v26", "v27", "v28", "v29", "v30", "v31", "v32", "v33", "v34", "v35", "v36", "v37", "v38", "v39", "v40", "v41", "v42", "v43", "v44", "v45", "v46", "v47", "v48", "v49", "v50", "v51", "v52", "v53", "v54", "v55", "v56", "v57", "v58", "v59", "v60", "v61", "v62", "v63", "v64", "v65", "v66", "v67", "v68", "v69", "v70", "v71", "v72", "v73", "v74", "v75", "v76", "v77", "v78", "v79", "v80", "v81", "v82", "v83", "v84", "v85", "v86", "v87", "v88", "v89", "v90", "v91", "v92", "v93", "v94", "v95", "v96", "v97", "v98", "v99", "v100", "v101", "v102", "v103", "v104", "v105", "v106", "v107", "v108", "v109", "v110", "v111", "v112", "v113", "v114", "v115", "v116", "v117", "v118", "v119", "v120", "v121", "v122", "v123", "v124", "v125", "v126", "v127", "v128", "v129", "v130", "v131", "v132", "v133", "v134", "v135", "v136", "v137", "v138", "v139", "v140", "v141", "v142", "v143", "v144", "v145", "v146", "v147", "v148", "v149", "v150", "v151", "v152", "v153", "v154", "v155", "v156", "v157", "v158", "v159", "v160", "v161", "v162", "v163", "v164", "v165", "v166", "v167", "v168", "v169", "v170", "v171", "v172", "v173", "v174", "v175", "v176", "v177", "v178", "v179", "v180", "v181", "v182", "v183", "v184", "v185", "v186", "v187", "v188", "v189", "v190", "v191", "v192", "v193", "v194", "v195", "v196", "v197", "v198", "v199", "v200", "v201", "v202", "v203", "v204", "v205", "v206", "v207", "v208", "v209", "v210", "v211", "v212", "v213", "v214", "v215", "v216", "v217", "v218", "v219", "v220", "v221", "v222", "v223", "v224", "v225", "v226", "v227", "v228", "v229", "v230", "v231", "v232", "v233", "v234", "v235", "v236", "v237", "v238", "v239", "v240", "v241", "v242", "v243", "v244", "v245", "v246", "v247", "v248", "v249", "v250", "v251", "v252", "v253", "v254", "v255", "a0", "a1", "a2", "a3", "a4", "a5", "a6", "a7", "a8", "a9", "a10", "a11", "a12", "a13", "a14", "a15", "a16", "a17", "a18", "a19", "a20", "a21", "a22", "a23", "a24", "a25", "a26", "a27", "a28", "a29", "a30", "a31", "a32", "a33", "a34", "a35", "a36", "a37", "a38", "a39", "a40", "a41", "a42", "a43", "a44", "a45", "a46", "a47", "a48", "a49", "a50", "a51", "a52", "a53", "a54", "a55", "a56", "a57", "a58", "a59", "a60", "a61", "a62", "a63", "a64", "a65", "a66", "a67", "a68", "a69", "a70", "a71", "a72", "a73", "a74", "a75", "a76", "a77", "a78", "a79", "a80", "a81", "a82", "a83", "a84", "a85", "a86", "a87", "a88", "a89", "a90", "a91", "a92", "a93", "a94", "a95", "a96", "a97", "a98", "a99", "a100", "a101", "a102", "a103", "a104", "a105", "a106", "a107", "a108", "a109", "a110", "a111", "a112", "a113", "a114", "a115", "a116", "a117", "a118", "a119", "a120", "a121", "a122", "a123", "a124", "a125", "a126", "a127", "a128", "a129", "a130", "a131", "a132", "a133", "a134", "a135", "a136", "a137", "a138", "a139", "a140", "a141", "a142", "a143", "a144", "a145", "a146", "a147", "a148", "a149", "a150", "a151", "a152", "a153", "a154", "a155", "a156", "a157", "a158", "a159", "a160", "a161", "a162", "a163", "a164", "a165", "a166", "a167", "a168", "a169", "a170", "a171", "a172", "a173", "a174", "a175", "a176", "a177", "a178", "a179", "a180", "a181", "a182", "a183", "a184", "a185", "a186", "a187", "a188", "a189", "a190", "a191", "a192", "a193", "a194", "a195", "a196", "a197", "a198", "a199", "a200", "a201", "a202", "a203", "a204", "a205", "a206", "a207", "a208", "a209", "a210", "a211", "a212", "a213", "a214", "a215", "a216", "a217", "a218", "a219", "a220", "a221", "a222", "a223", "a224", "a225", "a226", "a227", "a228", "a229", "a230", "a231", "a232", "a233", "a234", "a235", "a236", "a237", "a238", "a239", "a240", "a241", "a242", "a243", "a244", "a245", "a246", "a247", "a248", "a249", "a250", "a251", "a252", "a253", "a254", "a255", "memory");
#endif
}

extern "C" void kernel_launch(void* const* d_in, const int* in_sizes, int n_in, void* d_out, int out_size, void* d_ws, size_t ws_size,
                              hipStream_t stream) {
  const unsigned C = 0x00020000u;
  fused_block_kernel<<<256, 256, 0, stream>>>(d_in[0], 2048u * 640u * 2u, C, d_in[1], 640u * 1920u * 2u, C, d_in[2], 640u * 640u * 2u, C,
                                              d_in[4], 640u * 640u * 2u, C, d_in[5], 640u * 640u * 2u, C, d_in[6], 640u * 1728u * 2u, C,
                                              d_in[7], 1728u * 640u * 2u, C, d_out, 2048u * 640u * 2u, C, d_ws, 19179520u, C);
}
```
